```python
import torch

def _softmax(x, axis=-1):
    return torch.softmax(x, dim=axis)

def _gelu(x):
    return torch.nn.functional.gelu(x, approximate='tanh')

def _silu(x):
    return torch.nn.functional.silu(x)

def _sigmoid(x):
    return torch.sigmoid(x)

def _layernorm(x, axis=-1, eps=1e-05):
    m = x.mean(dim=axis, keepdim=True)
    v = x.var(dim=axis, keepdim=True, unbiased=False)
    return (x - m) / torch.sqrt(v + eps)

def _scatter_add(tgt, idx, src, axis=0):
    return tgt.clone().index_add_(axis, idx, src)

def _segment_sum(src, seg, n):
    out = torch.zeros((n,) + src.shape[1:], dtype=src.dtype)
    return out.index_add_(0, seg, src)

def _pad(x, pw):
    flat = tuple((v for pair in reversed(pw) for v in pair))
    return torch.nn.functional.pad(x, flat)
kernel_name = 'adaLN_cond__mha_block__glu_gated_bilinear__prompt'

def setup_inputs():

    _w = lambda a: (lambda t: t.to(torch.bfloat16) if t.is_floating_point() else t)(torch.as_tensor(a))

    def _A_setup():
        B, S, D, H = (1, 2048, 1024, 32)
        return {'x': _w((lambda *s: torch.randn(*s, dtype=torch.bfloat16) * 0.05)(B, S, D)), 'wqkv': _w((lambda *s: torch.randn(*s, dtype=torch.bfloat16) * 0.05)(D, 3 * D)), 'wo': _w((lambda *s: torch.randn(*s, dtype=torch.bfloat16) * 0.05)(D, D))}
    _A_in = _A_setup()

    def _B_setup():
        B, S, D, F = (1, 2048, 1024, 3584)
        return {'x': _w((lambda *s: torch.randn(*s, dtype=torch.bfloat16) * 0.05)(B, S, D)), 'w_gate': _w((lambda *s: torch.randn(*s, dtype=torch.bfloat16) * 1.6)(D, F) * D ** (-0.5)), 'w_up': _w((lambda *s: torch.randn(*s, dtype=torch.bfloat16) * 1.6)(D, F) * D ** (-0.5)), 'w_down': _w((lambda *s: torch.randn(*s, dtype=torch.bfloat16) * 1.6)(F, D) * F ** (-0.5))}
    _B_in = _B_setup()
    return {'A_x': _A_in['x'], 'A_wqkv': _A_in['wqkv'], 'A_wo': _A_in['wo'], 'B_x': _B_in['x'], 'B_w_gate': _B_in['w_gate'], 'B_w_up': _B_in['w_up'], 'B_w_down': _B_in['w_down'], 'c': _w((lambda *s: torch.randn(*s, dtype=torch.bfloat16) * 0.2)(1, 1024) * 8.0), 'w_mod': _w((lambda *s: torch.randn(*s, dtype=torch.bfloat16) * 0.2)(1024, 6 * 1024) * 1024 ** (-0.5) * 8.0)}

def reference(A_x, A_wqkv, A_wo, B_x, B_w_gate, B_w_up, B_w_down, c, w_mod):

    def _A_ref(x, wqkv, wo):
        B, S, D = x.shape
        H = 32
        Dh = D // H
        qkv = (lambda x, *d: x.permute(*d))((x @ wqkv).reshape(B, S, 3, H, Dh), 2, 0, 3, 1, 4)
        q, k, v = (qkv[0], qkv[1], qkv[2])
        scores = torch.einsum('bhqd,bhkd->bhqk', q, k) * Dh ** (-0.5)
        p = _softmax(scores, axis=-1)
        o = torch.einsum('bhqk,bhkd->bhqd', p, v)
        return (lambda x, *d: x.permute(*d))(o, 0, 2, 1, 3).reshape(B, S, D) @ wo

    def _B_ref(x, w_gate, w_up, w_down):
        g = torch.einsum('bsd,df->bsf', x, w_gate)
        u = torch.einsum('bsd,df->bsf', x, w_up)
        h = g * u
        return torch.einsum('bsf,fd->bsd', h, w_down)
    x = A_x
    mod = (_silu(c) @ w_mod).reshape(c.shape[0], 6, 1, -1)
    sa, ca, ga, sb, cb, gb = (mod[:, 0].clone(), mod[:, 1].clone(), mod[:, 2].clone(), mod[:, 3].clone(), mod[:, 4].clone(), mod[:, 5].clone())
    h = _layernorm(x, axis=-1) * (1 + ca) + sa
    x = x + ga * _A_ref(h, A_wqkv, A_wo)
    h = _layernorm(x, axis=-1) * (1 + cb) + sb
    x = x + gb * _B_ref(h, B_w_gate, B_w_up, B_w_down)
    return x
```

```cpp
#include <hip/hip_runtime.h>
#include <hip/hip_bf16.h>
#include <stdint.h>

typedef unsigned short u16;
typedef short bf16x8 __attribute__((ext_vector_type(8)));
typedef short s16x4 __attribute__((ext_vector_type(4)));
typedef float f32x2 __attribute__((ext_vector_type(2)));
typedef float f32x4 __attribute__((ext_vector_type(4)));
typedef float f32x16 __attribute__((ext_vector_type(16)));
typedef unsigned u32x2 __attribute__((ext_vector_type(2)));
typedef unsigned u32x4 __attribute__((ext_vector_type(4)));
typedef __bf16 bf16x2_t __attribute__((ext_vector_type(2)));
#define LAS __attribute__((address_space(3)))
#define GAS __attribute__((address_space(1)))

constexpr int S_ = 2048, D_ = 1024, H_ = 32, DH_ = 32, F_ = 3584;

__device__ __forceinline__ float bf2f(u16 u) { return __uint_as_float((unsigned)u << 16); }
__device__ __forceinline__ unsigned pack2(float lo, float hi) {
  f32x2 v = {lo, hi};
  bf16x2_t b = __builtin_convertvector(v, bf16x2_t);
  return __builtin_bit_cast(unsigned, b);
}
__device__ __forceinline__ float rbf(float f) {
  __bf16 b = (__bf16)f;
  return __uint_as_float((unsigned)__builtin_bit_cast(u16, b) << 16);
}
__device__ __forceinline__ float lo_f(unsigned u) { return __uint_as_float(u << 16); }
__device__ __forceinline__ float hi_f(unsigned u) { return __uint_as_float(u & 0xffff0000u); }

#define WAIT_V(n) asm volatile("s_waitcnt vmcnt(%0)" ::"n"(n) : "memory")
#define WAIT_L0() asm volatile("s_waitcnt lgkmcnt(0)" ::: "memory")
#define BAR() __builtin_amdgcn_s_barrier()

__device__ __forceinline__ void glds16(const void* sbase, unsigned voff, unsigned ldsaddr) {
  asm volatile("s_mov_b32 m0, %2\n\ts_nop 0\n\tglobal_load_lds_dwordx4 %0, %1" ::"v"(voff), "s"(sbase), "s"(ldsaddr) : "memory");
}
#define SBAR() __builtin_amdgcn_sched_barrier(0)
#define SGB(mask, n) __builtin_amdgcn_sched_group_barrier(mask, n, 0)
__device__ __forceinline__ s16x4 tr_read(const char* p) {
  return __builtin_amdgcn_ds_read_tr16_b64_v4i16((LAS s16x4*)p);
}
__device__ __forceinline__ bf16x8 lds_read16(const char* p) { return *(const LAS bf16x8*)p; }

__global__ __launch_bounds__(512) void mod_kernel(const u16* __restrict__ c, const u16* __restrict__ wmod,
                                                  float* __restrict__ mod) {
  __shared__ float red[64][65];
  __shared__ float sc[1024];
  const int tid = threadIdx.x;
  for (int i = tid; i < 1024; i += 512) {
    float v = bf2f(c[i]);
    float s = v / (1.0f + __expf(-v));
    sc[i] = rbf(s);
  }
  __syncthreads();
  const int cl = tid & 7, rg = tid >> 3;
  const int col0 = blockIdx.x * 64 + cl * 8;
  float acc[8];
#pragma unroll
  for (int j = 0; j < 8; ++j) acc[j] = 0.f;
  u32x4 w[16];
#pragma unroll
  for (int i = 0; i < 16; ++i) {
    const int k = rg + i * 64;
    w[i] = __builtin_nontemporal_load((const u32x4*)(wmod + (size_t)k * 6144 + col0));
  }
#pragma unroll
  for (int i = 0; i < 16; ++i) {
    const float s = sc[rg + i * 64];
#pragma unroll
    for (int j = 0; j < 4; ++j) {
      acc[2 * j] += s * lo_f(w[i][j]);
      acc[2 * j + 1] += s * hi_f(w[i][j]);
    }
  }
#pragma unroll
  for (int j = 0; j < 8; ++j) red[rg][cl * 8 + j] = acc[j];
  __syncthreads();
  if (tid < 64) {
    float s = 0.f;
#pragma unroll 8
    for (int r = 0; r < 64; ++r) s += red[r][tid];
    mod[blockIdx.x * 64 + tid] = rbf(s);
  }
}

__global__ __launch_bounds__(256) void ln_kernel(const u16* __restrict__ x, const float* __restrict__ shift,
                                                 const float* __restrict__ scale, u16* __restrict__ h) {
  const int lane = threadIdx.x & 63, wid = threadIdx.x >> 6;
  const int row = blockIdx.x * 4 + wid;
  const u16* xr = x + (size_t)row * D_;
  u32x4 v0 = *(const u32x4*)(xr + lane * 8);
  u32x4 v1 = *(const u32x4*)(xr + 512 + lane * 8);
  float f[16];
#pragma unroll
  for (int j = 0; j < 4; ++j) {
    f[2 * j] = lo_f(v0[j]); f[2 * j + 1] = hi_f(v0[j]);
    f[8 + 2 * j] = lo_f(v1[j]); f[8 + 2 * j + 1] = hi_f(v1[j]);
  }
  float s = 0.f;
#pragma unroll
  for (int j = 0; j < 16; ++j) s += f[j];
#pragma unroll
  for (int o = 32; o >= 1; o >>= 1) s += __shfl_xor(s, o);
  const float mean = s * (1.0f / D_);
  float q = 0.f;
#pragma unroll
  for (int j = 0; j < 16; ++j) { float d = f[j] - mean; q += d * d; }
#pragma unroll
  for (int o = 32; o >= 1; o >>= 1) q += __shfl_xor(q, o);
  const float mb = rbf(mean);
  const float vb = rbf(q * (1.0f / D_));
  const float sd = rbf(sqrtf(rbf(vb + 1e-5f)));
  const float inv = 1.0f / sd;
  float o[16];
#pragma unroll
  for (int half = 0; half < 2; ++half) {
    const int c0 = half * 512 + lane * 8;
    f32x4 sa0 = *(const f32x4*)(shift + c0), sa1 = *(const f32x4*)(shift + c0 + 4);
    f32x4 ca0 = *(const f32x4*)(scale + c0), ca1 = *(const f32x4*)(scale + c0 + 4);
#pragma unroll
    for (int j = 0; j < 8; ++j) {
      const float sh = j < 4 ? sa0[j & 3] : sa1[j & 3];
      const float sc = j < 4 ? ca0[j & 3] : ca1[j & 3];
      float t = rbf(f[half * 8 + j] - mb);
      t = rbf(t * inv);
      t = rbf(t * rbf(1.0f + sc));
      o[half * 8 + j] = t + sh;
    }
  }
  u16* hr = h + (size_t)row * D_;
  u32x4 w0, w1;
#pragma unroll
  for (int j = 0; j < 4; ++j) {
    w0[j] = pack2(o[2 * j], o[2 * j + 1]);
    w1[j] = pack2(o[8 + 2 * j], o[8 + 2 * j + 1]);
  }
  *(u32x4*)(hr + lane * 8) = w0;
  *(u32x4*)(hr + 512 + lane * 8) = w1;
}

struct GemmP {
  const u16* A; const u16* B0; const u16* B1; const u16* res; const float* gate; u16* out;
};

template <int MODE> struct GCfg;
template <> struct GCfg<0> { static constexpr int BM = 128, BN = 192, WM = 2, WN = 2, NS = 3, K = 1024, LDA = 1024, LDB = 3072, NTM = 16, NTN = 16, RM = 8, RN = 4; };
template <> struct GCfg<1> { static constexpr int BM = 128, BN = 64, WM = 2, WN = 2, NS = 4, K = 1024, LDA = 1024, LDB = 1024, NTM = 16, NTN = 16, RM = 4, RN = 8; };
template <> struct GCfg<2> { static constexpr int BM = 256, BN = 256, WM = 4, WN = 2, NS = 2, K = 1024, LDA = 1024, LDB = 3584, NTM = 8, NTN = 28, RM = 4, RN = 7; };
template <> struct GCfg<3> { static constexpr int BM = 128, BN = 64, WM = 2, WN = 2, NS = 4, K = 3584, LDA = 3584, LDB = 1024, NTM = 16, NTN = 16, RM = 4, RN = 8; };

template <int MODE>
__global__ __launch_bounds__(GCfg<MODE>::WM* GCfg<MODE>::WN * 64) void gemm_kernel(GemmP p) {
  using C = GCfg<MODE>;
  constexpr int BM = C::BM, BN = C::BN, WM = C::WM, WN = C::WN, NS = C::NS, NW = WM * WN;
  constexpr int TM = BM / WM, TN = BN / WN, MI = TM / 16, NI = TN / 16;
  constexpr int A_BYTES = BM * 128, B_BYTES = BN * 128, ST_BYTES = A_BYTES + B_BYTES;
  constexpr int GA = BM / 8 / NW, GB = BN / 8 / NW, G = GA + GB;
  constexpr int NT = C::K / 64;
  static_assert(BM % (8 * NW) == 0 && BN % (8 * NW) == 0, "stage split");
  static_assert(NT >= NS + 1, "K too small");
  extern __shared__ __attribute__((aligned(16))) char smem[];

  const int tid = threadIdx.x, lane = tid & 63;
  const int wid = __builtin_amdgcn_readfirstlane(tid >> 6);
  const int wm = wid / WN, wn = wid % WN;
  int tm, tn;
  {
    const int x = blockIdx.x & 7, idx = blockIdx.x >> 3;
    constexpr int NRM = C::NTM / C::RM;
    const int rm = x % NRM, rn = x / NRM;
    tm = rm * C::RM + idx % C::RM;
    tn = rn * C::RN + idx / C::RM;
  }
  const int m0 = tm * BM;

  const int par = wid & 1;
  const unsigned a_voff = (unsigned)(((lane >> 3) * C::LDA + 8 * ((lane & 7) ^ ((4 * par + (lane >> 4)) & 7))) * 2);
  const unsigned b_voff = (unsigned)(((lane >> 3) * C::LDB + 8 * ((lane & 7) ^ ((((lane >> 4) & 1) | (par << 1)) << 1))) * 2);
  const char* Abase = (const char*)(p.A + (size_t)m0 * C::LDA);
  const unsigned lds0 = (unsigned)(size_t)(LAS char*)smem;

  auto issue = [&](int t, int bufoff) {
#pragma unroll
    for (int j = 0; j < GA; ++j) {
      const int i = wid + NW * j;
      const char* g = Abase + ((size_t)(8 * i) * C::LDA + (size_t)t * 64) * 2;
      glds16(g, a_voff, lds0 + bufoff + i * 1024);
    }
#pragma unroll
    for (int j = 0; j < GB; ++j) {
      const int i = wid + NW * j;
      const int pn = i >> 3, kr = 8 * (i & 7);
      const u16* bp;
      if constexpr (MODE == 2) bp = (pn < 2 ? p.B0 : p.B1) + tn * 128 + (pn & 1) * 64;
      else bp = p.B0 + tn * BN + pn * 64;
      const char* g = (const char*)(bp + ((size_t)t * 64 + kr) * C::LDB);
      glds16(g, b_voff, lds0 + bufoff + A_BYTES + i * 1024);
    }
  };

  const int fr = lane & 15, fq = lane >> 4;
  const int a_rd0 = (wm * TM + fr) * 128 + ((fq ^ (fr >> 1)) << 4);
  const int a_rd1 = a_rd0 ^ 64;
  int b_rd[(MODE == 2) ? 4 : NI];
  {
    const int q = (lane & 15) >> 2, pp = lane & 3;
    const int gl = ((q >> 1) & 1) | ((fq & 1) << 1);
#pragma unroll
    for (int ni = 0; ni < ((MODE == 2) ? 4 : NI); ++ni) {
      int pn, un;
      if constexpr (MODE == 2) { pn = wn; un = ni & 3; }
      else { const int j = wn * NI + ni; pn = j >> 2; un = j & 3; }
      b_rd[ni] = A_BYTES + pn * 8192 + (8 * fq + q) * 128 + ((un ^ gl) << 5) + pp * 8;
    }
  }

  f32x4 acc[MI][NI];
#pragma unroll
  for (int mi = 0; mi < MI; ++mi)
#pragma unroll
    for (int ni = 0; ni < NI; ++ni) acc[mi][ni] = (f32x4){0.f, 0.f, 0.f, 0.f};

  constexpr int NSP = (MODE == 2) ? 2 : 1;
  constexpr int NH = NI / NSP;
  bf16x8 a0[MI], a1[MI], b0[NH], b1[NH];
#define READ_B(BF, bufoff, ks, hf)                                                              \
  do {                                                                                          \
    const char* sb_ = smem + (bufoff);                                                          \
    _Pragma("unroll") for (int ni = 0; ni < NH; ++ni) {                                         \
      const int bo_ = (MODE == 2) ? b_rd[ni] + (hf) * 16384 : b_rd[ni];                         \
      s16x4 lo_ = tr_read(sb_ + bo_ + (ks) * 4096);                                             \
      s16x4 hi_ = tr_read(sb_ + bo_ + (ks) * 4096 + 512);                                       \
      BF[ni] = __builtin_shufflevector(lo_, hi_, 0, 1, 2, 3, 4, 5, 6, 7);                       \
    }                                                                                           \
  } while (0)
#define READ_A(AF, bufoff, ks)                                                                  \
  do {                                                                                          \
    const char* sb_ = smem + (bufoff);                                                          \
    _Pragma("unroll") for (int mi = 0; mi < MI; ++mi)                                           \
        AF[mi] = lds_read16(sb_ + ((ks) ? a_rd1 : a_rd0) + mi * 2048);                          \
  } while (0)
#define MMA(AF, BF, hf)                                                                         \
  do {                                                                                          \
    _Pragma("unroll") for (int mi = 0; mi < MI; ++mi)                                           \
        _Pragma("unroll") for (int ni = 0; ni < NH; ++ni)                                       \
            acc[mi][(hf) * NH + ni] = __builtin_amdgcn_mfma_f32_16x16x32_bf16(BF[ni], AF[mi], acc[mi][(hf) * NH + ni], 0, 0, 0); \
  } while (0)
#define ILV(NR)                                                                                 \
  do {                                                                                          \
    _Pragma("unroll") for (int i_ = 0; i_ < (NR); ++i_) { SGB(0x008, 1); SGB(0x100, 1); }       \
    SGB(0x008, MI * NH - (NR));                                                                 \
    SBAR();                                                                                     \
  } while (0)
#define KTILE(SYNC, LAST)                                                                       \
  do {                                                                                          \
    const int nxt = (cur + ST_BYTES == NS * ST_BYTES) ? 0 : cur + ST_BYTES;                     \
    if constexpr (NSP == 1) {                                                                   \
      READ_B(b1, cur, 1, 0); READ_A(a1, cur, 1);                                                \
      MMA(a0, b0, 0);                                                                           \
      ILV(MI * NH < 2 * NH + MI ? MI * NH : 2 * NH + MI);                                       \
      if (!(LAST)) { SYNC; READ_B(b0, nxt, 0, 0); READ_A(a0, nxt, 0); }                         \
      MMA(a1, b1, 0);                                                                           \
      if (!(LAST)) ILV(MI * NH < 2 * NH + MI ? MI * NH : 2 * NH + MI);                          \
    } else {                                                                                    \
      READ_B(b1, cur, 0, 1);                                                                    \
      MMA(a0, b0, 0);                                                                           \
      ILV(2 * NH);                                                                              \
      READ_B(b0, cur, 1, 0); READ_A(a1, cur, 1);                                                \
      MMA(a0, b1, 1);                                                                           \
      ILV(2 * NH + MI);                                                                         \
      READ_B(b1, cur, 1, 1);                                                                    \
      MMA(a1, b0, 0);                                                                           \
      ILV(2 * NH);                                                                              \
      if (!(LAST)) { SYNC; READ_B(b0, nxt, 0, 0); READ_A(a0, nxt, 0); }                         \
      MMA(a1, b1, 1);                                                                           \
      if (!(LAST)) ILV(2 * NH + MI);                                                            \
    }                                                                                           \
    cur = nxt;                                                                                  \
  } while (0)

#pragma unroll
  for (int s = 0; s < NS; ++s) issue(s, s * ST_BYTES);
  WAIT_V((NS - 1) * G);
  BAR();
  int cur = 0;
  READ_B(b0, 0, 0, 0); READ_A(a0, 0, 0);
  for (int t = 0; t < NT - NS; ++t) {
    KTILE({ WAIT_V((NS - 2) * G); WAIT_L0(); BAR(); issue(t + NS, cur); }, false);
  }
  if constexpr (NS >= 4) KTILE({ WAIT_V(2 * G); WAIT_L0(); BAR(); }, false);
  if constexpr (NS >= 3) KTILE({ WAIT_V(G); WAIT_L0(); BAR(); }, false);
  KTILE({ WAIT_V(0); WAIT_L0(); BAR(); }, false);
  KTILE({}, true);
#undef READ_A
#undef READ_B
#undef MMA
#undef KTILE
#undef ILV

  if constexpr (MODE == 0) {
    const float qs = 0.17677669529663687f * 1.4426950408889634f;
#pragma unroll
    for (int ni = 0; ni < NI; ++ni) {
      const int n = tn * BN + wn * TN + ni * 16 + 4 * fq;
      const int which = n >> 10, hd = (n >> 5) & 31, d = n & 31;
      const float sc = which == 0 ? qs : 1.0f;
      u16* op = p.out + ((size_t)(which * 32 + hd) * S_) * 32 + d;
#pragma unroll
      for (int mi = 0; mi < MI; ++mi) {
        const int m = m0 + wm * TM + mi * 16 + fr;
        u32x2 w;
        w[0] = pack2(acc[mi][ni][0] * sc, acc[mi][ni][1] * sc);
        w[1] = pack2(acc[mi][ni][2] * sc, acc[mi][ni][3] * sc);
        *(u32x2*)(op + (size_t)m * 32) = w;
      }
    }
  } else if constexpr (MODE == 1 || MODE == 3) {
#pragma unroll
    for (int ni = 0; ni < NI; ++ni) {
      const int n = tn * BN + wn * TN + ni * 16 + 4 * fq;
      const f32x4 g = *(const f32x4*)(p.gate + n);
#pragma unroll
      for (int mi = 0; mi < MI; ++mi) {
        const int m = m0 + wm * TM + mi * 16 + fr;
        const u32x2 rv = *(const u32x2*)(p.res + (size_t)m * D_ + n);
        float o0 = lo_f(rv[0]) + rbf(g[0] * rbf(acc[mi][ni][0]));
        float o1 = hi_f(rv[0]) + rbf(g[1] * rbf(acc[mi][ni][1]));
        float o2 = lo_f(rv[1]) + rbf(g[2] * rbf(acc[mi][ni][2]));
        float o3 = hi_f(rv[1]) + rbf(g[3] * rbf(acc[mi][ni][3]));
        u32x2 w;
        w[0] = pack2(o0, o1);
        w[1] = pack2(o2, o3);
        *(u32x2*)(p.out + (size_t)m * D_ + n) = w;
      }
    }
  } else {
#pragma unroll
    for (int ni = 0; ni < 4; ++ni) {
      const int n = tn * 128 + wn * 64 + ni * 16 + 4 * fq;
#pragma unroll
      for (int mi = 0; mi < MI; ++mi) {
        const int m = m0 + wm * TM + mi * 16 + fr;
        float o[4];
#pragma unroll
        for (int r = 0; r < 4; ++r) o[r] = rbf(acc[mi][ni][r]) * rbf(acc[mi][ni + 4][r]);
        u32x2 w;
        w[0] = pack2(o[0], o[1]);
        w[1] = pack2(o[2], o[3]);
        *(u32x2*)(p.out + (size_t)m * F_ + n) = w;
      }
    }
  }
}

constexpr int AT_KT = 128, AT_NS = 3, AT_ST = 16384;
#define MFMA32(a, b, c) __builtin_amdgcn_mfma_f32_32x32x16_bf16(a, b, c, 0, 0, 0)
#define MX3(a, b, c) __builtin_fmaxf(__builtin_fmaxf((a), (b)), (c))

__global__ __launch_bounds__(512, 2) void attn_kernel(const u16* __restrict__ qkv, u16* __restrict__ o) {
  extern __shared__ __attribute__((aligned(16))) char smem[];
  const int tid = threadIdx.x, lane = tid & 63;
  const int wid = __builtin_amdgcn_readfirstlane(tid >> 6);
  const int x = blockIdx.x & 7, idx = blockIdx.x >> 3;
  const int hd = x * 4 + (idx >> 3), qb = idx & 7;
  const int r32 = lane & 31, hi = lane >> 5;
  const u16* Q = qkv + ((size_t)(0 * 32 + hd) * S_) * 32;
  const u16* Kp = qkv + ((size_t)(1 * 32 + hd) * S_) * 32;
  const u16* Vp = qkv + ((size_t)(2 * 32 + hd) * S_) * 32;
  const int q0 = qb * 256 + wid * 32;

  const unsigned k_voff = (unsigned)(((lane >> 2) * 32 + 8 * ((lane & 3) ^ ((lane >> 4) & 3))) * 2);
  const unsigned v_voff = (unsigned)(lane * 16);
  const unsigned lds0 = (unsigned)(size_t)(LAS char*)smem;
  auto issue = [&](int t, int bufoff) {
    const char* gk = (const char*)(Kp + ((size_t)t * AT_KT + wid * 16) * 32);
    const char* gv = (const char*)(Vp + ((size_t)t * AT_KT + wid * 16) * 32);
    glds16(gk, k_voff, lds0 + bufoff + wid * 1024);
    glds16(gv, v_voff, lds0 + bufoff + 8192 + wid * 1024);
  };
  issue(0, 0);
  issue(1, AT_ST);
  bf16x8 qf0 = *(const bf16x8*)(Q + (size_t)(q0 + r32) * 32 + hi * 8);
  bf16x8 qf1 = *(const bf16x8*)(Q + (size_t)(q0 + r32) * 32 + 16 + hi * 8);

  const int k_rd0 = r32 * 64 + ((hi ^ ((r32 >> 2) & 3)) << 4);
  const int k_rd1 = k_rd0 ^ 32;
  const int v_rd = 8192 + (4 * hi + ((lane & 15) >> 2)) * 64 + ((lane >> 4) & 1) * 32 + (lane & 3) * 8;

  f32x16 oacc = {};
  float m = 0.f, l = 0.f;
  constexpr int NT = S_ / AT_KT;
  int cur = 0;
  for (int t = 0; t < NT; ++t) {
    if (t + 1 < NT) WAIT_V(2); else WAIT_V(0);
    BAR();
    {
      int nb = cur + 2 * AT_ST; if (nb >= AT_NS * AT_ST) nb -= AT_NS * AT_ST;
      if (t + 2 < NT) issue(t + 2, nb);
    }
    const char* sb = smem + cur;
#pragma unroll
    for (int hb = 0; hb < 2; ++hb) {
      const char* kb = sb + hb * 4096;
      bf16x8 k00 = lds_read16(kb + k_rd0), k01 = lds_read16(kb + k_rd1);
      bf16x8 k10 = lds_read16(kb + 2048 + k_rd0), k11 = lds_read16(kb + 2048 + k_rd1);
      f32x16 s0 = {}, s1 = {};
      s0 = MFMA32(k00, qf0, s0); s1 = MFMA32(k10, qf0, s1);
      s0 = MFMA32(k01, qf1, s0); s1 = MFMA32(k11, qf1, s1);
      float a = MX3(s0[0], s0[1], s1[0]), b = MX3(s0[2], s0[3], s1[1]);
      a = MX3(a, s1[2], s1[3]);
#pragma unroll
      for (int r = 4; r < 16; r += 4) {
        a = MX3(a, s0[r], s0[r + 1]); b = MX3(b, s0[r + 2], s0[r + 3]);
        a = MX3(a, s1[r], s1[r + 1]); b = MX3(b, s1[r + 2], s1[r + 3]);
      }
      float mx = __builtin_fmaxf(a, b);
      {
        auto rr = __builtin_amdgcn_permlane32_swap(__float_as_uint(mx), __float_as_uint(mx), false, false);
        mx = __builtin_fmaxf(__uint_as_float(rr[0]), __uint_as_float(rr[1]));
      }
      if (t == 0 && hb == 0) {
        m = mx;
      } else if (__any(mx > m + 8.0f)) {
        const float mn = __builtin_fmaxf(m, mx);
        const float f = __builtin_amdgcn_exp2f(m - mn);
        m = mn;
        l *= f;
#pragma unroll
        for (int r = 0; r < 16; ++r) oacc[r] *= f;
      }
      float ls = 0.f;
#pragma unroll
      for (int r = 0; r < 16; ++r) {
        s0[r] = __builtin_amdgcn_exp2f(s0[r] - m);
        s1[r] = __builtin_amdgcn_exp2f(s1[r] - m);
        ls += s0[r] + s1[r];
      }
      l += ls;
      u32x4 p0, p1, p2, p3;
#pragma unroll
      for (int j = 0; j < 4; ++j) {
        p0[j] = pack2(s0[2 * j], s0[2 * j + 1]);
        p1[j] = pack2(s0[8 + 2 * j], s0[8 + 2 * j + 1]);
        p2[j] = pack2(s1[2 * j], s1[2 * j + 1]);
        p3[j] = pack2(s1[8 + 2 * j], s1[8 + 2 * j + 1]);
      }
      const char* vb = sb + hb * 4096 + v_rd;
#define VFRAG(s) __builtin_shufflevector(tr_read(vb + (s) * 1024), tr_read(vb + (s) * 1024 + 512), 0, 1, 2, 3, 4, 5, 6, 7)
      oacc = MFMA32(VFRAG(0), __builtin_bit_cast(bf16x8, p0), oacc);
      oacc = MFMA32(VFRAG(1), __builtin_bit_cast(bf16x8, p1), oacc);
      oacc = MFMA32(VFRAG(2), __builtin_bit_cast(bf16x8, p2), oacc);
      oacc = MFMA32(VFRAG(3), __builtin_bit_cast(bf16x8, p3), oacc);
#undef VFRAG
    }
    WAIT_L0();
    cur += AT_ST; if (cur >= AT_NS * AT_ST) cur = 0;
  }
  {
    auto rr = __builtin_amdgcn_permlane32_swap(__float_as_uint(l), __float_as_uint(l), false, false);
    l = __uint_as_float(rr[0]) + __uint_as_float(rr[1]);
  }
  const float inv = 1.0f / l;
  u16* op = o + (size_t)(q0 + r32) * D_ + hd * 32 + 4 * hi;
#pragma unroll
  for (int g = 0; g < 4; ++g) {
    u32x2 w;
    w[0] = pack2(oacc[4 * g] * inv, oacc[4 * g + 1] * inv);
    w[1] = pack2(oacc[4 * g + 2] * inv, oacc[4 * g + 3] * inv);
    *(u32x2*)(op + 8 * g) = w;
  }
}

template <int MODE> static void launch_gemm(const GemmP& p, hipStream_t stream) {
  using C = GCfg<MODE>;
  constexpr int lds = C::NS * (C::BM + C::BN) * 128;
  hipFuncSetAttribute((const void*)gemm_kernel<MODE>, hipFuncAttributeMaxDynamicSharedMemorySize, lds);
  gemm_kernel<MODE><<<C::NTM * C::NTN, C::WM * C::WN * 64, lds, stream>>>(p);
}

extern "C" void kernel_launch(void* const* d_in, const int* in_sizes, int n_in, void* d_out, int out_size,
                              void* d_ws, size_t ws_size, hipStream_t stream) {
  const u16* x = (const u16*)d_in[0];
  const u16* wqkv = (const u16*)d_in[1];
  const u16* wo = (const u16*)d_in[2];
  const u16* wg = (const u16*)d_in[4];
  const u16* wu = (const u16*)d_in[5];
  const u16* wd = (const u16*)d_in[6];
  const u16* c = (const u16*)d_in[7];
  const u16* wmod = (const u16*)d_in[8];
  u16* out = (u16*)d_out;

  char* ws = (char*)d_ws;
  float* mod = (float*)ws;
  u16* h = (u16*)(ws + (1 << 20));
  u16* qkv = (u16*)(ws + (8 << 20));
  u16* ao = (u16*)(ws + (24 << 20));
  u16* x1 = (u16*)(ws + (32 << 20));
  u16* hh = (u16*)(ws + (40 << 20));

  mod_kernel<<<96, 512, 0, stream>>>(c, wmod, mod);
  ln_kernel<<<512, 256, 0, stream>>>(x, mod + 0 * 1024, mod + 1 * 1024, h);
  { GemmP p{h, wqkv, nullptr, nullptr, nullptr, qkv}; launch_gemm<0>(p, stream); }
  hipFuncSetAttribute((const void*)attn_kernel, hipFuncAttributeMaxDynamicSharedMemorySize, AT_NS * AT_ST);
  attn_kernel<<<256, 512, AT_NS * AT_ST, stream>>>(qkv, ao);
  { GemmP p{ao, wo, nullptr, x, mod + 2 * 1024, x1}; launch_gemm<1>(p, stream); }
  ln_kernel<<<512, 256, 0, stream>>>(x1, mod + 3 * 1024, mod + 4 * 1024, h);
  { GemmP p{h, wg, wu, nullptr, nullptr, hh}; launch_gemm<2>(p, stream); }
  { GemmP p{hh, wd, nullptr, x1, mod + 5 * 1024, out}; launch_gemm<3>(p, stream); }
}
```

```cpp
#include <hip/hip_runtime.h>
#include <hip/hip_bf16.h>
#include <stdint.h>

typedef unsigned short u16;
typedef short bf16x8 __attribute__((ext_vector_type(8)));
typedef short s16x4 __attribute__((ext_vector_type(4)));
typedef float f32x2 __attribute__((ext_vector_type(2)));
typedef float f32x4 __attribute__((ext_vector_type(4)));
typedef float f32x16 __attribute__((ext_vector_type(16)));
typedef unsigned u32x2 __attribute__((ext_vector_type(2)));
typedef unsigned u32x4 __attribute__((ext_vector_type(4)));
typedef __bf16 bf16x2_t __attribute__((ext_vector_type(2)));
#define LAS __attribute__((address_space(3)))
#define GAS __attribute__((address_space(1)))

constexpr int S_ = 2048, D_ = 1024, H_ = 32, DH_ = 32, F_ = 3584;

__device__ __forceinline__ float bf2f(u16 u) { return __uint_as_float((unsigned)u << 16); }
__device__ __forceinline__ unsigned pack2(float lo, float hi) {
  f32x2 v = {lo, hi};
  bf16x2_t b = __builtin_convertvector(v, bf16x2_t);
  return __builtin_bit_cast(unsigned, b);
}
__device__ __forceinline__ float rbf(float f) {
  __bf16 b = (__bf16)f;
  return __uint_as_float((unsigned)__builtin_bit_cast(u16, b) << 16);
}
__device__ __forceinline__ float lo_f(unsigned u) { return __uint_as_float(u << 16); }
__device__ __forceinline__ float hi_f(unsigned u) { return __uint_as_float(u & 0xffff0000u); }

#define STAMP_BEGIN()
#define STAMP_END(kid)
#define STAMP_MID(kid, i)
#define WAIT_V(n) asm volatile("s_waitcnt vmcnt(%0)" ::"n"(n) : "memory")
#define WAIT_L0() asm volatile("s_waitcnt lgkmcnt(0)" ::: "memory")
#define BAR() __builtin_amdgcn_s_barrier()

__device__ __forceinline__ void glds16(const void* sbase, unsigned voff, unsigned ldsaddr) {
  asm volatile("s_mov_b32 m0, %2\n\ts_nop 0\n\tglobal_load_lds_dwordx4 %0, %1" ::"v"(voff), "s"(sbase), "s"(ldsaddr) : "memory");
}
#define MFMA16(c, a, b) c = __builtin_amdgcn_mfma_f32_16x16x32_bf16(a, b, c, 0, 0, 0)
#define SBAR() __builtin_amdgcn_sched_barrier(0)
#define SGB(mask, n) __builtin_amdgcn_sched_group_barrier(mask, n, 0)
template <int I> struct IC { static constexpr int value = I; };
template <int I, int N, class F> __device__ __forceinline__ void sfor(F&& f) {
  if constexpr (I < N) { f(IC<I>{}); sfor<I + 1, N>(f); }
}
__device__ __forceinline__ s16x4 tr_read(const char* p) {
  return __builtin_amdgcn_ds_read_tr16_b64_v4i16((LAS s16x4*)p);
}
__device__ __forceinline__ bf16x8 lds_read16(const char* p) { return *(const LAS bf16x8*)p; }

__global__ __launch_bounds__(512) void mod_kernel(const u16* __restrict__ c, const u16* __restrict__ wmod,
                                                  float* __restrict__ mod, const u16* __restrict__ pf0,
                                                  const u16* __restrict__ pf1) {
  __shared__ float red[64][65];
  __shared__ float sc[1024];
  STAMP_BEGIN();
  const int tid = threadIdx.x;
  if (blockIdx.x >= 96) {
    const int b = blockIdx.x - 96;
    const char* src = b < 64 ? (const char*)pf0 + (size_t)b * 65536 : (const char*)pf1 + (size_t)(b - 64) * 65536;
    u32x4 v[8];
#pragma unroll
    for (int i = 0; i < 8; ++i) v[i] = *(const u32x4*)(src + i * 8192 + tid * 16);
#pragma unroll
    for (int i = 0; i < 8; ++i) asm volatile("" ::"v"(v[i]));
    return;
  }
  for (int i = tid; i < 1024; i += 512) {
    float v = bf2f(c[i]);
    float s = v / (1.0f + __expf(-v));
    sc[i] = rbf(s);
  }
  __syncthreads();
  const int cl = tid & 7, rg = tid >> 3;
  const int col0 = blockIdx.x * 64 + cl * 8;
  float acc[8];
#pragma unroll
  for (int j = 0; j < 8; ++j) acc[j] = 0.f;
  u32x4 w[16];
#pragma unroll
  for (int i = 0; i < 16; ++i) {
    const int k = rg + i * 64;
    w[i] = __builtin_nontemporal_load((const u32x4*)(wmod + (size_t)k * 6144 + col0));
  }
#pragma unroll
  for (int i = 0; i < 16; ++i) {
    const float s = sc[rg + i * 64];
#pragma unroll
    for (int j = 0; j < 4; ++j) {
      acc[2 * j] += s * lo_f(w[i][j]);
      acc[2 * j + 1] += s * hi_f(w[i][j]);
    }
  }
#pragma unroll
  for (int j = 0; j < 8; ++j) red[rg][cl * 8 + j] = acc[j];
  __syncthreads();
  if (tid < 64) {
    float s = 0.f;
#pragma unroll 8
    for (int r = 0; r < 64; ++r) s += red[r][tid];
    mod[blockIdx.x * 64 + tid] = rbf(s);
  }
  STAMP_END(0);
}

__global__ __launch_bounds__(256) void ln_kernel(const u16* __restrict__ x, const float* __restrict__ shift,
                                                 const float* __restrict__ scale, u16* __restrict__ h, int kid) {
  STAMP_BEGIN();
  const int lane = threadIdx.x & 63, wid = threadIdx.x >> 6;
  const int row = blockIdx.x * 4 + wid;
  const u16* xr = x + (size_t)row * D_;
  u32x4 v0 = *(const u32x4*)(xr + lane * 8);
  u32x4 v1 = *(const u32x4*)(xr + 512 + lane * 8);
  float f[16];
#pragma unroll
  for (int j = 0; j < 4; ++j) {
    f[2 * j] = lo_f(v0[j]); f[2 * j + 1] = hi_f(v0[j]);
    f[8 + 2 * j] = lo_f(v1[j]); f[8 + 2 * j + 1] = hi_f(v1[j]);
  }
  float s = 0.f;
#pragma unroll
  for (int j = 0; j < 16; ++j) s += f[j];
#pragma unroll
  for (int o = 32; o >= 1; o >>= 1) s += __shfl_xor(s, o);
  const float mean = s * (1.0f / D_);
  float q = 0.f;
#pragma unroll
  for (int j = 0; j < 16; ++j) { float d = f[j] - mean; q += d * d; }
#pragma unroll
  for (int o = 32; o >= 1; o >>= 1) q += __shfl_xor(q, o);
  const float mb = rbf(mean);
  const float vb = rbf(q * (1.0f / D_));
  const float sd = rbf(sqrtf(rbf(vb + 1e-5f)));
  const float inv = 1.0f / sd;
  float o[16];
#pragma unroll
  for (int half = 0; half < 2; ++half) {
    const int c0 = half * 512 + lane * 8;
    f32x4 sa0 = *(const f32x4*)(shift + c0), sa1 = *(const f32x4*)(shift + c0 + 4);
    f32x4 ca0 = *(const f32x4*)(scale + c0), ca1 = *(const f32x4*)(scale + c0 + 4);
#pragma unroll
    for (int j = 0; j < 8; ++j) {
      const float sh = j < 4 ? sa0[j & 3] : sa1[j & 3];
      const float sc = j < 4 ? ca0[j & 3] : ca1[j & 3];
      float t = rbf(f[half * 8 + j] - mb);
      t = rbf(t * inv);
      t = rbf(t * rbf(1.0f + sc));
      o[half * 8 + j] = t + sh;
    }
  }
  u16* hr = h + (size_t)row * D_;
  u32x4 w0, w1;
#pragma unroll
  for (int j = 0; j < 4; ++j) {
    w0[j] = pack2(o[2 * j], o[2 * j + 1]);
    w1[j] = pack2(o[8 + 2 * j], o[8 + 2 * j + 1]);
  }
  *(u32x4*)(hr + lane * 8) = w0;
  *(u32x4*)(hr + 512 + lane * 8) = w1;
  STAMP_END(kid);
}

struct GemmP {
  const u16* A; const u16* B0; const u16* B1; const u16* res; const float* gate; u16* out;
};

#define NS0 3
#define NS1 4
#define NS3 4
#define LW0 1
#define LW1 1
#define LW3 1
template <int MODE> struct GCfg;
template <> struct GCfg<0> { static constexpr int BM = 128, BN = 192, WM = 2, WN = 2, NS = NS0, K = 1024, LDA = 1024, LDB = 3072, NTM = 16, NTN = 16, RM = 8, RN = 4, LW = LW0; };
template <> struct GCfg<1> { static constexpr int BM = 128, BN = 64, WM = 2, WN = 2, NS = NS1, K = 1024, LDA = 1024, LDB = 1024, NTM = 16, NTN = 16, RM = 4, RN = 8, LW = LW1; };
template <> struct GCfg<2> { static constexpr int BM = 256, BN = 256, WM = 4, WN = 2, NS = 2, K = 1024, LDA = 1024, LDB = 3584, NTM = 8, NTN = 28, RM = 4, RN = 7, LW = 0; };
template <> struct GCfg<3> { static constexpr int BM = 128, BN = 64, WM = 2, WN = 2, NS = NS3, K = 3584, LDA = 3584, LDB = 1024, NTM = 16, NTN = 16, RM = 4, RN = 8, LW = LW3; };

template <int MODE>
__global__ __launch_bounds__(GCfg<MODE>::WM* GCfg<MODE>::WN * 64 * (1 + GCfg<MODE>::LW), 2) void gemm_kernel(GemmP p) {
  using C = GCfg<MODE>;
  constexpr int BM = C::BM, BN = C::BN, WM = C::WM, WN = C::WN, NS = C::NS, NW = WM * WN;
  constexpr int TM = BM / WM, TN = BN / WN, MI = TM / 16, NI = TN / 16;
  constexpr int A_BYTES = BM * 128, B_BYTES = BN * 128, ST_BYTES = A_BYTES + B_BYTES;
  constexpr int GA = BM / 8 / NW, GB = BN / 8 / NW, G = GA + GB;
  constexpr int NT = C::K / 64;
  static_assert(BM % (8 * NW) == 0 && BN % (8 * NW) == 0, "stage split");
  static_assert(NT >= NS + 1, "K too small");
  extern __shared__ __attribute__((aligned(16))) char smem[];
  STAMP_BEGIN();

  const int tid = threadIdx.x, lane = tid & 63;
  constexpr bool LW = C::LW != 0;
  const int wid_all = __builtin_amdgcn_readfirstlane(tid >> 6);
  const int wid = wid_all % NW;
  const int wm = wid / WN, wn = wid % WN;
  int tm, tn;
  {
    const int x = blockIdx.x & 7, idx = blockIdx.x >> 3;
    constexpr int NRM = C::NTM / C::RM;
    const int rm = x % NRM, rn = x / NRM;
    tm = rm * C::RM + idx % C::RM;
    tn = rn * C::RN + idx / C::RM;
  }
  const int m0 = tm * BM;

  const int par = wid & 1;
  const unsigned a_voff = (unsigned)(((lane >> 3) * C::LDA + 8 * ((lane & 7) ^ ((4 * par + (lane >> 4)) & 7))) * 2);
  const unsigned b_voff = (unsigned)(((lane >> 3) * C::LDB + 8 * ((lane & 7) ^ ((((lane >> 4) & 1) | (par << 1)) << 1))) * 2);
  const char* Abase = (const char*)(p.A + (size_t)m0 * C::LDA);
  const unsigned lds0 = (unsigned)(size_t)(LAS char*)smem;

  auto issue1 = [&](auto gi, int t, int bufoff) {
    constexpr int g = decltype(gi)::value;
    if constexpr (g < GA) {
      const int i = wid + NW * g;
      const char* gp = Abase + ((size_t)(8 * i) * C::LDA + (size_t)t * 64) * 2;
      glds16(gp, a_voff, lds0 + bufoff + i * 1024);
    } else {
      const int i = wid + NW * (g - GA);
      const int pn = i >> 3, kr = 8 * (i & 7);
      const u16* bp;
      if constexpr (MODE == 2) bp = (pn < 2 ? p.B0 : p.B1) + tn * 128 + (pn & 1) * 64;
      else bp = p.B0 + tn * BN + pn * 64;
      const char* gp = (const char*)(bp + ((size_t)t * 64 + kr) * C::LDB);
      glds16(gp, b_voff, lds0 + bufoff + A_BYTES + i * 1024);
    }
  };

  if constexpr (LW) {
    if (wid_all >= NW) {
      sfor<0, NS>([&](auto sc_) { sfor<0, G>([&](auto gc) { issue1(gc, decltype(sc_)::value, decltype(sc_)::value * ST_BYTES); }); });
      WAIT_V((NS - 1) * G);
      BAR();
      int lb = 0;
      for (int t = 0; t < NT - NS; ++t) {
        WAIT_V((NS - 2) * G);
        BAR();
        sfor<0, G>([&](auto gc) { issue1(gc, t + NS, lb); });
        lb = (lb + ST_BYTES == NS * ST_BYTES) ? 0 : lb + ST_BYTES;
      }
      sfor<0, NS - 1>([&](auto rc) { WAIT_V((NS - 2 - decltype(rc)::value) * G); BAR(); });
      return;
    }
  }

  const int fr = lane & 15, fq = lane >> 4;
  const int a_rd0 = (wm * TM + fr) * 128 + ((fq ^ (fr >> 1)) << 4);
  const int a_rd1 = a_rd0 ^ 64;
  int b_rd[(MODE == 2) ? 4 : NI];
  {
    const int q = (lane & 15) >> 2, pp = lane & 3;
    const int gl = ((q >> 1) & 1) | ((fq & 1) << 1);
#pragma unroll
    for (int ni = 0; ni < ((MODE == 2) ? 4 : NI); ++ni) {
      int pn, un;
      if constexpr (MODE == 2) { pn = wn; un = ni & 3; }
      else { const int j = wn * NI + ni; pn = j >> 2; un = j & 3; }
      b_rd[ni] = A_BYTES + pn * 8192 + (8 * fq + q) * 128 + ((un ^ gl) << 5) + pp * 8;
    }
  }

  f32x4 acc[MI][NI];
#pragma unroll
  for (int mi = 0; mi < MI; ++mi)
#pragma unroll
    for (int ni = 0; ni < NI; ++ni) acc[mi][ni] = (f32x4){0.f, 0.f, 0.f, 0.f};

  constexpr int NSP = (MODE == 2) ? 2 : 1;
  constexpr int NH = NI / NSP, NM = MI * NH, NPH = 2 * NSP;
  constexpr int GW = (NS >= 3) ? NPH : NPH / 2;
  constexpr int GPP = (G + GW - 1) / GW;
  bf16x8 a0[MI], a1[MI], b0[NH], b1[NH];
  s16x4 blo[NH];

  auto rd_b = [&](auto ri, bf16x8* BF, const char* sb, int ks, int hf) {
    constexpr int r = decltype(ri)::value, ni = r >> 1;
    const int bo = (MODE == 2) ? b_rd[ni] + hf * 16384 : b_rd[ni];
    if constexpr ((r & 1) == 0) blo[ni] = tr_read(sb + bo + ks * 4096);
    else BF[ni] = __builtin_shufflevector(blo[ni], tr_read(sb + bo + ks * 4096 + 512), 0, 1, 2, 3, 4, 5, 6, 7);
  };
  auto rd_a = [&](auto ri, bf16x8* AF, const char* sb, int ks) {
    constexpr int mi = decltype(ri)::value;
    AF[mi] = lds_read16(sb + (ks ? a_rd1 : a_rd0) + mi * 2048);
  };
  auto phase = [&](auto hfc, auto rdm, auto g0c, auto g1c, bf16x8* AF, bf16x8* BF, bf16x8* AN_, bf16x8* BN_,
                   const char* rsb, int rks, int rhf, int gt, int gbuf) {
    constexpr int hf = decltype(hfc)::value, RDM = decltype(rdm)::value;
    constexpr int g0 = decltype(g0c)::value, g1 = decltype(g1c)::value, NG = g1 - g0;
    constexpr int NR = RDM == 0 ? 0 : (RDM == 1 ? 2 * NH : 2 * NH + MI);
    sfor<0, NM>([&](auto ic) {
      constexpr int i = decltype(ic)::value, mi = i / NH, ni = i % NH;
      MFMA16(acc[mi][hf * NH + ni], BF[ni], AF[mi]);
      sfor<0, NR>([&](auto rc) {
        constexpr int r = decltype(rc)::value;
        if constexpr ((r * (NM - NM / 4)) / (NR > 0 ? NR : 1) == i) {
          if constexpr (r < 2 * NH) rd_b(rc, BN_, rsb, rks, rhf);
          else rd_a(IC<r - 2 * NH>{}, AN_, rsb, rks);
        }
      });
      sfor<0, NG>([&](auto gc) {
        constexpr int g = decltype(gc)::value;
        if constexpr ((g * NM) / (NG > 0 ? NG : 1) == i) issue1(IC<g0 + g>{}, gt, gbuf);
      });
      SBAR();
    });
  };
  constexpr auto gmin = [](int a, int b) { return a < b ? a : b; };
  int cur = 0, prv = 0;
  auto ktile = [&](auto vmc, auto restc, auto firstc, int t) {
    constexpr int VM = decltype(vmc)::value;
    constexpr bool REST = decltype(restc)::value && !LW, FIRST = decltype(firstc)::value && !LW;
    const int nxt = (cur + ST_BYTES == NS * ST_BYTES) ? 0 : cur + ST_BYTES;
    const char* sc = smem + cur;
    const char* sn = smem + nxt;
    constexpr int R1 = REST ? gmin(G, GPP) : 0, R2 = REST ? gmin(G, 2 * GPP) : 0, R3 = REST ? gmin(G, 3 * GPP) : 0, R4 = REST ? gmin(G, 4 * GPP) : 0;
    constexpr int F1 = FIRST ? gmin(G, GPP) : 0;
    if constexpr (NSP == 1) {
      phase(IC<0>{}, IC<2>{}, IC<R1>{}, IC<R2>{}, a0, b0, a1, b1, sc, 1, 0, t - 1 + NS, prv);
      if constexpr (VM >= 0) {
        if constexpr (!LW) WAIT_V(VM);
        WAIT_L0(); BAR();
        phase(IC<0>{}, IC<2>{}, IC<0>{}, IC<F1>{}, a1, b1, a0, b0, sn, 0, 0, t + NS, cur);
      } else {
        phase(IC<0>{}, IC<0>{}, IC<0>{}, IC<0>{}, a1, b1, a0, b0, sn, 0, 0, 0, 0);
      }
    } else {
      phase(IC<0>{}, IC<1>{}, IC<R1>{}, IC<R2>{}, a0, b0, a1, b1, sc, 0, 1, t - 1 + NS, prv);
      phase(IC<1>{}, IC<2>{}, IC<R2>{}, IC<R3>{}, a0, b1, a1, b0, sc, 1, 0, t - 1 + NS, prv);
      phase(IC<0>{}, IC<1>{}, IC<R3>{}, IC<R4>{}, a1, b0, a1, b1, sc, 1, 1, t - 1 + NS, prv);
      if constexpr (VM >= 0) {
        WAIT_V(VM); WAIT_L0(); BAR();
        phase(IC<1>{}, IC<2>{}, IC<0>{}, IC<F1>{}, a1, b1, a0, b0, sn, 0, 0, t + NS, cur);
      } else {
        phase(IC<1>{}, IC<0>{}, IC<0>{}, IC<0>{}, a1, b1, a0, b0, sn, 0, 0, 0, 0);
      }
    }
    prv = cur; cur = nxt;
  };

  if constexpr (!LW) {
    sfor<0, NS>([&](auto sc_) { sfor<0, G>([&](auto gc) { issue1(gc, decltype(sc_)::value, decltype(sc_)::value * ST_BYTES); }); });
    WAIT_V((NS - 1) * G);
  }
  BAR();
  STAMP_MID(MODE == 0 ? 2 : MODE == 1 ? 4 : MODE == 2 ? 6 : 7, 0);
  sfor<0, 2 * NH>([&](auto rc) { rd_b(rc, b0, smem, 0, 0); });
  sfor<0, MI>([&](auto rc) { rd_a(rc, a0, smem, 0); });
  using TT = IC<1>; using FF = IC<0>;
  ktile(IC<(NS - 2) * G>{}, FF{}, TT{}, 0);
  for (int t = 1; t < NT - NS; ++t) ktile(IC<(NS - 2) * G>{}, TT{}, TT{}, t);
  ktile(IC<(NS - 2) * G>{}, TT{}, FF{}, NT - NS);
  sfor<0, NS - 2>([&](auto rc) { ktile(IC<(NS - 3 - decltype(rc)::value) * G>{}, FF{}, FF{}, 0); });
  ktile(IC<-1>{}, FF{}, FF{}, 0);

  STAMP_MID(MODE == 0 ? 2 : MODE == 1 ? 4 : MODE == 2 ? 6 : 7, 1);
  if constexpr (MODE == 0) {
    const float qs = 0.17677669529663687f * 1.4426950408889634f;
#pragma unroll
    for (int ni = 0; ni < NI; ++ni) {
      const int n = tn * BN + wn * TN + ni * 16 + 4 * fq;
      const int which = n >> 10, hd = (n >> 5) & 31, d = n & 31;
      const float sc = which == 0 ? qs : 1.0f;
      u16* op = p.out + ((size_t)(which * 32 + hd) * S_) * 32 + d;
#pragma unroll
      for (int mi = 0; mi < MI; ++mi) {
        const int m = m0 + wm * TM + mi * 16 + fr;
        u32x2 w;
        w[0] = pack2(acc[mi][ni][0] * sc, acc[mi][ni][1] * sc);
        w[1] = pack2(acc[mi][ni][2] * sc, acc[mi][ni][3] * sc);
        *(u32x2*)(op + (size_t)m * 32) = w;
      }
    }
  } else if constexpr (MODE == 1 || MODE == 3) {
#pragma unroll
    for (int ni = 0; ni < NI; ++ni) {
      const int n = tn * BN + wn * TN + ni * 16 + 4 * fq;
      const f32x4 g = *(const f32x4*)(p.gate + n);
#pragma unroll
      for (int mi = 0; mi < MI; ++mi) {
        const int m = m0 + wm * TM + mi * 16 + fr;
        const u32x2 rv = *(const u32x2*)(p.res + (size_t)m * D_ + n);
        float o0 = lo_f(rv[0]) + rbf(g[0] * rbf(acc[mi][ni][0]));
        float o1 = hi_f(rv[0]) + rbf(g[1] * rbf(acc[mi][ni][1]));
        float o2 = lo_f(rv[1]) + rbf(g[2] * rbf(acc[mi][ni][2]));
        float o3 = hi_f(rv[1]) + rbf(g[3] * rbf(acc[mi][ni][3]));
        u32x2 w;
        w[0] = pack2(o0, o1);
        w[1] = pack2(o2, o3);
        *(u32x2*)(p.out + (size_t)m * D_ + n) = w;
      }
    }
  } else {
#pragma unroll
    for (int ni = 0; ni < 4; ++ni) {
      const int n = tn * 128 + wn * 64 + ni * 16 + 4 * fq;
#pragma unroll
      for (int mi = 0; mi < MI; ++mi) {
        const int m = m0 + wm * TM + mi * 16 + fr;
        float o[4];
#pragma unroll
        for (int r = 0; r < 4; ++r) o[r] = rbf(acc[mi][ni][r]) * rbf(acc[mi][ni + 4][r]);
        u32x2 w;
        w[0] = pack2(o[0], o[1]);
        w[1] = pack2(o[2], o[3]);
        *(u32x2*)(p.out + (size_t)m * F_ + n) = w;
      }
    }
  }
  STAMP_END(MODE == 0 ? 2 : MODE == 1 ? 4 : MODE == 2 ? 6 : 7);
}

#define AT_NSTAGE 6
constexpr int AT_KT = 128, AT_NS = AT_NSTAGE, AT_ST = 16384;
#define MFMA32(a, b, c) __builtin_amdgcn_mfma_f32_32x32x16_bf16(a, b, c, 0, 0, 0)
#define AEXP(x) __builtin_amdgcn_exp2f(x)
#define MX3(a, b, c) __builtin_fmaxf(__builtin_fmaxf((a), (b)), (c))

__device__ __forceinline__ float tile_rowmax(const f32x16& s0, const f32x16& s1) {
  float a = MX3(s0[0], s0[1], s1[0]), b = MX3(s0[2], s0[3], s1[1]);
  a = MX3(a, s1[2], s1[3]);
#pragma unroll
  for (int r = 4; r < 16; r += 4) {
    a = MX3(a, s0[r], s0[r + 1]); b = MX3(b, s0[r + 2], s0[r + 3]);
    a = MX3(a, s1[r], s1[r + 1]); b = MX3(b, s1[r + 2], s1[r + 3]);
  }
  float mx = __builtin_fmaxf(a, b);
  auto rr = __builtin_amdgcn_permlane32_swap(__float_as_uint(mx), __float_as_uint(mx), false, false);
  return __builtin_fmaxf(__uint_as_float(rr[0]), __uint_as_float(rr[1]));
}

__global__ __launch_bounds__(512, 2) void attn_kernel(const u16* __restrict__ qkv, u16* __restrict__ o) {
  extern __shared__ __attribute__((aligned(16))) char smem[];
  STAMP_BEGIN();
  const int tid = threadIdx.x, lane = tid & 63;
  const int wid = __builtin_amdgcn_readfirstlane(tid >> 6);
  const int x = blockIdx.x & 7, idx = blockIdx.x >> 3;
  const int hd = x * 4 + (idx >> 3), qb = idx & 7;
  const int r32 = lane & 31, hi = lane >> 5;
  const u16* Q = qkv + ((size_t)(0 * 32 + hd) * S_) * 32;
  const u16* Kp = qkv + ((size_t)(1 * 32 + hd) * S_) * 32;
  const u16* Vp = qkv + ((size_t)(2 * 32 + hd) * S_) * 32;
  const int q0 = qb * 256 + wid * 32;

  const unsigned k_voff = (unsigned)(((lane >> 2) * 32 + 8 * ((lane & 3) ^ ((lane >> 4) & 3))) * 2);
  const unsigned v_voff = (unsigned)(lane * 16);
  const unsigned lds0 = (unsigned)(size_t)(LAS char*)smem;
  auto issue = [&](int t, int bufoff) {
    const char* gk = (const char*)(Kp + ((size_t)t * AT_KT + wid * 16) * 32);
    const char* gv = (const char*)(Vp + ((size_t)t * AT_KT + wid * 16) * 32);
    glds16(gk, k_voff, lds0 + bufoff + wid * 1024);
    glds16(gv, v_voff, lds0 + bufoff + 8192 + wid * 1024);
  };
#pragma unroll
  for (int s = 0; s < AT_NS - 1; ++s) issue(s, s * AT_ST);
  const bf16x8 qf0 = *(const bf16x8*)(Q + (size_t)(q0 + r32) * 32 + hi * 8);
  const bf16x8 qf1 = *(const bf16x8*)(Q + (size_t)(q0 + r32) * 32 + 16 + hi * 8);

  const int k_rd0 = r32 * 64 + ((hi ^ ((r32 >> 2) & 3)) << 4);
  const int k_rd1 = k_rd0 ^ 32;
  const int v_rd = 8192 + (4 * hi + ((lane & 15) >> 2)) * 64 + ((lane >> 4) & 1) * 32 + (lane & 3) * 8;

  f32x16 oacc = {};
  f32x16 negm;
  float m, l = 0.f;
  f32x16 sa0, sa1, sb0, sb1;
  u32x4 pp0 = {}, pp1 = {}, pp2 = {}, pp3 = {};
  bf16x8 vf0, vf1, vf2, vf3;
#define AT_BIG 1.0e30f
  constexpr float BIG = AT_BIG;
  constexpr int NT = S_ / AT_KT;

#define SCORES(D0, D1, kb, CIN)                                                         \
  do {                                                                                  \
    bf16x8 k00 = lds_read16((kb) + k_rd0), k01 = lds_read16((kb) + k_rd1);              \
    bf16x8 k10 = lds_read16((kb) + 2048 + k_rd0), k11 = lds_read16((kb) + 2048 + k_rd1);\
    D0 = MFMA32(k00, qf0, CIN); D1 = MFMA32(k10, qf0, CIN);                             \
    D0 = MFMA32(k01, qf1, D0);  D1 = MFMA32(k11, qf1, D1);                              \
  } while (0)
#define VFRAG(vb, s) __builtin_shufflevector(tr_read((vb) + (s) * 1024), tr_read((vb) + (s) * 1024 + 512), 0, 1, 2, 3, 4, 5, 6, 7)
#define READV(vb) do { vf0 = VFRAG(vb, 0); vf1 = VFRAG(vb, 1); vf2 = VFRAG(vb, 2); vf3 = VFRAG(vb, 3); } while (0)
#define PV()                                                                            \
  do {                                                                                  \
    oacc = MFMA32(vf0, __builtin_bit_cast(bf16x8, pp0), oacc);                          \
    oacc = MFMA32(vf1, __builtin_bit_cast(bf16x8, pp1), oacc);                          \
    oacc = MFMA32(vf2, __builtin_bit_cast(bf16x8, pp2), oacc);                          \
    oacc = MFMA32(vf3, __builtin_bit_cast(bf16x8, pp3), oacc);                          \
  } while (0)
#define EXPSUM(C0, C1, LS)                                                              \
  do {                                                                                  \
    f32x2 ls2_ = {0.f, 0.f}, ls3_ = {0.f, 0.f};                                         \
    _Pragma("unroll") for (int r = 0; r < 16; r += 2) {                                 \
      C0[r] = AEXP(C0[r]); C0[r + 1] = AEXP(C0[r + 1]);                                 \
      C1[r] = AEXP(C1[r]); C1[r + 1] = AEXP(C1[r + 1]);                                 \
      ls2_ += (f32x2){C0[r], C0[r + 1]};                                                \
      ls3_ += (f32x2){C1[r], C1[r + 1]};                                                \
    }                                                                                   \
    ls2_ += ls3_;                                                                       \
    LS = ls2_[0] + ls2_[1];                                                             \
  } while (0)
#define PACK(C0, C1)                                                                    \
  do {                                                                                  \
    _Pragma("unroll") for (int j = 0; j < 4; ++j) {                                     \
      pp0[j] = pack2(C0[2 * j], C0[2 * j + 1]);                                         \
      pp1[j] = pack2(C0[8 + 2 * j], C0[8 + 2 * j + 1]);                                 \
      pp2[j] = pack2(C1[2 * j], C1[2 * j + 1]);                                         \
      pp3[j] = pack2(C1[8 + 2 * j], C1[8 + 2 * j + 1]);                                 \
    }                                                                                   \
  } while (0)
#define STEP(C0, C1, N0, N1, kcur, knext, vprev, HASN)                                  \
  do {                                                                                  \
    READV(vprev);                                                                       \
    if (HASN) SCORES(N0, N1, knext, negm);                                              \
    PV();                                                                               \
    float ls;                                                                           \
    EXPSUM(C0, C1, ls);                                                                 \
    if (__builtin_expect(__any(!(ls <= BIG)), 0)) {                                     \
                  \
      f32x16 z_ = {};                                                                   \
      SCORES(C0, C1, kcur, z_);                                                         \
      const float mn = __builtin_fmaxf(m, tile_rowmax(C0, C1));                         \
      const float dm = m - mn;                                                          \
      const float f = __builtin_amdgcn_exp2f(dm);                                       \
      m = mn; l *= f;                                                                   \
      _Pragma("unroll") for (int r = 0; r < 16; ++r) {                                  \
        oacc[r] *= f; negm[r] = -mn; C0[r] -= mn; C1[r] -= mn;                          \
        if (HASN) { N0[r] += dm; N1[r] += dm; }                                         \
      }                                                                                 \
      EXPSUM(C0, C1, ls);                                                               \
    }                                                                                   \
    l += ls;                                                                            \
    PACK(C0, C1);                                                                       \
  } while (0)

  WAIT_V(2 * (AT_NS - 2));
  BAR();
  {
    f32x16 z = {};
    SCORES(sa0, sa1, smem, z);
    m = tile_rowmax(sa0, sa1);
#pragma unroll
    for (int r = 0; r < 16; ++r) { negm[r] = -m; sa0[r] -= m; sa1[r] -= m; }
  }
  int cur = 0, prv = 0;
  for (int t = 0; t < NT - 1; ++t) {
    const char* sb = smem + cur;
    int nx = cur + AT_ST; if (nx >= AT_NS * AT_ST) nx = 0;
    int prv2 = cur - AT_ST; if (prv2 < 0) prv2 = (AT_NS - 1) * AT_ST;
    STEP(sa0, sa1, sb0, sb1, sb, sb + 4096, smem + prv + 4096 + v_rd, true);
    if (t + AT_NS - 1 < NT) {
      WAIT_V(2 * (AT_NS - 3));
      WAIT_L0();
      BAR();
      issue(t + AT_NS - 1, prv2);
    } else {
      WAIT_V(0);
      WAIT_L0();
      BAR();
    }
    STEP(sb0, sb1, sa0, sa1, sb + 4096, smem + nx, sb + v_rd, true);
    prv = cur; cur = nx;
  }
  {
    const char* sb = smem + cur;
    STEP(sa0, sa1, sb0, sb1, sb, sb + 4096, smem + prv + 4096 + v_rd, true);
    STEP(sb0, sb1, sa0, sa1, sb + 4096, sb, sb + v_rd, false);
    prv = cur;
  }
  READV(smem + prv + 4096 + v_rd);
  PV();
#undef SCORES
#undef VFRAG
#undef READV
#undef PV
#undef EXPSUM
#undef PACK
#undef STEP
  {
    auto rr = __builtin_amdgcn_permlane32_swap(__float_as_uint(l), __float_as_uint(l), false, false);
    l = __uint_as_float(rr[0]) + __uint_as_float(rr[1]);
  }
  const float inv = 1.0f / l;
  u16* op = o + (size_t)(q0 + r32) * D_ + hd * 32 + 4 * hi;
#pragma unroll
  for (int g = 0; g < 4; ++g) {
    u32x2 w;
    w[0] = pack2(oacc[4 * g] * inv, oacc[4 * g + 1] * inv);
    w[1] = pack2(oacc[4 * g + 2] * inv, oacc[4 * g + 3] * inv);
    *(u32x2*)(op + 8 * g) = w;
  }
  STAMP_END(3);
}

constexpr int A2_KT = 256, A2_NS = 3, A2_ST = 32768;
__global__ __launch_bounds__(1024, 4) void attn2_kernel(const u16* __restrict__ qkv, u16* __restrict__ o,
                                                        const u16* pw0, const u16* pw1, const u16* pw2, const u16* pw3) {
  extern __shared__ __attribute__((aligned(16))) char smem[];
  STAMP_BEGIN();
  const int tid = threadIdx.x, lane = tid & 63;
  const int wid = __builtin_amdgcn_readfirstlane(tid >> 6);
  const int wq = wid & 7, wk = wid >> 3;
  const int x = blockIdx.x & 7, idx = blockIdx.x >> 3;
  const int hd = x * 4 + (idx >> 3), qb = idx & 7;
  const int r32 = lane & 31, hi = lane >> 5;
  const u16* Q = qkv + ((size_t)(0 * 32 + hd) * S_) * 32;
  const u16* Kp = qkv + ((size_t)(1 * 32 + hd) * S_) * 32;
  const u16* Vp = qkv + ((size_t)(2 * 32 + hd) * S_) * 32;
  const int q0 = qb * 256 + wq * 32;

  const unsigned k_voff = (unsigned)(((lane >> 2) * 32 + 8 * ((lane & 3) ^ ((lane >> 4) & 3))) * 2);
  const unsigned v_voff = (unsigned)(lane * 16);
  const unsigned lds0 = (unsigned)(size_t)(LAS char*)smem;
  auto issue = [&](int t, int bufoff) {
    const char* gk = (const char*)(Kp + ((size_t)t * A2_KT + wid * 16) * 32);
    const char* gv = (const char*)(Vp + ((size_t)t * A2_KT + wid * 16) * 32);
    glds16(gk, k_voff, lds0 + bufoff + wid * 1024);
    glds16(gv, v_voff, lds0 + bufoff + 16384 + wid * 1024);
  };
  issue(0, 0);
  issue(1, A2_ST);
  const bf16x8 qf0 = *(const bf16x8*)(Q + (size_t)(q0 + r32) * 32 + hi * 8);
  const bf16x8 qf1 = *(const bf16x8*)(Q + (size_t)(q0 + r32) * 32 + 16 + hi * 8);

  const int k_rd0 = wk * 8192 + r32 * 64 + ((hi ^ ((r32 >> 2) & 3)) << 4);
  const int k_rd1 = k_rd0 ^ 32;
  const int v_rd = 16384 + wk * 8192 + (4 * hi + ((lane & 15) >> 2)) * 64 + ((lane >> 4) & 1) * 32 + (lane & 3) * 8;

  f32x16 oacc = {};
  f32x16 negm = {};
  float m = 0.f, l = 0.f;
  constexpr float BIG = AT_BIG;
  constexpr int NT = S_ / A2_KT;
  int cur = 0;
  for (int t = 0; t < NT; ++t) {
    if (t == 0) WAIT_V(2); else if (t + 1 < NT) WAIT_V(3); else WAIT_V(0);
    BAR();
    {
      int ck = (blockIdx.x * NT + t) * 16 + wid;
      if (ck >= 2048 + 3 * 7168) ck -= 16384;
      const u16* pb = pw0;
      if (ck >= 2048) { pb = pw1; ck -= 2048; if (ck >= 7168) { pb = pw2; ck -= 7168; if (ck >= 7168) { pb = pw3; ck -= 7168; } } }
      glds16((const char*)pb + (size_t)ck * 1024, v_voff, lds0 + A2_NS * A2_ST);
      int nb = cur + 2 * A2_ST; if (nb >= A2_NS * A2_ST) nb -= A2_NS * A2_ST;
      if (t + 2 < NT) issue(t + 2, nb);
    }
#pragma unroll
    for (int hb = 0; hb < 2; ++hb) {
      const char* kb = smem + cur + hb * 4096;
      const bf16x8 k00 = lds_read16(kb + k_rd0), k01 = lds_read16(kb + k_rd1);
      const bf16x8 k10 = lds_read16(kb + 2048 + k_rd0), k11 = lds_read16(kb + 2048 + k_rd1);
      f32x16 s0 = MFMA32(k00, qf0, negm), s1 = MFMA32(k10, qf0, negm);
      s0 = MFMA32(k01, qf1, s0); s1 = MFMA32(k11, qf1, s1);
      float ls;
#define EXPSUM2()                                                                       \
  do {                                                                                  \
    f32x2 ls2_ = {0.f, 0.f}, ls3_ = {0.f, 0.f};                                         \
    _Pragma("unroll") for (int r = 0; r < 16; r += 2) {                                 \
      s0[r] = AEXP(s0[r]); s0[r + 1] = AEXP(s0[r + 1]);                                 \
      s1[r] = AEXP(s1[r]); s1[r + 1] = AEXP(s1[r + 1]);                                 \
      ls2_ += (f32x2){s0[r], s0[r + 1]};                                                \
      ls3_ += (f32x2){s1[r], s1[r + 1]};                                                \
    }                                                                                   \
    ls2_ += ls3_;                                                                       \
    ls = ls2_[0] + ls2_[1];                                                             \
  } while (0)
      const bool first = (t == 0 && hb == 0);
      if (!first) EXPSUM2();
      if (first || __builtin_expect(__any(!(ls <= BIG)), 0)) {
        f32x16 z_ = {};
        s0 = MFMA32(k00, qf0, z_); s1 = MFMA32(k10, qf0, z_);
        s0 = MFMA32(k01, qf1, s0); s1 = MFMA32(k11, qf1, s1);
        const float tm = tile_rowmax(s0, s1);
        const float mn = first ? tm : __builtin_fmaxf(m, tm);
        const float f = first ? 0.f : __builtin_amdgcn_exp2f(m - mn);
        m = mn; l *= f;
#pragma unroll
        for (int r = 0; r < 16; ++r) { oacc[r] *= f; negm[r] = -mn; s0[r] -= mn; s1[r] -= mn; }
        EXPSUM2();
      }
#undef EXPSUM2
      l += ls;
      u32x4 p0, p1, p2, p3;
#pragma unroll
      for (int j = 0; j < 4; ++j) {
        p0[j] = pack2(s0[2 * j], s0[2 * j + 1]);
        p1[j] = pack2(s0[8 + 2 * j], s0[8 + 2 * j + 1]);
        p2[j] = pack2(s1[2 * j], s1[2 * j + 1]);
        p3[j] = pack2(s1[8 + 2 * j], s1[8 + 2 * j + 1]);
      }
      const char* vb = smem + cur + hb * 4096 + v_rd;
#define VFRAG(s) __builtin_shufflevector(tr_read(vb + (s) * 1024), tr_read(vb + (s) * 1024 + 512), 0, 1, 2, 3, 4, 5, 6, 7)
      oacc = MFMA32(VFRAG(0), __builtin_bit_cast(bf16x8, p0), oacc);
      oacc = MFMA32(VFRAG(1), __builtin_bit_cast(bf16x8, p1), oacc);
      oacc = MFMA32(VFRAG(2), __builtin_bit_cast(bf16x8, p2), oacc);
      oacc = MFMA32(VFRAG(3), __builtin_bit_cast(bf16x8, p3), oacc);
#undef VFRAG
    }
    WAIT_L0();
    cur += A2_ST; if (cur >= A2_NS * A2_ST) cur = 0;
  }
  {
    auto rr = __builtin_amdgcn_permlane32_swap(__float_as_uint(l), __float_as_uint(l), false, false);
    l = __uint_as_float(rr[0]) + __uint_as_float(rr[1]);
  }
  WAIT_V(0);
  __syncthreads();
  float* xb = (float*)smem + wq * (18 * 64);
  if (wk == 1) {
    xb[lane] = m; xb[64 + lane] = l;
#pragma unroll
    for (int r = 0; r < 16; ++r) xb[(2 + r) * 64 + lane] = oacc[r];
  }
  __syncthreads();
  if (wk == 0) {
    const float m2 = xb[lane], l2 = xb[64 + lane];
    const float mn = __builtin_fmaxf(m, m2);
    const float f1 = __builtin_amdgcn_exp2f(m - mn), f2 = __builtin_amdgcn_exp2f(m2 - mn);
    const float inv = 1.0f / (l * f1 + l2 * f2);
    const float g1 = f1 * inv, g2 = f2 * inv;
    u16* op = o + (size_t)(q0 + r32) * D_ + hd * 32 + 4 * hi;
#pragma unroll
    for (int g = 0; g < 4; ++g) {
      float v[4];
#pragma unroll
      for (int e = 0; e < 4; ++e) v[e] = oacc[4 * g + e] * g1 + xb[(2 + 4 * g + e) * 64 + lane] * g2;
      u32x2 w;
      w[0] = pack2(v[0], v[1]);
      w[1] = pack2(v[2], v[3]);
      *(u32x2*)(op + 8 * g) = w;
    }
  }
  STAMP_END(3);
}

template <int MODE> static void launch_gemm(const GemmP& p, hipStream_t stream) {
  using C = GCfg<MODE>;
  constexpr int lds = C::NS * (C::BM + C::BN) * 128;
  hipFuncSetAttribute((const void*)gemm_kernel<MODE>, hipFuncAttributeMaxDynamicSharedMemorySize, lds);
  gemm_kernel<MODE><<<C::NTM * C::NTN, C::WM * C::WN * 64 * (1 + C::LW), lds, stream>>>(p);
}

extern "C" void kernel_launch(void* const* d_in, const int* in_sizes, int n_in, void* d_out, int out_size,
                              void* d_ws, size_t ws_size, hipStream_t stream) {
  const u16* x = (const u16*)d_in[0];
  const u16* wqkv = (const u16*)d_in[1];
  const u16* wo = (const u16*)d_in[2];
  const u16* wg = (const u16*)d_in[4];
  const u16* wu = (const u16*)d_in[5];
  const u16* wd = (const u16*)d_in[6];
  const u16* c = (const u16*)d_in[7];
  const u16* wmod = (const u16*)d_in[8];
  u16* out = (u16*)d_out;

  char* ws = (char*)d_ws;
  float* mod = (float*)ws;
  u16* h = (u16*)(ws + (1 << 20));
  u16* qkv = (u16*)(ws + (8 << 20));
  u16* ao = (u16*)(ws + (24 << 20));
  u16* x1 = (u16*)(ws + (32 << 20));
  u16* hh = (u16*)(ws + (40 << 20));

  mod_kernel<<<256, 512, 0, stream>>>(c, wmod, mod, x, wqkv);
  ln_kernel<<<512, 256, 0, stream>>>(x, mod + 0 * 1024, mod + 1 * 1024, h, 1);
  { GemmP p{h, wqkv, nullptr, nullptr, nullptr, qkv}; launch_gemm<0>(p, stream); }
  hipFuncSetAttribute((const void*)attn2_kernel, hipFuncAttributeMaxDynamicSharedMemorySize, A2_NS * A2_ST + 1024);
  attn2_kernel<<<256, 1024, A2_NS * A2_ST + 1024, stream>>>(qkv, ao, wo, wg, wu, wd);
  { GemmP p{ao, wo, nullptr, x, mod + 2 * 1024, x1}; launch_gemm<1>(p, stream); }
  ln_kernel<<<512, 256, 0, stream>>>(x1, mod + 3 * 1024, mod + 4 * 1024, h, 5);
  { GemmP p{h, wg, wu, nullptr, nullptr, hh}; launch_gemm<2>(p, stream); }
  { GemmP p{hh, wd, nullptr, x1, mod + 5 * 1024, out}; launch_gemm<3>(p, stream); }
}
```

```cpp
#include <hip/hip_runtime.h>
#include <hip/hip_bf16.h>
#include <stdint.h>

typedef unsigned short u16;
typedef short bf16x8 __attribute__((ext_vector_type(8)));
typedef short s16x4 __attribute__((ext_vector_type(4)));
typedef float f32x2 __attribute__((ext_vector_type(2)));
typedef float f32x4 __attribute__((ext_vector_type(4)));
typedef float f32x16 __attribute__((ext_vector_type(16)));
typedef unsigned u32x2 __attribute__((ext_vector_type(2)));
typedef unsigned u32x4 __attribute__((ext_vector_type(4)));
typedef __bf16 bf16x2_t __attribute__((ext_vector_type(2)));
#define LAS __attribute__((address_space(3)))
#define GAS __attribute__((address_space(1)))

constexpr int S_ = 2048, D_ = 1024, H_ = 32, DH_ = 32, F_ = 3584;

__device__ __forceinline__ float bf2f(u16 u) { return __uint_as_float((unsigned)u << 16); }
__device__ __forceinline__ unsigned pack2(float lo, float hi) {
  f32x2 v = {lo, hi};
  bf16x2_t b = __builtin_convertvector(v, bf16x2_t);
  return __builtin_bit_cast(unsigned, b);
}
__device__ __forceinline__ float rbf(float f) {
  __bf16 b = (__bf16)f;
  return __uint_as_float((unsigned)__builtin_bit_cast(u16, b) << 16);
}
__device__ __forceinline__ float lo_f(unsigned u) { return __uint_as_float(u << 16); }
__device__ __forceinline__ float hi_f(unsigned u) { return __uint_as_float(u & 0xffff0000u); }

#define STAMP_BEGIN()
#define STAMP_END(kid)
#define STAMP_MID(kid, i)
#define WAIT_V(n) asm volatile("s_waitcnt vmcnt(%0)" ::"n"(n) : "memory")
#define WAIT_L0() asm volatile("s_waitcnt lgkmcnt(0)" ::: "memory")
#define BAR() __builtin_amdgcn_s_barrier()

__device__ __forceinline__ void glds16(const void* sbase, unsigned voff, unsigned ldsaddr) {
  asm volatile("s_mov_b32 m0, %2\n\ts_nop 0\n\tglobal_load_lds_dwordx4 %0, %1" ::"v"(voff), "s"(sbase), "s"(ldsaddr) : "memory");
}
#define MFMA16(c, a, b) c = __builtin_amdgcn_mfma_f32_16x16x32_bf16(a, b, c, 0, 0, 0)
#define ST16(p, v) asm volatile("global_store_dwordx4 %0, %1, off sc0 sc1\n\ts_nop 1" ::"v"(p), "v"(v) : "memory")
#define SBAR() __builtin_amdgcn_sched_barrier(0)
#define SGB(mask, n) __builtin_amdgcn_sched_group_barrier(mask, n, 0)
template <int I> struct IC { static constexpr int value = I; };
template <int I, int N, class F> __device__ __forceinline__ void sfor(F&& f) {
  if constexpr (I < N) { f(IC<I>{}); sfor<I + 1, N>(f); }
}
__device__ __forceinline__ s16x4 tr_read(const char* p) {
  return __builtin_amdgcn_ds_read_tr16_b64_v4i16((LAS s16x4*)p);
}
__device__ __forceinline__ bf16x8 lds_read16(const char* p) { return *(const LAS bf16x8*)p; }

__device__ __forceinline__ void mod_block(const u16* __restrict__ c, const u16* __restrict__ wmod, float* __restrict__ modp,
                                          int cg, int ks, float* sh) {
  const int tid = threadIdx.x;
  const int cl = tid & 7, rg = tid >> 3;
  const int col0 = cg * 64 + cl * 8;
  u32x4 w[8];
#pragma unroll
  for (int i = 0; i < 8; ++i)
    w[i] = __builtin_nontemporal_load((const u32x4*)(wmod + (size_t)(ks * 256 + rg + i * 32) * 6144 + col0));
  {
    const float v = bf2f(c[ks * 256 + tid]);
    sh[tid] = rbf(v / (1.0f + __expf(-v)));
  }
  __syncthreads();
  float acc[8];
#pragma unroll
  for (int j = 0; j < 8; ++j) acc[j] = 0.f;
#pragma unroll
  for (int i = 0; i < 8; ++i) {
    const float s = sh[rg + i * 32];
#pragma unroll
    for (int j = 0; j < 4; ++j) {
      acc[2 * j] += s * lo_f(w[i][j]);
      acc[2 * j + 1] += s * hi_f(w[i][j]);
    }
  }
  float* red = sh + 256;
#pragma unroll
  for (int j = 0; j < 8; ++j) red[rg * 65 + cl * 8 + j] = acc[j];
  __syncthreads();
  if (tid < 64) {
    float s = 0.f;
#pragma unroll 8
    for (int r = 0; r < 32; ++r) s += red[r * 65 + tid];
    modp[ks * 6144 + cg * 64 + tid] = s;
  }
}
__device__ __forceinline__ void touch_block(const char* src) {
  u32x4 v[8];
#pragma unroll
  for (int i = 0; i < 8; ++i) v[i] = *(const u32x4*)(src + i * 4096 + threadIdx.x * 16);
#pragma unroll
  for (int i = 0; i < 8; ++i) asm volatile("" ::"v"(v[i]));
}
__device__ __forceinline__ f32x4 ld_mod4(const float* modp, int idx) {
  f32x4 a = *(const f32x4*)(modp + idx), b = *(const f32x4*)(modp + 6144 + idx);
  f32x4 c = *(const f32x4*)(modp + 2 * 6144 + idx), d = *(const f32x4*)(modp + 3 * 6144 + idx);
  f32x4 s = (a + b) + (c + d);
  return (f32x4){rbf(s[0]), rbf(s[1]), rbf(s[2]), rbf(s[3])};
}

__global__ __launch_bounds__(256) void mod_kernel(const u16* __restrict__ c, const u16* __restrict__ wmod,
                                                  float* __restrict__ modp, const u16* __restrict__ pf0) {
  __shared__ float sh[256 + 32 * 65];
  STAMP_BEGIN();
  const int b = blockIdx.x;
  if (b < 128) mod_block(c, wmod, modp, b >> 2, b & 3, sh);
  else touch_block((const char*)pf0 + (size_t)(b - 128) * 32768);
  STAMP_END(0);
}

__global__ __launch_bounds__(256) void ln_kernel(const u16* __restrict__ x, const float* modp, int moff, u16* __restrict__ h,
                                                 const u16* __restrict__ c, const u16* __restrict__ wmod, float* modp_w,
                                                 const u16* __restrict__ pf, int kid) {
  __shared__ float sh[256 + 32 * 65];
  STAMP_BEGIN();
  const int tid = threadIdx.x;
  if (blockIdx.x >= 256) {
    const int b = blockIdx.x - 256;
    if (b < 256) mod_block(c, wmod, modp_w, 32 + (b >> 2), b & 3, sh);
    else touch_block((const char*)pf + (size_t)(b - 256) * 32768);
    return;
  }
  const int lane = tid & 63, wid = tid >> 6;
  const int row = blockIdx.x * 8 + wid * 2;
  const u16* xr = x + (size_t)row * D_;
  u32x4 v[2][2];
#pragma unroll
  for (int r = 0; r < 2; ++r) {
    v[r][0] = *(const u32x4*)(xr + r * D_ + lane * 8);
    v[r][1] = *(const u32x4*)(xr + r * D_ + 512 + lane * 8);
  }
  {
    const f32x4 s0 = ld_mod4(modp, moff + tid * 4);
    const f32x4 s1 = ld_mod4(modp, moff + 1024 + tid * 4);
    *(f32x4*)(sh + tid * 4) = s0;
    *(f32x4*)(sh + 1024 + tid * 4) = (f32x4){rbf(1.0f + s1[0]), rbf(1.0f + s1[1]), rbf(1.0f + s1[2]), rbf(1.0f + s1[3])};
  }
  __syncthreads();
#pragma unroll
  for (int r = 0; r < 2; ++r) {
    float f[16];
#pragma unroll
    for (int j = 0; j < 4; ++j) {
      f[2 * j] = lo_f(v[r][0][j]); f[2 * j + 1] = hi_f(v[r][0][j]);
      f[8 + 2 * j] = lo_f(v[r][1][j]); f[8 + 2 * j + 1] = hi_f(v[r][1][j]);
    }
    float s = 0.f;
#pragma unroll
    for (int j = 0; j < 16; ++j) s += f[j];
#pragma unroll
    for (int o = 32; o >= 1; o >>= 1) s += __shfl_xor(s, o);
    const float mean = s * (1.0f / D_);
    float q = 0.f;
#pragma unroll
    for (int j = 0; j < 16; ++j) { float d = f[j] - mean; q += d * d; }
#pragma unroll
    for (int o = 32; o >= 1; o >>= 1) q += __shfl_xor(q, o);
    const float mb = rbf(mean);
    const float vb = rbf(q * (1.0f / D_));
    const float sd = rbf(sqrtf(rbf(vb + 1e-5f)));
    const float inv = 1.0f / sd;
    float o[16];
#pragma unroll
    for (int half = 0; half < 2; ++half) {
      const int c0 = half * 512 + lane * 8;
      const f32x4 sa0 = *(const f32x4*)(sh + c0), sa1 = *(const f32x4*)(sh + c0 + 4);
      const f32x4 ca0 = *(const f32x4*)(sh + 1024 + c0), ca1 = *(const f32x4*)(sh + 1024 + c0 + 4);
#pragma unroll
      for (int j = 0; j < 8; ++j) {
        const float shf = j < 4 ? sa0[j & 3] : sa1[j & 3];
        const float sc = j < 4 ? ca0[j & 3] : ca1[j & 3];
        float t = rbf(f[half * 8 + j] - mb);
        t = rbf(t * inv);
        t = rbf(t * sc);
        o[half * 8 + j] = t + shf;
      }
    }
    u16* hr = h + (size_t)(row + r) * D_;
    u32x4 w0, w1;
#pragma unroll
    for (int j = 0; j < 4; ++j) {
      w0[j] = pack2(o[2 * j], o[2 * j + 1]);
      w1[j] = pack2(o[8 + 2 * j], o[8 + 2 * j + 1]);
    }
    ST16(hr + lane * 8, w0);
    ST16(hr + 512 + lane * 8, w1);
  }
  STAMP_END(kid);
}

struct GemmP {
  const u16* A; const u16* B0; const u16* B1; const u16* res; const float* gate; u16* out;
};

#define NS0 3
#define NS1 4
#define NS3 4
#define LW0 1
#define LW1 1
#define LW3 1
template <int MODE> struct GCfg;
template <> struct GCfg<0> { static constexpr int BM = 128, BN = 192, WM = 2, WN = 2, NS = NS0, K = 1024, LDA = 1024, LDB = 3072, NTM = 16, NTN = 16, RM = 8, RN = 4, LW = LW0; };
template <> struct GCfg<1> { static constexpr int BM = 128, BN = 64, WM = 2, WN = 2, NS = NS1, K = 1024, LDA = 1024, LDB = 1024, NTM = 16, NTN = 16, RM = 4, RN = 8, LW = LW1; };
template <> struct GCfg<2> { static constexpr int BM = 256, BN = 256, WM = 4, WN = 2, NS = 2, K = 1024, LDA = 1024, LDB = 3584, NTM = 8, NTN = 28, RM = 4, RN = 7, LW = 0; };
template <> struct GCfg<3> { static constexpr int BM = 128, BN = 64, WM = 2, WN = 2, NS = NS3, K = 3584, LDA = 3584, LDB = 1024, NTM = 16, NTN = 16, RM = 4, RN = 8, LW = LW3; };

template <int MODE>
__global__ __launch_bounds__(GCfg<MODE>::WM* GCfg<MODE>::WN * 64 * (1 + GCfg<MODE>::LW), 2) void gemm_kernel(GemmP p) {
  using C = GCfg<MODE>;
  constexpr int BM = C::BM, BN = C::BN, WM = C::WM, WN = C::WN, NS = C::NS, NW = WM * WN;
  constexpr int TM = BM / WM, TN = BN / WN, MI = TM / 16, NI = TN / 16;
  constexpr int A_BYTES = BM * 128, B_BYTES = BN * 128, ST_BYTES = A_BYTES + B_BYTES;
  constexpr int GA = BM / 8 / NW, GB = BN / 8 / NW, G = GA + GB;
  constexpr int NT = C::K / 64;
  static_assert(BM % (8 * NW) == 0 && BN % (8 * NW) == 0, "stage split");
  static_assert(NT >= NS + 1, "K too small");
  extern __shared__ __attribute__((aligned(16))) char smem[];
  STAMP_BEGIN();

  const int tid = threadIdx.x, lane = tid & 63;
  constexpr bool LW = C::LW != 0;
  const int wid_all = __builtin_amdgcn_readfirstlane(tid >> 6);
  const int wid = wid_all % NW;
  const int wm = wid / WN, wn = wid % WN;
  int tm, tn;
  {
    const int x = blockIdx.x & 7, idx = blockIdx.x >> 3;
    constexpr int NRM = C::NTM / C::RM;
    const int rm = x % NRM, rn = x / NRM;
    tm = rm * C::RM + idx % C::RM;
    tn = rn * C::RN + idx / C::RM;
  }
  const int m0 = tm * BM;

  const int par = wid & 1;
  const unsigned a_voff = (unsigned)(((lane >> 3) * C::LDA + 8 * ((lane & 7) ^ ((4 * par + (lane >> 4)) & 7))) * 2);
  const unsigned b_voff = (unsigned)(((lane >> 3) * C::LDB + 8 * ((lane & 7) ^ ((((lane >> 4) & 1) | (par << 1)) << 1))) * 2);
  const char* Abase = (const char*)(p.A + (size_t)m0 * C::LDA);
  const unsigned lds0 = (unsigned)(size_t)(LAS char*)smem;

  auto issue1 = [&](auto gi, int t, int bufoff) {
    constexpr int g = decltype(gi)::value;
    if constexpr (g < GA) {
      const int i = wid + NW * g;
      const char* gp = Abase + ((size_t)(8 * i) * C::LDA + (size_t)t * 64) * 2;
      glds16(gp, a_voff, lds0 + bufoff + i * 1024);
    } else {
      const int i = wid + NW * (g - GA);
      const int pn = i >> 3, kr = 8 * (i & 7);
      const u16* bp;
      if constexpr (MODE == 2) bp = (pn < 2 ? p.B0 : p.B1) + tn * 128 + (pn & 1) * 64;
      else bp = p.B0 + tn * BN + pn * 64;
      const char* gp = (const char*)(bp + ((size_t)t * 64 + kr) * C::LDB);
      glds16(gp, b_voff, lds0 + bufoff + A_BYTES + i * 1024);
    }
  };

  if constexpr (LW) {
    if (wid_all >= NW) {
      sfor<0, NS>([&](auto sc_) { sfor<0, G>([&](auto gc) { issue1(gc, decltype(sc_)::value, decltype(sc_)::value * ST_BYTES); }); });
      WAIT_V((NS - 1) * G);
      BAR();
      int lb = 0;
      for (int t = 0; t < NT - NS; ++t) {
        WAIT_V((NS - 2) * G);
        BAR();
        sfor<0, G>([&](auto gc) { issue1(gc, t + NS, lb); });
        lb = (lb + ST_BYTES == NS * ST_BYTES) ? 0 : lb + ST_BYTES;
      }
      sfor<0, NS - 1>([&](auto rc) { WAIT_V((NS - 2 - decltype(rc)::value) * G); BAR(); });
      return;
    }
  }

  const int fr = lane & 15, fq = lane >> 4;
  u32x4 rv[4];
  f32x4 gv[NI];
  if constexpr (MODE == 1 || MODE == 3) {
#pragma unroll
    for (int i = 0; i < 4; ++i) rv[i] = *(const u32x4*)(p.res + (size_t)(m0 + i * 32 + (tid >> 3)) * D_ + tn * BN + (tid & 7) * 8);
#pragma unroll
    for (int ni = 0; ni < NI; ++ni) gv[ni] = ld_mod4(p.gate, tn * BN + wn * TN + ni * 16 + 4 * fq);
  }
  const int a_rd0 = (wm * TM + fr) * 128 + ((fq ^ (fr >> 1)) << 4);
  const int a_rd1 = a_rd0 ^ 64;
  int b_rd[(MODE == 2) ? 4 : NI];
  {
    const int q = (lane & 15) >> 2, pp = lane & 3;
    const int gl = ((q >> 1) & 1) | ((fq & 1) << 1);
#pragma unroll
    for (int ni = 0; ni < ((MODE == 2) ? 4 : NI); ++ni) {
      int pn, un;
      if constexpr (MODE == 2) { pn = wn; un = ni & 3; }
      else { const int j = wn * NI + ni; pn = j >> 2; un = j & 3; }
      b_rd[ni] = A_BYTES + pn * 8192 + (8 * fq + q) * 128 + ((un ^ gl) << 5) + pp * 8;
    }
  }

  f32x4 acc[MI][NI];
#pragma unroll
  for (int mi = 0; mi < MI; ++mi)
#pragma unroll
    for (int ni = 0; ni < NI; ++ni) acc[mi][ni] = (f32x4){0.f, 0.f, 0.f, 0.f};

  constexpr int NSP = (MODE == 2) ? 2 : 1;
  constexpr int NH = NI / NSP, NM = MI * NH, NPH = 2 * NSP;
  constexpr int GW = (NS >= 3) ? NPH : NPH / 2;
  constexpr int GPP = (G + GW - 1) / GW;
  bf16x8 a0[MI], a1[MI], b0[NH], b1[NH];
  s16x4 blo[NH];

  auto rd_b = [&](auto ri, bf16x8* BF, const char* sb, int ks, int hf) {
    constexpr int r = decltype(ri)::value, ni = r >> 1;
    const int bo = (MODE == 2) ? b_rd[ni] + hf * 16384 : b_rd[ni];
    if constexpr ((r & 1) == 0) blo[ni] = tr_read(sb + bo + ks * 4096);
    else BF[ni] = __builtin_shufflevector(blo[ni], tr_read(sb + bo + ks * 4096 + 512), 0, 1, 2, 3, 4, 5, 6, 7);
  };
  auto rd_a = [&](auto ri, bf16x8* AF, const char* sb, int ks) {
    constexpr int mi = decltype(ri)::value;
    AF[mi] = lds_read16(sb + (ks ? a_rd1 : a_rd0) + mi * 2048);
  };
  auto phase = [&](auto hfc, auto rdm, auto g0c, auto g1c, bf16x8* AF, bf16x8* BF, bf16x8* AN_, bf16x8* BN_,
                   const char* rsb, int rks, int rhf, int gt, int gbuf) {
    constexpr int hf = decltype(hfc)::value, RDM = decltype(rdm)::value;
    constexpr int g0 = decltype(g0c)::value, g1 = decltype(g1c)::value, NG = g1 - g0;
    constexpr int NR = RDM == 0 ? 0 : (RDM == 1 ? 2 * NH : 2 * NH + MI);
    sfor<0, NM>([&](auto ic) {
      constexpr int i = decltype(ic)::value, mi = i / NH, ni = i % NH;
      MFMA16(acc[mi][hf * NH + ni], BF[ni], AF[mi]);
      sfor<0, NR>([&](auto rc) {
        constexpr int r = decltype(rc)::value;
        if constexpr ((r * (NM - NM / 4)) / (NR > 0 ? NR : 1) == i) {
          if constexpr (r < 2 * NH) rd_b(rc, BN_, rsb, rks, rhf);
          else rd_a(IC<r - 2 * NH>{}, AN_, rsb, rks);
        }
      });
      sfor<0, NG>([&](auto gc) {
        constexpr int g = decltype(gc)::value;
        if constexpr ((g * NM) / (NG > 0 ? NG : 1) == i) issue1(IC<g0 + g>{}, gt, gbuf);
      });
      SBAR();
    });
  };
  constexpr auto gmin = [](int a, int b) { return a < b ? a : b; };
  int cur = 0, prv = 0;
  auto ktile = [&](auto vmc, auto restc, auto firstc, int t) {
    constexpr int VM = decltype(vmc)::value;
    constexpr bool REST = decltype(restc)::value && !LW, FIRST = decltype(firstc)::value && !LW;
    const int nxt = (cur + ST_BYTES == NS * ST_BYTES) ? 0 : cur + ST_BYTES;
    const char* sc = smem + cur;
    const char* sn = smem + nxt;
    constexpr int R1 = REST ? gmin(G, GPP) : 0, R2 = REST ? gmin(G, 2 * GPP) : 0, R3 = REST ? gmin(G, 3 * GPP) : 0, R4 = REST ? gmin(G, 4 * GPP) : 0;
    constexpr int F1 = FIRST ? gmin(G, GPP) : 0;
    if constexpr (NSP == 1) {
      phase(IC<0>{}, IC<2>{}, IC<R1>{}, IC<R2>{}, a0, b0, a1, b1, sc, 1, 0, t - 1 + NS, prv);
      if constexpr (VM >= 0) {
        if constexpr (!LW) WAIT_V(VM);
        WAIT_L0(); BAR();
        phase(IC<0>{}, IC<2>{}, IC<0>{}, IC<F1>{}, a1, b1, a0, b0, sn, 0, 0, t + NS, cur);
      } else {
        phase(IC<0>{}, IC<0>{}, IC<0>{}, IC<0>{}, a1, b1, a0, b0, sn, 0, 0, 0, 0);
      }
    } else {
      phase(IC<0>{}, IC<1>{}, IC<R1>{}, IC<R2>{}, a0, b0, a1, b1, sc, 0, 1, t - 1 + NS, prv);
      phase(IC<1>{}, IC<2>{}, IC<R2>{}, IC<R3>{}, a0, b1, a1, b0, sc, 1, 0, t - 1 + NS, prv);
      phase(IC<0>{}, IC<1>{}, IC<R3>{}, IC<R4>{}, a1, b0, a1, b1, sc, 1, 1, t - 1 + NS, prv);
      if constexpr (VM >= 0) {
        WAIT_V(VM); WAIT_L0(); BAR();
        phase(IC<1>{}, IC<2>{}, IC<0>{}, IC<F1>{}, a1, b1, a0, b0, sn, 0, 0, t + NS, cur);
      } else {
        phase(IC<1>{}, IC<0>{}, IC<0>{}, IC<0>{}, a1, b1, a0, b0, sn, 0, 0, 0, 0);
      }
    }
    prv = cur; cur = nxt;
  };

  if constexpr (!LW) {
    sfor<0, NS>([&](auto sc_) { sfor<0, G>([&](auto gc) { issue1(gc, decltype(sc_)::value, decltype(sc_)::value * ST_BYTES); }); });
    WAIT_V((NS - 1) * G);
  }
  BAR();
  STAMP_MID(MODE == 0 ? 2 : MODE == 1 ? 4 : MODE == 2 ? 6 : 7, 0);
  sfor<0, 2 * NH>([&](auto rc) { rd_b(rc, b0, smem, 0, 0); });
  sfor<0, MI>([&](auto rc) { rd_a(rc, a0, smem, 0); });
  using TT = IC<1>; using FF = IC<0>;
  ktile(IC<(NS - 2) * G>{}, FF{}, TT{}, 0);
  for (int t = 1; t < NT - NS; ++t) ktile(IC<(NS - 2) * G>{}, TT{}, TT{}, t);
  ktile(IC<(NS - 2) * G>{}, TT{}, FF{}, NT - NS);
  sfor<0, NS - 2>([&](auto rc) { ktile(IC<(NS - 3 - decltype(rc)::value) * G>{}, FF{}, FF{}, 0); });
  ktile(IC<-1>{}, FF{}, FF{}, 0);

  STAMP_MID(MODE == 0 ? 2 : MODE == 1 ? 4 : MODE == 2 ? 6 : 7, 1);
  if constexpr (MODE == 0) {
    const float qs = 0.17677669529663687f * 1.4426950408889634f;
    BAR();
    char* st = smem + wid * (64 * 208);
#pragma unroll
    for (int ni = 0; ni < NI; ++ni) {
      const int n = tn * BN + wn * TN + ni * 16;
      const float sc = n < 1024 ? qs : 1.0f;
#pragma unroll
      for (int mi = 0; mi < MI; ++mi) {
        u32x2 w;
        w[0] = pack2(acc[mi][ni][0] * sc, acc[mi][ni][1] * sc);
        w[1] = pack2(acc[mi][ni][2] * sc, acc[mi][ni][3] * sc);
        *(LAS u32x2*)(st + (mi * 16 + fr) * 208 + ni * 32 + fq * 8) = w;
      }
    }
#pragma unroll
    for (int j = 0; j < 3; ++j) {
      const int hh = (tn * BN + wn * TN + j * 32) >> 5;
      u16* ob = p.out + ((size_t)hh * S_ + m0 + wm * TM) * 32 + lane * 8;
#pragma unroll
      for (int i = 0; i < 4; ++i) {
        const u32x4 v = *(const LAS u32x4*)(st + (i * 16 + (lane >> 2)) * 208 + j * 64 + (lane & 3) * 16);
        ST16(ob + i * 512, v);
      }
    }
  } else if constexpr (MODE == 1 || MODE == 3) {
    BAR();
#pragma unroll
    for (int ni = 0; ni < NI; ++ni) {
#pragma unroll
      for (int mi = 0; mi < MI; ++mi) {
        u32x2 w;
        w[0] = pack2(gv[ni][0] * rbf(acc[mi][ni][0]), gv[ni][1] * rbf(acc[mi][ni][1]));
        w[1] = pack2(gv[ni][2] * rbf(acc[mi][ni][2]), gv[ni][3] * rbf(acc[mi][ni][3]));
        *(LAS u32x2*)(smem + (wm * TM + mi * 16 + fr) * 144 + wn * 64 + ni * 32 + fq * 8) = w;
      }
    }
    WAIT_L0();
    BAR();
#pragma unroll
    for (int i = 0; i < 4; ++i) {
      const u32x4 v = *(const LAS u32x4*)(smem + (i * 32 + (tid >> 3)) * 144 + (tid & 7) * 16);
      u32x4 w;
#pragma unroll
      for (int j = 0; j < 4; ++j) w[j] = pack2(lo_f(rv[i][j]) + lo_f(v[j]), hi_f(rv[i][j]) + hi_f(v[j]));
      ST16(p.out + (size_t)(m0 + i * 32 + (tid >> 3)) * D_ + tn * BN + (tid & 7) * 8, w);
    }
  } else {
    BAR();
    char* st = smem + wid * (64 * 144);
#pragma unroll
    for (int ni = 0; ni < 4; ++ni)
#pragma unroll
      for (int mi = 0; mi < MI; ++mi) {
        float o[4];
#pragma unroll
        for (int r = 0; r < 4; ++r) o[r] = rbf(acc[mi][ni][r]) * rbf(acc[mi][ni + 4][r]);
        u32x2 w;
        w[0] = pack2(o[0], o[1]);
        w[1] = pack2(o[2], o[3]);
        *(LAS u32x2*)(st + (mi * 16 + fr) * 144 + ni * 32 + fq * 8) = w;
      }
    u16* ob = p.out + (size_t)(m0 + wm * TM + (lane >> 3)) * F_ + tn * 128 + wn * 64 + (lane & 7) * 8;
#pragma unroll
    for (int i = 0; i < 8; ++i) {
      const u32x4 v = *(const LAS u32x4*)(st + (i * 8 + (lane >> 3)) * 144 + (lane & 7) * 16);
      ST16(ob + (size_t)i * 8 * F_, v);
    }
  }
  STAMP_END(MODE == 0 ? 2 : MODE == 1 ? 4 : MODE == 2 ? 6 : 7);
}

#define AT_NSTAGE 6
constexpr int AT_KT = 128, AT_NS = AT_NSTAGE, AT_ST = 16384;
#define MFMA32(a, b, c) __builtin_amdgcn_mfma_f32_32x32x16_bf16(a, b, c, 0, 0, 0)
#define AEXP(x) __builtin_amdgcn_exp2f(x)
#define MX3(a, b, c) __builtin_fmaxf(__builtin_fmaxf((a), (b)), (c))

__device__ __forceinline__ float tile_rowmax(const f32x16& s0, const f32x16& s1) {
  float a = MX3(s0[0], s0[1], s1[0]), b = MX3(s0[2], s0[3], s1[1]);
  a = MX3(a, s1[2], s1[3]);
#pragma unroll
  for (int r = 4; r < 16; r += 4) {
    a = MX3(a, s0[r], s0[r + 1]); b = MX3(b, s0[r + 2], s0[r + 3]);
    a = MX3(a, s1[r], s1[r + 1]); b = MX3(b, s1[r + 2], s1[r + 3]);
  }
  float mx = __builtin_fmaxf(a, b);
  auto rr = __builtin_amdgcn_permlane32_swap(__float_as_uint(mx), __float_as_uint(mx), false, false);
  return __builtin_fmaxf(__uint_as_float(rr[0]), __uint_as_float(rr[1]));
}

__global__ __launch_bounds__(512, 2) void attn_kernel(const u16* __restrict__ qkv, u16* __restrict__ o) {
  extern __shared__ __attribute__((aligned(16))) char smem[];
  STAMP_BEGIN();
  const int tid = threadIdx.x, lane = tid & 63;
  const int wid = __builtin_amdgcn_readfirstlane(tid >> 6);
  const int x = blockIdx.x & 7, idx = blockIdx.x >> 3;
  const int hd = x * 4 + (idx >> 3), qb = idx & 7;
  const int r32 = lane & 31, hi = lane >> 5;
  const u16* Q = qkv + ((size_t)(0 * 32 + hd) * S_) * 32;
  const u16* Kp = qkv + ((size_t)(1 * 32 + hd) * S_) * 32;
  const u16* Vp = qkv + ((size_t)(2 * 32 + hd) * S_) * 32;
  const int q0 = qb * 256 + wid * 32;

  const unsigned k_voff = (unsigned)(((lane >> 2) * 32 + 8 * ((lane & 3) ^ ((lane >> 4) & 3))) * 2);
  const unsigned v_voff = (unsigned)(lane * 16);
  const unsigned lds0 = (unsigned)(size_t)(LAS char*)smem;
  auto issue = [&](int t, int bufoff) {
    const char* gk = (const char*)(Kp + ((size_t)t * AT_KT + wid * 16) * 32);
    const char* gv = (const char*)(Vp + ((size_t)t * AT_KT + wid * 16) * 32);
    glds16(gk, k_voff, lds0 + bufoff + wid * 1024);
    glds16(gv, v_voff, lds0 + bufoff + 8192 + wid * 1024);
  };
#pragma unroll
  for (int s = 0; s < AT_NS - 1; ++s) issue(s, s * AT_ST);
  const bf16x8 qf0 = *(const bf16x8*)(Q + (size_t)(q0 + r32) * 32 + hi * 8);
  const bf16x8 qf1 = *(const bf16x8*)(Q + (size_t)(q0 + r32) * 32 + 16 + hi * 8);

  const int k_rd0 = r32 * 64 + ((hi ^ ((r32 >> 2) & 3)) << 4);
  const int k_rd1 = k_rd0 ^ 32;
  const int v_rd = 8192 + (4 * hi + ((lane & 15) >> 2)) * 64 + ((lane >> 4) & 1) * 32 + (lane & 3) * 8;

  f32x16 oacc = {};
  f32x16 negm;
  float m, l = 0.f;
  f32x16 sa0, sa1, sb0, sb1;
  u32x4 pp0 = {}, pp1 = {}, pp2 = {}, pp3 = {};
  bf16x8 vf0, vf1, vf2, vf3;
#define AT_BIG 1.0e30f
  constexpr float BIG = AT_BIG;
  constexpr int NT = S_ / AT_KT;

#define SCORES(D0, D1, kb, CIN)                                                         \
  do {                                                                                  \
    bf16x8 k00 = lds_read16((kb) + k_rd0), k01 = lds_read16((kb) + k_rd1);              \
    bf16x8 k10 = lds_read16((kb) + 2048 + k_rd0), k11 = lds_read16((kb) + 2048 + k_rd1);\
    D0 = MFMA32(k00, qf0, CIN); D1 = MFMA32(k10, qf0, CIN);                             \
    D0 = MFMA32(k01, qf1, D0);  D1 = MFMA32(k11, qf1, D1);                              \
  } while (0)
#define VFRAG(vb, s) __builtin_shufflevector(tr_read((vb) + (s) * 1024), tr_read((vb) + (s) * 1024 + 512), 0, 1, 2, 3, 4, 5, 6, 7)
#define READV(vb) do { vf0 = VFRAG(vb, 0); vf1 = VFRAG(vb, 1); vf2 = VFRAG(vb, 2); vf3 = VFRAG(vb, 3); } while (0)
#define PV()                                                                            \
  do {                                                                                  \
    oacc = MFMA32(vf0, __builtin_bit_cast(bf16x8, pp0), oacc);                          \
    oacc = MFMA32(vf1, __builtin_bit_cast(bf16x8, pp1), oacc);                          \
    oacc = MFMA32(vf2, __builtin_bit_cast(bf16x8, pp2), oacc);                          \
    oacc = MFMA32(vf3, __builtin_bit_cast(bf16x8, pp3), oacc);                          \
  } while (0)
#define EXPSUM(C0, C1, LS)                                                              \
  do {                                                                                  \
    f32x2 ls2_ = {0.f, 0.f}, ls3_ = {0.f, 0.f};                                         \
    _Pragma("unroll") for (int r = 0; r < 16; r += 2) {                                 \
      C0[r] = AEXP(C0[r]); C0[r + 1] = AEXP(C0[r + 1]);                                 \
      C1[r] = AEXP(C1[r]); C1[r + 1] = AEXP(C1[r + 1]);                                 \
      ls2_ += (f32x2){C0[r], C0[r + 1]};                                                \
      ls3_ += (f32x2){C1[r], C1[r + 1]};                                                \
    }                                                                                   \
    ls2_ += ls3_;                                                                       \
    LS = ls2_[0] + ls2_[1];                                                             \
  } while (0)
#define PACK(C0, C1)                                                                    \
  do {                                                                                  \
    _Pragma("unroll") for (int j = 0; j < 4; ++j) {                                     \
      pp0[j] = pack2(C0[2 * j], C0[2 * j + 1]);                                         \
      pp1[j] = pack2(C0[8 + 2 * j], C0[8 + 2 * j + 1]);                                 \
      pp2[j] = pack2(C1[2 * j], C1[2 * j + 1]);                                         \
      pp3[j] = pack2(C1[8 + 2 * j], C1[8 + 2 * j + 1]);                                 \
    }                                                                                   \
  } while (0)
#define STEP(C0, C1, N0, N1, kcur, knext, vprev, HASN)                                  \
  do {                                                                                  \
    READV(vprev);                                                                       \
    if (HASN) SCORES(N0, N1, knext, negm);                                              \
    PV();                                                                               \
    float ls;                                                                           \
    EXPSUM(C0, C1, ls);                                                                 \
    if (__builtin_expect(__any(!(ls <= BIG)), 0)) {                                     \
                  \
      f32x16 z_ = {};                                                                   \
      SCORES(C0, C1, kcur, z_);                                                         \
      const float mn = __builtin_fmaxf(m, tile_rowmax(C0, C1));                         \
      const float dm = m - mn;                                                          \
      const float f = __builtin_amdgcn_exp2f(dm);                                       \
      m = mn; l *= f;                                                                   \
      _Pragma("unroll") for (int r = 0; r < 16; ++r) {                                  \
        oacc[r] *= f; negm[r] = -mn; C0[r] -= mn; C1[r] -= mn;                          \
        if (HASN) { N0[r] += dm; N1[r] += dm; }                                         \
      }                                                                                 \
      EXPSUM(C0, C1, ls);                                                               \
    }                                                                                   \
    l += ls;                                                                            \
    PACK(C0, C1);                                                                       \
  } while (0)

  WAIT_V(2 * (AT_NS - 2));
  BAR();
  {
    f32x16 z = {};
    SCORES(sa0, sa1, smem, z);
    m = tile_rowmax(sa0, sa1);
#pragma unroll
    for (int r = 0; r < 16; ++r) { negm[r] = -m; sa0[r] -= m; sa1[r] -= m; }
  }
  int cur = 0, prv = 0;
  for (int t = 0; t < NT - 1; ++t) {
    const char* sb = smem + cur;
    int nx = cur + AT_ST; if (nx >= AT_NS * AT_ST) nx = 0;
    int prv2 = cur - AT_ST; if (prv2 < 0) prv2 = (AT_NS - 1) * AT_ST;
    STEP(sa0, sa1, sb0, sb1, sb, sb + 4096, smem + prv + 4096 + v_rd, true);
    if (t + AT_NS - 1 < NT) {
      WAIT_V(2 * (AT_NS - 3));
      WAIT_L0();
      BAR();
      issue(t + AT_NS - 1, prv2);
    } else {
      WAIT_V(0);
      WAIT_L0();
      BAR();
    }
    STEP(sb0, sb1, sa0, sa1, sb + 4096, smem + nx, sb + v_rd, true);
    prv = cur; cur = nx;
  }
  {
    const char* sb = smem + cur;
    STEP(sa0, sa1, sb0, sb1, sb, sb + 4096, smem + prv + 4096 + v_rd, true);
    STEP(sb0, sb1, sa0, sa1, sb + 4096, sb, sb + v_rd, false);
    prv = cur;
  }
  READV(smem + prv + 4096 + v_rd);
  PV();
#undef SCORES
#undef VFRAG
#undef READV
#undef PV
#undef EXPSUM
#undef PACK
#undef STEP
  {
    auto rr = __builtin_amdgcn_permlane32_swap(__float_as_uint(l), __float_as_uint(l), false, false);
    l = __uint_as_float(rr[0]) + __uint_as_float(rr[1]);
  }
  const float inv = 1.0f / l;
  u16* op = o + (size_t)(q0 + r32) * D_ + hd * 32 + 4 * hi;
#pragma unroll
  for (int g = 0; g < 4; ++g) {
    u32x2 w;
    w[0] = pack2(oacc[4 * g] * inv, oacc[4 * g + 1] * inv);
    w[1] = pack2(oacc[4 * g + 2] * inv, oacc[4 * g + 3] * inv);
    *(u32x2*)(op + 8 * g) = w;
  }
  STAMP_END(3);
}

constexpr int A2_KT = 256, A2_NS = 3, A2_ST = 32768;
template <bool SAFE>
__device__ __forceinline__ void attn_pass(char* smem, const u16* Kp, const u16* Vp, bf16x8 qf0, bf16x8 qf1, int wid, int lane,
                                          f32x16& oacc, float& m, float& l,
                                          const u16* pw0, const u16* pw1, const u16* pw2, const u16* pw3) {
  const int wk = wid >> 3, r32 = lane & 31, hi = lane >> 5;
  const unsigned k_voff = (unsigned)(((lane >> 2) * 32 + 8 * ((lane & 3) ^ ((lane >> 4) & 3))) * 2);
  const unsigned v_voff = (unsigned)(lane * 16);
  const unsigned lds0 = (unsigned)(size_t)(LAS char*)smem;
  auto issue = [&](int t, int bufoff) {
    const char* gk = (const char*)(Kp + ((size_t)t * A2_KT + wid * 16) * 32);
    const char* gv = (const char*)(Vp + ((size_t)t * A2_KT + wid * 16) * 32);
    glds16(gk, k_voff, lds0 + bufoff + wid * 1024);
    glds16(gv, v_voff, lds0 + bufoff + 16384 + wid * 1024);
  };
  issue(0, 0);
  issue(1, A2_ST);
  const int k_rd0 = wk * 8192 + r32 * 64 + ((hi ^ ((r32 >> 2) & 3)) << 4);
  const int k_rd1 = k_rd0 ^ 32;
  const int v_rd = 16384 + wk * 8192 + (4 * hi + ((lane & 15) >> 2)) * 64 + ((lane >> 4) & 1) * 32 + (lane & 3) * 8;
  f32x16 negm = {};
  f32x2 lsa = {0.f, 0.f}, lsb = {0.f, 0.f};
  oacc = (f32x16){};
  m = 0.f; l = 0.f;
  constexpr int NT = S_ / A2_KT;
  int cur = 0;
  for (int t = 0; t < NT; ++t) {
    if (t == 0) WAIT_V(2); else if (t + 1 < NT) WAIT_V(3); else WAIT_V(0);
    BAR();
    {
      int ck = (blockIdx.x * NT + t) * 16 + wid;
      if (ck >= 2048 + 3 * 7168) ck -= 16384;
      const u16* pb = pw0;
      if (ck >= 2048) { pb = pw1; ck -= 2048; if (ck >= 7168) { pb = pw2; ck -= 7168; if (ck >= 7168) { pb = pw3; ck -= 7168; } } }
      glds16((const char*)pb + (size_t)ck * 1024, v_voff, lds0 + A2_NS * A2_ST);
      int nb = cur + 2 * A2_ST; if (nb >= A2_NS * A2_ST) nb -= A2_NS * A2_ST;
      if (t + 2 < NT) issue(t + 2, nb);
    }
#pragma unroll
    for (int hb = 0; hb < 2; ++hb) {
      const char* kb = smem + cur + hb * 4096;
      const bf16x8 k00 = lds_read16(kb + k_rd0), k01 = lds_read16(kb + k_rd1);
      const bf16x8 k10 = lds_read16(kb + 2048 + k_rd0), k11 = lds_read16(kb + 2048 + k_rd1);
      f32x16 s0, s1;
      if (SAFE || (t == 0 && hb == 0)) {
        f32x16 z_ = {};
        s0 = MFMA32(k00, qf0, z_); s1 = MFMA32(k10, qf0, z_);
        s0 = MFMA32(k01, qf1, s0); s1 = MFMA32(k11, qf1, s1);
        const float tm = tile_rowmax(s0, s1);
        const bool first = (t == 0 && hb == 0);
        const float mn = first ? tm : __builtin_fmaxf(m, tm);
        if (SAFE) {
          const float f = first ? 0.f : __builtin_amdgcn_exp2f(m - mn);
          lsa *= f; lsb *= f;
          oacc *= f;
        }
        m = mn;
#pragma unroll
        for (int r = 0; r < 16; ++r) { negm[r] = -mn; s0[r] -= mn; s1[r] -= mn; }
      } else {
        s0 = MFMA32(k00, qf0, negm); s1 = MFMA32(k10, qf0, negm);
        s0 = MFMA32(k01, qf1, s0); s1 = MFMA32(k11, qf1, s1);
      }
#pragma unroll
      for (int r = 0; r < 16; r += 2) {
        s0[r] = AEXP(s0[r]); s0[r + 1] = AEXP(s0[r + 1]);
        s1[r] = AEXP(s1[r]); s1[r + 1] = AEXP(s1[r + 1]);
        lsa += (f32x2){s0[r], s0[r + 1]};
        lsb += (f32x2){s1[r], s1[r + 1]};
      }
      u32x4 p0, p1, p2, p3;
#pragma unroll
      for (int j = 0; j < 4; ++j) {
        p0[j] = pack2(s0[2 * j], s0[2 * j + 1]);
        p1[j] = pack2(s0[8 + 2 * j], s0[8 + 2 * j + 1]);
        p2[j] = pack2(s1[2 * j], s1[2 * j + 1]);
        p3[j] = pack2(s1[8 + 2 * j], s1[8 + 2 * j + 1]);
      }
      const char* vb = smem + cur + hb * 4096 + v_rd;
#define VFRAG(s) __builtin_shufflevector(tr_read(vb + (s) * 1024), tr_read(vb + (s) * 1024 + 512), 0, 1, 2, 3, 4, 5, 6, 7)
      oacc = MFMA32(VFRAG(0), __builtin_bit_cast(bf16x8, p0), oacc);
      oacc = MFMA32(VFRAG(1), __builtin_bit_cast(bf16x8, p1), oacc);
      oacc = MFMA32(VFRAG(2), __builtin_bit_cast(bf16x8, p2), oacc);
      oacc = MFMA32(VFRAG(3), __builtin_bit_cast(bf16x8, p3), oacc);
#undef VFRAG
    }
    WAIT_L0();
    cur += A2_ST; if (cur >= A2_NS * A2_ST) cur = 0;
  }
  lsa += lsb;
  l = lsa[0] + lsa[1];
  {
    auto rr = __builtin_amdgcn_permlane32_swap(__float_as_uint(l), __float_as_uint(l), false, false);
    l = __uint_as_float(rr[0]) + __uint_as_float(rr[1]);
  }
  WAIT_V(0);
}

__global__ __launch_bounds__(1024, 4) void attn2_kernel(const u16* __restrict__ qkv, u16* __restrict__ o,
                                                        const u16* pw0, const u16* pw1, const u16* pw2, const u16* pw3) {
  extern __shared__ __attribute__((aligned(16))) char smem[];
  STAMP_BEGIN();
  const int tid = threadIdx.x, lane = tid & 63;
  const int wid = __builtin_amdgcn_readfirstlane(tid >> 6);
  const int wq = wid & 7, wk = wid >> 3;
  const int x = blockIdx.x & 7, idx = blockIdx.x >> 3;
  const int hd = x * 4 + (idx >> 3), qb = idx & 7;
  const int r32 = lane & 31, hi = lane >> 5;
  const u16* Q = qkv + ((size_t)(0 * 32 + hd) * S_) * 32;
  const u16* Kp = qkv + ((size_t)(1 * 32 + hd) * S_) * 32;
  const u16* Vp = qkv + ((size_t)(2 * 32 + hd) * S_) * 32;
  const int q0 = qb * 256 + wq * 32;
  const bf16x8 qf0 = *(const bf16x8*)(Q + (size_t)(q0 + r32) * 32 + hi * 8);
  const bf16x8 qf1 = *(const bf16x8*)(Q + (size_t)(q0 + r32) * 32 + 16 + hi * 8);

  f32x16 oacc;
  float m, l;
  attn_pass<false>(smem, Kp, Vp, qf0, qf1, wid, lane, oacc, m, l, pw0, pw1, pw2, pw3);
  {
    float chk = l;
#pragma unroll
    for (int r = 0; r < 16; ++r) chk += __builtin_fabsf(oacc[r]);
    const int bad = !(chk < 1.0e37f);
    if (__syncthreads_or(bad)) attn_pass<true>(smem, Kp, Vp, qf0, qf1, wid, lane, oacc, m, l, pw0, pw1, pw2, pw3);
  }
  __syncthreads();
  float* xb = (float*)smem + wq * (18 * 64);
  if (wk == 1) {
    xb[lane] = m; xb[64 + lane] = l;
#pragma unroll
    for (int r = 0; r < 16; ++r) xb[(2 + r) * 64 + lane] = oacc[r];
  }
  __syncthreads();
  if (wk == 0) {
    const float m2 = xb[lane], l2 = xb[64 + lane];
    const float mn = __builtin_fmaxf(m, m2);
    const float e1 = (m - mn) * 0.5f, e2 = (m2 - mn) * 0.5f;
    const float f1 = __builtin_amdgcn_exp2f(e1), f2 = __builtin_amdgcn_exp2f(e2);
    const float lt = (l * f1) * f1 + (l2 * f2) * f2;
    const float inv = 1.0f / lt;
    u16* op = o + (size_t)(q0 + r32) * D_ + hd * 32 + 4 * hi;
#pragma unroll
    for (int g = 0; g < 4; ++g) {
      float v[4];
#pragma unroll
      for (int e = 0; e < 4; ++e) v[e] = ((oacc[4 * g + e] * f1) * f1 + (xb[(2 + 4 * g + e) * 64 + lane] * f2) * f2) * inv;
      u32x2 w;
      w[0] = pack2(v[0], v[1]);
      w[1] = pack2(v[2], v[3]);
      *(u32x2*)(op + 8 * g) = w;
    }
  }
  STAMP_END(3);
}

template <int MODE> static void launch_gemm(const GemmP& p, hipStream_t stream) {
  using C = GCfg<MODE>;
  constexpr int lds = C::NS * (C::BM + C::BN) * 128;
  hipFuncSetAttribute((const void*)gemm_kernel<MODE>, hipFuncAttributeMaxDynamicSharedMemorySize, lds);
  gemm_kernel<MODE><<<C::NTM * C::NTN, C::WM * C::WN * 64 * (1 + C::LW), lds, stream>>>(p);
}

extern "C" void kernel_launch(void* const* d_in, const int* in_sizes, int n_in, void* d_out, int out_size,
                              void* d_ws, size_t ws_size, hipStream_t stream) {
  const u16* x = (const u16*)d_in[0];
  const u16* wqkv = (const u16*)d_in[1];
  const u16* wo = (const u16*)d_in[2];
  const u16* wg = (const u16*)d_in[4];
  const u16* wu = (const u16*)d_in[5];
  const u16* wd = (const u16*)d_in[6];
  const u16* c = (const u16*)d_in[7];
  const u16* wmod = (const u16*)d_in[8];
  u16* out = (u16*)d_out;

  char* ws = (char*)d_ws;
  float* mod = (float*)ws;
  u16* h = (u16*)(ws + (1 << 20));
  u16* qkv = (u16*)(ws + (8 << 20));
  u16* ao = (u16*)(ws + (24 << 20));
  u16* x1 = (u16*)(ws + (32 << 20));
  u16* hh = (u16*)(ws + (40 << 20));

  mod_kernel<<<256, 256, 0, stream>>>(c, wmod, mod, x);
  ln_kernel<<<256 + 256 + 192, 256, 0, stream>>>(x, mod, 0, h, c, wmod, mod, wqkv, 1);
  { GemmP p{h, wqkv, nullptr, nullptr, nullptr, qkv}; launch_gemm<0>(p, stream); }
  hipFuncSetAttribute((const void*)attn2_kernel, hipFuncAttributeMaxDynamicSharedMemorySize, A2_NS * A2_ST + 1024);
  attn2_kernel<<<256, 1024, A2_NS * A2_ST + 1024, stream>>>(qkv, ao, wo, wg, wu, wd);
  { GemmP p{ao, wo, nullptr, x, mod + 2 * 1024, x1}; launch_gemm<1>(p, stream); }
  ln_kernel<<<256, 256, 0, stream>>>(x1, mod, 3 * 1024, h, c, wmod, mod, wqkv, 5);
  { GemmP p{h, wg, wu, nullptr, nullptr, hh}; launch_gemm<2>(p, stream); }
  { GemmP p{hh, wd, nullptr, x1, mod + 5 * 1024, out}; launch_gemm<3>(p, stream); }
}
```

```cpp
#define AT_LSUM_MFMA
#include <hip/hip_runtime.h>
#include <hip/hip_bf16.h>
#include <stdint.h>

typedef unsigned short u16;
typedef short bf16x8 __attribute__((ext_vector_type(8)));
typedef short s16x4 __attribute__((ext_vector_type(4)));
typedef float f32x2 __attribute__((ext_vector_type(2)));
typedef float f32x4 __attribute__((ext_vector_type(4)));
typedef float f32x16 __attribute__((ext_vector_type(16)));
typedef unsigned u32x2 __attribute__((ext_vector_type(2)));
typedef unsigned u32x4 __attribute__((ext_vector_type(4)));
typedef __bf16 bf16x2_t __attribute__((ext_vector_type(2)));
#define LAS __attribute__((address_space(3)))
#define GAS __attribute__((address_space(1)))

constexpr int S_ = 2048, D_ = 1024, H_ = 32, DH_ = 32, F_ = 3584;

__device__ __forceinline__ float bf2f(u16 u) { return __uint_as_float((unsigned)u << 16); }
__device__ __forceinline__ unsigned pack2(float lo, float hi) {
  f32x2 v = {lo, hi};
  bf16x2_t b = __builtin_convertvector(v, bf16x2_t);
  return __builtin_bit_cast(unsigned, b);
}
__device__ __forceinline__ float rbf(float f) {
  __bf16 b = (__bf16)f;
  return __uint_as_float((unsigned)__builtin_bit_cast(u16, b) << 16);
}
__device__ __forceinline__ float lo_f(unsigned u) { return __uint_as_float(u << 16); }
__device__ __forceinline__ float hi_f(unsigned u) { return __uint_as_float(u & 0xffff0000u); }

#define STAMP_BEGIN()
#define STAMP_END(kid)
#define STAMP_MID(kid, i)
#define WAIT_V(n) asm volatile("s_waitcnt vmcnt(%0)" ::"n"(n) : "memory")
#define WAIT_L0() asm volatile("s_waitcnt lgkmcnt(0)" ::: "memory")
#define BAR() __builtin_amdgcn_s_barrier()

__device__ __forceinline__ void glds16(const void* sbase, unsigned voff, unsigned ldsaddr) {
  asm volatile("s_mov_b32 m0, %2\n\ts_nop 0\n\tglobal_load_lds_dwordx4 %0, %1" ::"v"(voff), "s"(sbase), "s"(ldsaddr) : "memory");
}
#define MFMA16(c, a, b) c = __builtin_amdgcn_mfma_f32_16x16x32_bf16(a, b, c, 0, 0, 0)
#define ST16(p, v) asm volatile("global_store_dwordx4 %0, %1, off sc0 sc1\n\ts_nop 1" ::"v"(p), "v"(v) : "memory")
#define SBAR() __builtin_amdgcn_sched_barrier(0)
#define SGB(mask, n) __builtin_amdgcn_sched_group_barrier(mask, n, 0)
template <int I> struct IC { static constexpr int value = I; };
template <int I, int N, class F> __device__ __forceinline__ void sfor(F&& f) {
  if constexpr (I < N) { f(IC<I>{}); sfor<I + 1, N>(f); }
}
__device__ __forceinline__ s16x4 tr_read(const char* p) {
  return __builtin_amdgcn_ds_read_tr16_b64_v4i16((LAS s16x4*)p);
}
__device__ __forceinline__ bf16x8 lds_read16(const char* p) { return *(const LAS bf16x8*)p; }

__device__ __forceinline__ void mod_block(const u16* __restrict__ c, const u16* __restrict__ wmod, float* __restrict__ modp,
                                          int cg, int ks, float* sh) {
  const int tid = threadIdx.x;
  const int cl = tid & 7, rg = tid >> 3;
  const int col0 = cg * 64 + cl * 8;
  u32x4 w[8];
#pragma unroll
  for (int i = 0; i < 8; ++i)
    w[i] = __builtin_nontemporal_load((const u32x4*)(wmod + (size_t)(ks * 256 + rg + i * 32) * 6144 + col0));
  {
    const float v = bf2f(c[ks * 256 + tid]);
    sh[tid] = rbf(v / (1.0f + __expf(-v)));
  }
  __syncthreads();
  float acc[8];
#pragma unroll
  for (int j = 0; j < 8; ++j) acc[j] = 0.f;
#pragma unroll
  for (int i = 0; i < 8; ++i) {
    const float s = sh[rg + i * 32];
#pragma unroll
    for (int j = 0; j < 4; ++j) {
      acc[2 * j] += s * lo_f(w[i][j]);
      acc[2 * j + 1] += s * hi_f(w[i][j]);
    }
  }
  float* red = sh + 256;
#pragma unroll
  for (int j = 0; j < 8; ++j) red[rg * 65 + cl * 8 + j] = acc[j];
  __syncthreads();
  if (tid < 64) {
    float s = 0.f;
#pragma unroll 8
    for (int r = 0; r < 32; ++r) s += red[r * 65 + tid];
    modp[ks * 6144 + cg * 64 + tid] = s;
  }
}
__device__ __forceinline__ void touch_block(const char* src) {
  u32x4 v[8];
#pragma unroll
  for (int i = 0; i < 8; ++i) v[i] = *(const u32x4*)(src + i * 4096 + threadIdx.x * 16);
#pragma unroll
  for (int i = 0; i < 8; ++i) asm volatile("" ::"v"(v[i]));
}
__device__ __forceinline__ f32x4 ld_mod4(const float* modp, int idx) {
  f32x4 a = *(const f32x4*)(modp + idx), b = *(const f32x4*)(modp + 6144 + idx);
  f32x4 c = *(const f32x4*)(modp + 2 * 6144 + idx), d = *(const f32x4*)(modp + 3 * 6144 + idx);
  f32x4 s = (a + b) + (c + d);
  return (f32x4){rbf(s[0]), rbf(s[1]), rbf(s[2]), rbf(s[3])};
}

__global__ __launch_bounds__(256) void mod_kernel(const u16* __restrict__ c, const u16* __restrict__ wmod,
                                                  float* __restrict__ modp, const u16* __restrict__ pf0) {
  __shared__ float sh[256 + 32 * 65];
  STAMP_BEGIN();
  const int b = blockIdx.x;
  if (b < 128) mod_block(c, wmod, modp, b >> 2, b & 3, sh);
  else touch_block((const char*)pf0 + (size_t)(b - 128) * 32768);
  STAMP_END(0);
}

__global__ __launch_bounds__(256) void ln_kernel(const u16* __restrict__ x, const float* modp, int moff, u16* __restrict__ h,
                                                 const u16* __restrict__ c, const u16* __restrict__ wmod, float* modp_w,
                                                 const u16* __restrict__ pf, int kid) {
  __shared__ float sh[256 + 32 * 65];
  STAMP_BEGIN();
  const int tid = threadIdx.x;
  if (blockIdx.x >= 256) {
    const int b = blockIdx.x - 256;
    if (b < 256) mod_block(c, wmod, modp_w, 32 + (b >> 2), b & 3, sh);
    else touch_block((const char*)pf + (size_t)(b - 256) * 32768);
    return;
  }
  const int lane = tid & 63, wid = tid >> 6;
  const int row = blockIdx.x * 8 + wid * 2;
  const u16* xr = x + (size_t)row * D_;
  u32x4 v[2][2];
#pragma unroll
  for (int r = 0; r < 2; ++r) {
    v[r][0] = *(const u32x4*)(xr + r * D_ + lane * 8);
    v[r][1] = *(const u32x4*)(xr + r * D_ + 512 + lane * 8);
  }
  {
    const f32x4 s0 = ld_mod4(modp, moff + tid * 4);
    const f32x4 s1 = ld_mod4(modp, moff + 1024 + tid * 4);
    *(f32x4*)(sh + tid * 4) = s0;
    *(f32x4*)(sh + 1024 + tid * 4) = (f32x4){rbf(1.0f + s1[0]), rbf(1.0f + s1[1]), rbf(1.0f + s1[2]), rbf(1.0f + s1[3])};
  }
  __syncthreads();
#pragma unroll
  for (int r = 0; r < 2; ++r) {
    float f[16];
#pragma unroll
    for (int j = 0; j < 4; ++j) {
      f[2 * j] = lo_f(v[r][0][j]); f[2 * j + 1] = hi_f(v[r][0][j]);
      f[8 + 2 * j] = lo_f(v[r][1][j]); f[8 + 2 * j + 1] = hi_f(v[r][1][j]);
    }
    float s = 0.f;
#pragma unroll
    for (int j = 0; j < 16; ++j) s += f[j];
#pragma unroll
    for (int o = 32; o >= 1; o >>= 1) s += __shfl_xor(s, o);
    const float mean = s * (1.0f / D_);
    float q = 0.f;
#pragma unroll
    for (int j = 0; j < 16; ++j) { float d = f[j] - mean; q += d * d; }
#pragma unroll
    for (int o = 32; o >= 1; o >>= 1) q += __shfl_xor(q, o);
    const float mb = rbf(mean);
    const float vb = rbf(q * (1.0f / D_));
    const float sd = rbf(sqrtf(rbf(vb + 1e-5f)));
    const float inv = 1.0f / sd;
    float o[16];
#pragma unroll
    for (int half = 0; half < 2; ++half) {
      const int c0 = half * 512 + lane * 8;
      const f32x4 sa0 = *(const f32x4*)(sh + c0), sa1 = *(const f32x4*)(sh + c0 + 4);
      const f32x4 ca0 = *(const f32x4*)(sh + 1024 + c0), ca1 = *(const f32x4*)(sh + 1024 + c0 + 4);
#pragma unroll
      for (int j = 0; j < 8; ++j) {
        const float shf = j < 4 ? sa0[j & 3] : sa1[j & 3];
        const float sc = j < 4 ? ca0[j & 3] : ca1[j & 3];
        float t = rbf(f[half * 8 + j] - mb);
        t = rbf(t * inv);
        t = rbf(t * sc);
        o[half * 8 + j] = t + shf;
      }
    }
    u16* hr = h + (size_t)(row + r) * D_;
    u32x4 w0, w1;
#pragma unroll
    for (int j = 0; j < 4; ++j) {
      w0[j] = pack2(o[2 * j], o[2 * j + 1]);
      w1[j] = pack2(o[8 + 2 * j], o[8 + 2 * j + 1]);
    }
    ST16(hr + lane * 8, w0);
    ST16(hr + 512 + lane * 8, w1);
  }
  STAMP_END(kid);
}

struct GemmP {
  const u16* A; const u16* B0; const u16* B1; const u16* res; const float* gate; u16* out;
};

#define NS0 3
#define NS1 4
#define NS3 4
#define LW0 1
#define LW1 1
#define LW3 1
template <int MODE> struct GCfg;
template <> struct GCfg<0> { static constexpr int BM = 128, BN = 192, WM = 2, WN = 2, NS = NS0, K = 1024, LDA = 1024, LDB = 3072, NTM = 16, NTN = 16, RM = 8, RN = 4, LW = LW0; };
template <> struct GCfg<1> { static constexpr int BM = 128, BN = 64, WM = 2, WN = 2, NS = NS1, K = 1024, LDA = 1024, LDB = 1024, NTM = 16, NTN = 16, RM = 4, RN = 8, LW = LW1; };
template <> struct GCfg<2> { static constexpr int BM = 256, BN = 256, WM = 4, WN = 2, NS = 2, K = 1024, LDA = 1024, LDB = 3584, NTM = 8, NTN = 28, RM = 4, RN = 7, LW = 0; };
template <> struct GCfg<3> { static constexpr int BM = 128, BN = 64, WM = 2, WN = 2, NS = NS3, K = 3584, LDA = 3584, LDB = 1024, NTM = 16, NTN = 16, RM = 4, RN = 8, LW = LW3; };

template <int MODE>
__global__ __launch_bounds__(GCfg<MODE>::WM* GCfg<MODE>::WN * 64 * (1 + GCfg<MODE>::LW), 2) void gemm_kernel(GemmP p) {
  using C = GCfg<MODE>;
  constexpr int BM = C::BM, BN = C::BN, WM = C::WM, WN = C::WN, NS = C::NS, NW = WM * WN;
  constexpr int TM = BM / WM, TN = BN / WN, MI = TM / 16, NI = TN / 16;
  constexpr int A_BYTES = BM * 128, B_BYTES = BN * 128, ST_BYTES = A_BYTES + B_BYTES;
  constexpr int GA = BM / 8 / NW, GB = BN / 8 / NW, G = GA + GB;
  constexpr int NT = C::K / 64;
  static_assert(BM % (8 * NW) == 0 && BN % (8 * NW) == 0, "stage split");
  static_assert(NT >= NS + 1, "K too small");
  extern __shared__ __attribute__((aligned(16))) char smem[];
  STAMP_BEGIN();

  const int tid = threadIdx.x, lane = tid & 63;
  constexpr bool LW = C::LW != 0;
  const int wid_all = __builtin_amdgcn_readfirstlane(tid >> 6);
  const int wid = wid_all % NW;
  const int wm = wid / WN, wn = wid % WN;
  int tm, tn;
  {
    const int x = blockIdx.x & 7, idx = blockIdx.x >> 3;
    constexpr int NRM = C::NTM / C::RM;
    const int rm = x % NRM, rn = x / NRM;
    tm = rm * C::RM + idx % C::RM;
    tn = rn * C::RN + idx / C::RM;
  }
  const int m0 = tm * BM;

  const int par = wid & 1;
  const unsigned a_voff = (unsigned)(((lane >> 3) * C::LDA + 8 * ((lane & 7) ^ ((4 * par + (lane >> 4)) & 7))) * 2);
  const unsigned b_voff = (unsigned)(((lane >> 3) * C::LDB + 8 * ((lane & 7) ^ ((((lane >> 4) & 1) | (par << 1)) << 1))) * 2);
  const char* Abase = (const char*)(p.A + (size_t)m0 * C::LDA);
  const unsigned lds0 = (unsigned)(size_t)(LAS char*)smem;

  auto issue1 = [&](auto gi, int t, int bufoff) {
    constexpr int g = decltype(gi)::value;
    if constexpr (g < GA) {
      const int i = wid + NW * g;
      const char* gp = Abase + ((size_t)(8 * i) * C::LDA + (size_t)t * 64) * 2;
      glds16(gp, a_voff, lds0 + bufoff + i * 1024);
    } else {
      const int i = wid + NW * (g - GA);
      const int pn = i >> 3, kr = 8 * (i & 7);
      const u16* bp;
      if constexpr (MODE == 2) bp = (pn < 2 ? p.B0 : p.B1) + tn * 128 + (pn & 1) * 64;
      else bp = p.B0 + tn * BN + pn * 64;
      const char* gp = (const char*)(bp + ((size_t)t * 64 + kr) * C::LDB);
      glds16(gp, b_voff, lds0 + bufoff + A_BYTES + i * 1024);
    }
  };

  if constexpr (LW) {
    if (wid_all >= NW) {
      sfor<0, NS>([&](auto sc_) { sfor<0, G>([&](auto gc) { issue1(gc, decltype(sc_)::value, decltype(sc_)::value * ST_BYTES); }); });
      WAIT_V((NS - 1) * G);
      BAR();
      int lb = 0;
      for (int t = 0; t < NT - NS; ++t) {
        WAIT_V((NS - 2) * G);
        BAR();
        sfor<0, G>([&](auto gc) { issue1(gc, t + NS, lb); });
        lb = (lb + ST_BYTES == NS * ST_BYTES) ? 0 : lb + ST_BYTES;
      }
      sfor<0, NS - 1>([&](auto rc) { WAIT_V((NS - 2 - decltype(rc)::value) * G); BAR(); });
      return;
    }
  }

  const int fr = lane & 15, fq = lane >> 4;
  u32x4 rv[4];
  f32x4 gv[NI];
  if constexpr (MODE == 1 || MODE == 3) {
#pragma unroll
    for (int i = 0; i < 4; ++i) rv[i] = *(const u32x4*)(p.res + (size_t)(m0 + i * 32 + (tid >> 3)) * D_ + tn * BN + (tid & 7) * 8);
#pragma unroll
    for (int ni = 0; ni < NI; ++ni) gv[ni] = ld_mod4(p.gate, tn * BN + wn * TN + ni * 16 + 4 * fq);
  }
  const int a_rd0 = (wm * TM + fr) * 128 + ((fq ^ (fr >> 1)) << 4);
  const int a_rd1 = a_rd0 ^ 64;
  int b_rd[(MODE == 2) ? 4 : NI];
  {
    const int q = (lane & 15) >> 2, pp = lane & 3;
    const int gl = ((q >> 1) & 1) | ((fq & 1) << 1);
#pragma unroll
    for (int ni = 0; ni < ((MODE == 2) ? 4 : NI); ++ni) {
      int pn, un;
      if constexpr (MODE == 2) { pn = wn; un = ni & 3; }
      else { const int j = wn * NI + ni; pn = j >> 2; un = j & 3; }
      b_rd[ni] = A_BYTES + pn * 8192 + (8 * fq + q) * 128 + ((un ^ gl) << 5) + pp * 8;
    }
  }

  f32x4 acc[MI][NI];
#pragma unroll
  for (int mi = 0; mi < MI; ++mi)
#pragma unroll
    for (int ni = 0; ni < NI; ++ni) acc[mi][ni] = (f32x4){0.f, 0.f, 0.f, 0.f};

  constexpr int NSP = (MODE == 2) ? 2 : 1;
  constexpr int NH = NI / NSP, NM = MI * NH, NPH = 2 * NSP;
  constexpr int GW = (NS >= 3) ? NPH : NPH / 2;
  constexpr int GPP = (G + GW - 1) / GW;
  bf16x8 a0[MI], a1[MI], b0[NH], b1[NH];
  s16x4 blo[NH];

  auto rd_b = [&](auto ri, bf16x8* BF, const char* sb, int ks, int hf) {
    constexpr int r = decltype(ri)::value, ni = r >> 1;
    const int bo = (MODE == 2) ? b_rd[ni] + hf * 16384 : b_rd[ni];
    if constexpr ((r & 1) == 0) blo[ni] = tr_read(sb + bo + ks * 4096);
    else BF[ni] = __builtin_shufflevector(blo[ni], tr_read(sb + bo + ks * 4096 + 512), 0, 1, 2, 3, 4, 5, 6, 7);
  };
  auto rd_a = [&](auto ri, bf16x8* AF, const char* sb, int ks) {
    constexpr int mi = decltype(ri)::value;
    AF[mi] = lds_read16(sb + (ks ? a_rd1 : a_rd0) + mi * 2048);
  };
  auto phase = [&](auto hfc, auto rdm, auto g0c, auto g1c, bf16x8* AF, bf16x8* BF, bf16x8* AN_, bf16x8* BN_,
                   const char* rsb, int rks, int rhf, int gt, int gbuf) {
    constexpr int hf = decltype(hfc)::value, RDM = decltype(rdm)::value;
    constexpr int g0 = decltype(g0c)::value, g1 = decltype(g1c)::value, NG = g1 - g0;
    constexpr int NR = RDM == 0 ? 0 : (RDM == 1 ? 2 * NH : 2 * NH + MI);
    sfor<0, NM>([&](auto ic) {
      constexpr int i = decltype(ic)::value, mi = i / NH, ni = i % NH;
      MFMA16(acc[mi][hf * NH + ni], BF[ni], AF[mi]);
      sfor<0, NR>([&](auto rc) {
        constexpr int r = decltype(rc)::value;
        if constexpr ((r * (NM - NM / 4)) / (NR > 0 ? NR : 1) == i) {
          if constexpr (r < 2 * NH) rd_b(rc, BN_, rsb, rks, rhf);
          else rd_a(IC<r - 2 * NH>{}, AN_, rsb, rks);
        }
      });
      sfor<0, NG>([&](auto gc) {
        constexpr int g = decltype(gc)::value;
        if constexpr ((g * NM) / (NG > 0 ? NG : 1) == i) issue1(IC<g0 + g>{}, gt, gbuf);
      });
      SBAR();
    });
  };
  constexpr auto gmin = [](int a, int b) { return a < b ? a : b; };
  int cur = 0, prv = 0;
  auto ktile = [&](auto vmc, auto restc, auto firstc, int t) {
    constexpr int VM = decltype(vmc)::value;
    constexpr bool REST = decltype(restc)::value && !LW, FIRST = decltype(firstc)::value && !LW;
    const int nxt = (cur + ST_BYTES == NS * ST_BYTES) ? 0 : cur + ST_BYTES;
    const char* sc = smem + cur;
    const char* sn = smem + nxt;
    constexpr int R1 = REST ? gmin(G, GPP) : 0, R2 = REST ? gmin(G, 2 * GPP) : 0, R3 = REST ? gmin(G, 3 * GPP) : 0, R4 = REST ? gmin(G, 4 * GPP) : 0;
    constexpr int F1 = FIRST ? gmin(G, GPP) : 0;
    if constexpr (NSP == 1) {
      phase(IC<0>{}, IC<2>{}, IC<R1>{}, IC<R2>{}, a0, b0, a1, b1, sc, 1, 0, t - 1 + NS, prv);
      if constexpr (VM >= 0) {
        if constexpr (!LW) WAIT_V(VM);
        WAIT_L0(); BAR();
        phase(IC<0>{}, IC<2>{}, IC<0>{}, IC<F1>{}, a1, b1, a0, b0, sn, 0, 0, t + NS, cur);
      } else {
        phase(IC<0>{}, IC<0>{}, IC<0>{}, IC<0>{}, a1, b1, a0, b0, sn, 0, 0, 0, 0);
      }
    } else {
      phase(IC<0>{}, IC<1>{}, IC<R1>{}, IC<R2>{}, a0, b0, a1, b1, sc, 0, 1, t - 1 + NS, prv);
      phase(IC<1>{}, IC<2>{}, IC<R2>{}, IC<R3>{}, a0, b1, a1, b0, sc, 1, 0, t - 1 + NS, prv);
      phase(IC<0>{}, IC<1>{}, IC<R3>{}, IC<R4>{}, a1, b0, a1, b1, sc, 1, 1, t - 1 + NS, prv);
      if constexpr (VM >= 0) {
        WAIT_V(VM); WAIT_L0(); BAR();
        phase(IC<1>{}, IC<2>{}, IC<0>{}, IC<F1>{}, a1, b1, a0, b0, sn, 0, 0, t + NS, cur);
      } else {
        phase(IC<1>{}, IC<0>{}, IC<0>{}, IC<0>{}, a1, b1, a0, b0, sn, 0, 0, 0, 0);
      }
    }
    prv = cur; cur = nxt;
  };

  if constexpr (!LW) {
    sfor<0, NS>([&](auto sc_) { sfor<0, G>([&](auto gc) { issue1(gc, decltype(sc_)::value, decltype(sc_)::value * ST_BYTES); }); });
    WAIT_V((NS - 1) * G);
  }
  BAR();
  STAMP_MID(MODE == 0 ? 2 : MODE == 1 ? 4 : MODE == 2 ? 6 : 7, 0);
  sfor<0, 2 * NH>([&](auto rc) { rd_b(rc, b0, smem, 0, 0); });
  sfor<0, MI>([&](auto rc) { rd_a(rc, a0, smem, 0); });
  using TT = IC<1>; using FF = IC<0>;
  ktile(IC<(NS - 2) * G>{}, FF{}, TT{}, 0);
  for (int t = 1; t < NT - NS; ++t) ktile(IC<(NS - 2) * G>{}, TT{}, TT{}, t);
  ktile(IC<(NS - 2) * G>{}, TT{}, FF{}, NT - NS);
  sfor<0, NS - 2>([&](auto rc) { ktile(IC<(NS - 3 - decltype(rc)::value) * G>{}, FF{}, FF{}, 0); });
  ktile(IC<-1>{}, FF{}, FF{}, 0);

  STAMP_MID(MODE == 0 ? 2 : MODE == 1 ? 4 : MODE == 2 ? 6 : 7, 1);
  if constexpr (MODE == 0) {
    const float qs = 0.17677669529663687f * 1.4426950408889634f;
    BAR();
    char* st = smem + wid * (64 * 208);
#pragma unroll
    for (int ni = 0; ni < NI; ++ni) {
      const int n = tn * BN + wn * TN + ni * 16;
      const float sc = n < 1024 ? qs : 1.0f;
#pragma unroll
      for (int mi = 0; mi < MI; ++mi) {
        u32x2 w;
        w[0] = pack2(acc[mi][ni][0] * sc, acc[mi][ni][1] * sc);
        w[1] = pack2(acc[mi][ni][2] * sc, acc[mi][ni][3] * sc);
        *(LAS u32x2*)(st + (mi * 16 + fr) * 208 + ni * 32 + fq * 8) = w;
      }
    }
#pragma unroll
    for (int j = 0; j < 3; ++j) {
      const int hh = (tn * BN + wn * TN + j * 32) >> 5;
      u16* ob = p.out + ((size_t)hh * S_ + m0 + wm * TM) * 32 + lane * 8;
#pragma unroll
      for (int i = 0; i < 4; ++i) {
        const u32x4 v = *(const LAS u32x4*)(st + (i * 16 + (lane >> 2)) * 208 + j * 64 + (lane & 3) * 16);
        ST16(ob + i * 512, v);
      }
    }
  } else if constexpr (MODE == 1 || MODE == 3) {
    BAR();
#pragma unroll
    for (int ni = 0; ni < NI; ++ni) {
#pragma unroll
      for (int mi = 0; mi < MI; ++mi) {
        u32x2 w;
        w[0] = pack2(gv[ni][0] * rbf(acc[mi][ni][0]), gv[ni][1] * rbf(acc[mi][ni][1]));
        w[1] = pack2(gv[ni][2] * rbf(acc[mi][ni][2]), gv[ni][3] * rbf(acc[mi][ni][3]));
        *(LAS u32x2*)(smem + (wm * TM + mi * 16 + fr) * 144 + wn * 64 + ni * 32 + fq * 8) = w;
      }
    }
    WAIT_L0();
    BAR();
#pragma unroll
    for (int i = 0; i < 4; ++i) {
      const u32x4 v = *(const LAS u32x4*)(smem + (i * 32 + (tid >> 3)) * 144 + (tid & 7) * 16);
      u32x4 w;
#pragma unroll
      for (int j = 0; j < 4; ++j) w[j] = pack2(lo_f(rv[i][j]) + lo_f(v[j]), hi_f(rv[i][j]) + hi_f(v[j]));
      ST16(p.out + (size_t)(m0 + i * 32 + (tid >> 3)) * D_ + tn * BN + (tid & 7) * 8, w);
    }
  } else {
    BAR();
    char* st = smem + wid * (64 * 144);
#pragma unroll
    for (int ni = 0; ni < 4; ++ni)
#pragma unroll
      for (int mi = 0; mi < MI; ++mi) {
        float o[4];
#pragma unroll
        for (int r = 0; r < 4; ++r) o[r] = rbf(acc[mi][ni][r]) * rbf(acc[mi][ni + 4][r]);
        u32x2 w;
        w[0] = pack2(o[0], o[1]);
        w[1] = pack2(o[2], o[3]);
        *(LAS u32x2*)(st + (mi * 16 + fr) * 144 + ni * 32 + fq * 8) = w;
      }
    u16* ob = p.out + (size_t)(m0 + wm * TM + (lane >> 3)) * F_ + tn * 128 + wn * 64 + (lane & 7) * 8;
#pragma unroll
    for (int i = 0; i < 8; ++i) {
      const u32x4 v = *(const LAS u32x4*)(st + (i * 8 + (lane >> 3)) * 144 + (lane & 7) * 16);
      ST16(ob + (size_t)i * 8 * F_, v);
    }
  }
  STAMP_END(MODE == 0 ? 2 : MODE == 1 ? 4 : MODE == 2 ? 6 : 7);
}

#define AT_NSTAGE 6
constexpr int AT_KT = 128, AT_NS = AT_NSTAGE, AT_ST = 16384;
#define MFMA32(a, b, c) __builtin_amdgcn_mfma_f32_32x32x16_bf16(a, b, c, 0, 0, 0)
#define AEXP(x) __builtin_amdgcn_exp2f(x)
#define MX3(a, b, c) __builtin_fmaxf(__builtin_fmaxf((a), (b)), (c))

__device__ __forceinline__ float tile_rowmax(const f32x16& s0, const f32x16& s1) {
  float a = MX3(s0[0], s0[1], s1[0]), b = MX3(s0[2], s0[3], s1[1]);
  a = MX3(a, s1[2], s1[3]);
#pragma unroll
  for (int r = 4; r < 16; r += 4) {
    a = MX3(a, s0[r], s0[r + 1]); b = MX3(b, s0[r + 2], s0[r + 3]);
    a = MX3(a, s1[r], s1[r + 1]); b = MX3(b, s1[r + 2], s1[r + 3]);
  }
  float mx = __builtin_fmaxf(a, b);
  auto rr = __builtin_amdgcn_permlane32_swap(__float_as_uint(mx), __float_as_uint(mx), false, false);
  return __builtin_fmaxf(__uint_as_float(rr[0]), __uint_as_float(rr[1]));
}

__global__ __launch_bounds__(512, 2) void attn_kernel(const u16* __restrict__ qkv, u16* __restrict__ o) {
  extern __shared__ __attribute__((aligned(16))) char smem[];
  STAMP_BEGIN();
  const int tid = threadIdx.x, lane = tid & 63;
  const int wid = __builtin_amdgcn_readfirstlane(tid >> 6);
  const int x = blockIdx.x & 7, idx = blockIdx.x >> 3;
  const int hd = x * 4 + (idx >> 3), qb = idx & 7;
  const int r32 = lane & 31, hi = lane >> 5;
  const u16* Q = qkv + ((size_t)(0 * 32 + hd) * S_) * 32;
  const u16* Kp = qkv + ((size_t)(1 * 32 + hd) * S_) * 32;
  const u16* Vp = qkv + ((size_t)(2 * 32 + hd) * S_) * 32;
  const int q0 = qb * 256 + wid * 32;

  const unsigned k_voff = (unsigned)(((lane >> 2) * 32 + 8 * ((lane & 3) ^ ((lane >> 4) & 3))) * 2);
  const unsigned v_voff = (unsigned)(lane * 16);
  const unsigned lds0 = (unsigned)(size_t)(LAS char*)smem;
  auto issue = [&](int t, int bufoff) {
    const char* gk = (const char*)(Kp + ((size_t)t * AT_KT + wid * 16) * 32);
    const char* gv = (const char*)(Vp + ((size_t)t * AT_KT + wid * 16) * 32);
    glds16(gk, k_voff, lds0 + bufoff + wid * 1024);
    glds16(gv, v_voff, lds0 + bufoff + 8192 + wid * 1024);
  };
#pragma unroll
  for (int s = 0; s < AT_NS - 1; ++s) issue(s, s * AT_ST);
  const bf16x8 qf0 = *(const bf16x8*)(Q + (size_t)(q0 + r32) * 32 + hi * 8);
  const bf16x8 qf1 = *(const bf16x8*)(Q + (size_t)(q0 + r32) * 32 + 16 + hi * 8);

  const int k_rd0 = r32 * 64 + ((hi ^ ((r32 >> 2) & 3)) << 4);
  const int k_rd1 = k_rd0 ^ 32;
  const int v_rd = 8192 + (4 * hi + ((lane & 15) >> 2)) * 64 + ((lane >> 4) & 1) * 32 + (lane & 3) * 8;

  f32x16 oacc = {};
  f32x16 negm;
  float m, l = 0.f;
  f32x16 sa0, sa1, sb0, sb1;
  u32x4 pp0 = {}, pp1 = {}, pp2 = {}, pp3 = {};
  bf16x8 vf0, vf1, vf2, vf3;
#define AT_BIG 1.0e30f
  constexpr float BIG = AT_BIG;
  constexpr int NT = S_ / AT_KT;

#define SCORES(D0, D1, kb, CIN)                                                         \
  do {                                                                                  \
    bf16x8 k00 = lds_read16((kb) + k_rd0), k01 = lds_read16((kb) + k_rd1);              \
    bf16x8 k10 = lds_read16((kb) + 2048 + k_rd0), k11 = lds_read16((kb) + 2048 + k_rd1);\
    D0 = MFMA32(k00, qf0, CIN); D1 = MFMA32(k10, qf0, CIN);                             \
    D0 = MFMA32(k01, qf1, D0);  D1 = MFMA32(k11, qf1, D1);                              \
  } while (0)
#define VFRAG(vb, s) __builtin_shufflevector(tr_read((vb) + (s) * 1024), tr_read((vb) + (s) * 1024 + 512), 0, 1, 2, 3, 4, 5, 6, 7)
#define READV(vb) do { vf0 = VFRAG(vb, 0); vf1 = VFRAG(vb, 1); vf2 = VFRAG(vb, 2); vf3 = VFRAG(vb, 3); } while (0)
#define PV()                                                                            \
  do {                                                                                  \
    oacc = MFMA32(vf0, __builtin_bit_cast(bf16x8, pp0), oacc);                          \
    oacc = MFMA32(vf1, __builtin_bit_cast(bf16x8, pp1), oacc);                          \
    oacc = MFMA32(vf2, __builtin_bit_cast(bf16x8, pp2), oacc);                          \
    oacc = MFMA32(vf3, __builtin_bit_cast(bf16x8, pp3), oacc);                          \
  } while (0)
#define EXPSUM(C0, C1, LS)                                                              \
  do {                                                                                  \
    f32x2 ls2_ = {0.f, 0.f}, ls3_ = {0.f, 0.f};                                         \
    _Pragma("unroll") for (int r = 0; r < 16; r += 2) {                                 \
      C0[r] = AEXP(C0[r]); C0[r + 1] = AEXP(C0[r + 1]);                                 \
      C1[r] = AEXP(C1[r]); C1[r + 1] = AEXP(C1[r + 1]);                                 \
      ls2_ += (f32x2){C0[r], C0[r + 1]};                                                \
      ls3_ += (f32x2){C1[r], C1[r + 1]};                                                \
    }                                                                                   \
    ls2_ += ls3_;                                                                       \
    LS = ls2_[0] + ls2_[1];                                                             \
  } while (0)
#define PACK(C0, C1)                                                                    \
  do {                                                                                  \
    _Pragma("unroll") for (int j = 0; j < 4; ++j) {                                     \
      pp0[j] = pack2(C0[2 * j], C0[2 * j + 1]);                                         \
      pp1[j] = pack2(C0[8 + 2 * j], C0[8 + 2 * j + 1]);                                 \
      pp2[j] = pack2(C1[2 * j], C1[2 * j + 1]);                                         \
      pp3[j] = pack2(C1[8 + 2 * j], C1[8 + 2 * j + 1]);                                 \
    }                                                                                   \
  } while (0)
#define STEP(C0, C1, N0, N1, kcur, knext, vprev, HASN)                                  \
  do {                                                                                  \
    READV(vprev);                                                                       \
    if (HASN) SCORES(N0, N1, knext, negm);                                              \
    PV();                                                                               \
    float ls;                                                                           \
    EXPSUM(C0, C1, ls);                                                                 \
    if (__builtin_expect(__any(!(ls <= BIG)), 0)) {                                     \
                  \
      f32x16 z_ = {};                                                                   \
      SCORES(C0, C1, kcur, z_);                                                         \
      const float mn = __builtin_fmaxf(m, tile_rowmax(C0, C1));                         \
      const float dm = m - mn;                                                          \
      const float f = __builtin_amdgcn_exp2f(dm);                                       \
      m = mn; l *= f;                                                                   \
      _Pragma("unroll") for (int r = 0; r < 16; ++r) {                                  \
        oacc[r] *= f; negm[r] = -mn; C0[r] -= mn; C1[r] -= mn;                          \
        if (HASN) { N0[r] += dm; N1[r] += dm; }                                         \
      }                                                                                 \
      EXPSUM(C0, C1, ls);                                                               \
    }                                                                                   \
    l += ls;                                                                            \
    PACK(C0, C1);                                                                       \
  } while (0)

  WAIT_V(2 * (AT_NS - 2));
  BAR();
  {
    f32x16 z = {};
    SCORES(sa0, sa1, smem, z);
    m = tile_rowmax(sa0, sa1);
#pragma unroll
    for (int r = 0; r < 16; ++r) { negm[r] = -m; sa0[r] -= m; sa1[r] -= m; }
  }
  int cur = 0, prv = 0;
  for (int t = 0; t < NT - 1; ++t) {
    const char* sb = smem + cur;
    int nx = cur + AT_ST; if (nx >= AT_NS * AT_ST) nx = 0;
    int prv2 = cur - AT_ST; if (prv2 < 0) prv2 = (AT_NS - 1) * AT_ST;
    STEP(sa0, sa1, sb0, sb1, sb, sb + 4096, smem + prv + 4096 + v_rd, true);
    if (t + AT_NS - 1 < NT) {
      WAIT_V(2 * (AT_NS - 3));
      WAIT_L0();
      BAR();
      issue(t + AT_NS - 1, prv2);
    } else {
      WAIT_V(0);
      WAIT_L0();
      BAR();
    }
    STEP(sb0, sb1, sa0, sa1, sb + 4096, smem + nx, sb + v_rd, true);
    prv = cur; cur = nx;
  }
  {
    const char* sb = smem + cur;
    STEP(sa0, sa1, sb0, sb1, sb, sb + 4096, smem + prv + 4096 + v_rd, true);
    STEP(sb0, sb1, sa0, sa1, sb + 4096, sb, sb + v_rd, false);
    prv = cur;
  }
  READV(smem + prv + 4096 + v_rd);
  PV();
#undef SCORES
#undef VFRAG
#undef READV
#undef PV
#undef EXPSUM
#undef PACK
#undef STEP
  {
    auto rr = __builtin_amdgcn_permlane32_swap(__float_as_uint(l), __float_as_uint(l), false, false);
    l = __uint_as_float(rr[0]) + __uint_as_float(rr[1]);
  }
  const float inv = 1.0f / l;
  u16* op = o + (size_t)(q0 + r32) * D_ + hd * 32 + 4 * hi;
#pragma unroll
  for (int g = 0; g < 4; ++g) {
    u32x2 w;
    w[0] = pack2(oacc[4 * g] * inv, oacc[4 * g + 1] * inv);
    w[1] = pack2(oacc[4 * g + 2] * inv, oacc[4 * g + 3] * inv);
    *(u32x2*)(op + 8 * g) = w;
  }
  STAMP_END(3);
}

constexpr int A2_KT = 256, A2_NS = 3, A2_ST = 32768;
template <bool SAFE>
__device__ __forceinline__ void attn_pass(char* smem, const u16* Kp, const u16* Vp, bf16x8 qf0, bf16x8 qf1, int wid, int lane,
                                          f32x16& oacc, float& m, float& l,
                                          const u16* pw0, const u16* pw1, const u16* pw2, const u16* pw3) {
  const int wk = wid >> 3, r32 = lane & 31, hi = lane >> 5;
  const unsigned k_voff = (unsigned)(((lane >> 2) * 32 + 8 * ((lane & 3) ^ ((lane >> 4) & 3))) * 2);
  const unsigned v_voff = (unsigned)(lane * 16);
  const unsigned lds0 = (unsigned)(size_t)(LAS char*)smem;
  auto issue = [&](int t, int bufoff) {
    const char* gk = (const char*)(Kp + ((size_t)t * A2_KT + wid * 16) * 32);
    const char* gv = (const char*)(Vp + ((size_t)t * A2_KT + wid * 16) * 32);
    glds16(gk, k_voff, lds0 + bufoff + wid * 1024);
    glds16(gv, v_voff, lds0 + bufoff + 16384 + wid * 1024);
  };
  issue(0, 0);
  issue(1, A2_ST);
  const int k_rd0 = wk * 8192 + r32 * 64 + ((hi ^ ((r32 >> 2) & 3)) << 4);
  const int k_rd1 = k_rd0 ^ 32;
  const int v_rd = 16384 + wk * 8192 + (4 * hi + ((lane & 15) >> 2)) * 64 + ((lane >> 4) & 1) * 32 + (lane & 3) * 8;
  f32x16 negm = {};
  f32x16 lacc = {};
  const bf16x8 ones = {0x3f80, 0x3f80, 0x3f80, 0x3f80, 0x3f80, 0x3f80, 0x3f80, 0x3f80};
  oacc = (f32x16){};
  m = 0.f; l = 0.f;
  constexpr int NT = S_ / A2_KT;
  int cur = 0;
  for (int t = 0; t < NT; ++t) {
    if (t == 0) WAIT_V(2); else if (t + 1 < NT) WAIT_V(3); else WAIT_V(0);
    BAR();
    {
      int ck = (blockIdx.x * NT + t) * 16 + wid;
      if (ck >= 2048 + 3 * 7168) ck -= 16384;
      const u16* pb = pw0;
      if (ck >= 2048) { pb = pw1; ck -= 2048; if (ck >= 7168) { pb = pw2; ck -= 7168; if (ck >= 7168) { pb = pw3; ck -= 7168; } } }
      glds16((const char*)pb + (size_t)ck * 1024, v_voff, lds0 + A2_NS * A2_ST);
      int nb = cur + 2 * A2_ST; if (nb >= A2_NS * A2_ST) nb -= A2_NS * A2_ST;
      if (t + 2 < NT) issue(t + 2, nb);
    }
#pragma unroll
    for (int hb = 0; hb < 2; ++hb) {
      const char* kb = smem + cur + hb * 4096;
      const bf16x8 k00 = lds_read16(kb + k_rd0), k01 = lds_read16(kb + k_rd1);
      const bf16x8 k10 = lds_read16(kb + 2048 + k_rd0), k11 = lds_read16(kb + 2048 + k_rd1);
      f32x16 s0, s1;
      if (SAFE || (t == 0 && hb == 0)) {
        f32x16 z_ = {};
        s0 = MFMA32(k00, qf0, z_); s1 = MFMA32(k10, qf0, z_);
        s0 = MFMA32(k01, qf1, s0); s1 = MFMA32(k11, qf1, s1);
        const float tm = tile_rowmax(s0, s1);
        const bool first = (t == 0 && hb == 0);
        const float mn = first ? tm : __builtin_fmaxf(m, tm);
        if (SAFE) {
          const float f = first ? 0.f : __builtin_amdgcn_exp2f(m - mn);
          lacc *= f;
          oacc *= f;
        }
        m = mn;
#pragma unroll
        for (int r = 0; r < 16; ++r) { negm[r] = -mn; s0[r] -= mn; s1[r] -= mn; }
      } else {
        s0 = MFMA32(k00, qf0, negm); s1 = MFMA32(k10, qf0, negm);
        s0 = MFMA32(k01, qf1, s0); s1 = MFMA32(k11, qf1, s1);
      }
#pragma unroll
      for (int r = 0; r < 16; r += 2) {
        s0[r] = AEXP(s0[r]); s0[r + 1] = AEXP(s0[r + 1]);
        s1[r] = AEXP(s1[r]); s1[r + 1] = AEXP(s1[r + 1]);
      }
      u32x4 p0, p1, p2, p3;
#pragma unroll
      for (int j = 0; j < 4; ++j) {
        p0[j] = pack2(s0[2 * j], s0[2 * j + 1]);
        p1[j] = pack2(s0[8 + 2 * j], s0[8 + 2 * j + 1]);
        p2[j] = pack2(s1[2 * j], s1[2 * j + 1]);
        p3[j] = pack2(s1[8 + 2 * j], s1[8 + 2 * j + 1]);
      }
      const char* vb = smem + cur + hb * 4096 + v_rd;
#define VFRAG(s) __builtin_shufflevector(tr_read(vb + (s) * 1024), tr_read(vb + (s) * 1024 + 512), 0, 1, 2, 3, 4, 5, 6, 7)
      oacc = MFMA32(VFRAG(0), __builtin_bit_cast(bf16x8, p0), oacc);
      oacc = MFMA32(VFRAG(1), __builtin_bit_cast(bf16x8, p1), oacc);
      oacc = MFMA32(VFRAG(2), __builtin_bit_cast(bf16x8, p2), oacc);
      oacc = MFMA32(VFRAG(3), __builtin_bit_cast(bf16x8, p3), oacc);
#undef VFRAG
      lacc = MFMA32(ones, __builtin_bit_cast(bf16x8, p0), lacc);
      lacc = MFMA32(ones, __builtin_bit_cast(bf16x8, p1), lacc);
      lacc = MFMA32(ones, __builtin_bit_cast(bf16x8, p2), lacc);
      lacc = MFMA32(ones, __builtin_bit_cast(bf16x8, p3), lacc);
    }
    WAIT_L0();
    cur += A2_ST; if (cur >= A2_NS * A2_ST) cur = 0;
  }
  l = lacc[0];
  WAIT_V(0);
}

__global__ __launch_bounds__(1024, 4) void attn2_kernel(const u16* __restrict__ qkv, u16* __restrict__ o,
                                                        const u16* pw0, const u16* pw1, const u16* pw2, const u16* pw3) {
  extern __shared__ __attribute__((aligned(16))) char smem[];
  STAMP_BEGIN();
  const int tid = threadIdx.x, lane = tid & 63;
  const int wid = __builtin_amdgcn_readfirstlane(tid >> 6);
  const int wq = wid & 7, wk = wid >> 3;
  const int x = blockIdx.x & 7, idx = blockIdx.x >> 3;
  const int hd = x * 4 + (idx >> 3), qb = idx & 7;
  const int r32 = lane & 31, hi = lane >> 5;
  const u16* Q = qkv + ((size_t)(0 * 32 + hd) * S_) * 32;
  const u16* Kp = qkv + ((size_t)(1 * 32 + hd) * S_) * 32;
  const u16* Vp = qkv + ((size_t)(2 * 32 + hd) * S_) * 32;
  const int q0 = qb * 256 + wq * 32;
  const bf16x8 qf0 = *(const bf16x8*)(Q + (size_t)(q0 + r32) * 32 + hi * 8);
  const bf16x8 qf1 = *(const bf16x8*)(Q + (size_t)(q0 + r32) * 32 + 16 + hi * 8);

  f32x16 oacc;
  float m, l;
  attn_pass<false>(smem, Kp, Vp, qf0, qf1, wid, lane, oacc, m, l, pw0, pw1, pw2, pw3);
  {
    float chk = l;
#pragma unroll
    for (int r = 0; r < 16; ++r) chk += __builtin_fabsf(oacc[r]);
    const int bad = !(chk < 1.0e37f);
    if (__syncthreads_or(bad)) attn_pass<true>(smem, Kp, Vp, qf0, qf1, wid, lane, oacc, m, l, pw0, pw1, pw2, pw3);
  }
  __syncthreads();
  float* xb = (float*)smem + wq * (18 * 64);
  if (wk == 1) {
    xb[lane] = m; xb[64 + lane] = l;
#pragma unroll
    for (int r = 0; r < 16; ++r) xb[(2 + r) * 64 + lane] = oacc[r];
  }
  __syncthreads();
  if (wk == 0) {
    const float m2 = xb[lane], l2 = xb[64 + lane];
    const float mn = __builtin_fmaxf(m, m2);
    const float e1 = (m - mn) * 0.5f, e2 = (m2 - mn) * 0.5f;
    const float f1 = __builtin_amdgcn_exp2f(e1), f2 = __builtin_amdgcn_exp2f(e2);
    const float lt = (l * f1) * f1 + (l2 * f2) * f2;
    const float inv = 1.0f / lt;
    u16* op = o + (size_t)(q0 + r32) * D_ + hd * 32 + 4 * hi;
#pragma unroll
    for (int g = 0; g < 4; ++g) {
      float v[4];
#pragma unroll
      for (int e = 0; e < 4; ++e) v[e] = ((oacc[4 * g + e] * f1) * f1 + (xb[(2 + 4 * g + e) * 64 + lane] * f2) * f2) * inv;
      u32x2 w;
      w[0] = pack2(v[0], v[1]);
      w[1] = pack2(v[2], v[3]);
      *(u32x2*)(op + 8 * g) = w;
    }
  }
  STAMP_END(3);
}

template <int MODE> static void launch_gemm(const GemmP& p, hipStream_t stream) {
  using C = GCfg<MODE>;
  constexpr int lds = C::NS * (C::BM + C::BN) * 128;
  hipFuncSetAttribute((const void*)gemm_kernel<MODE>, hipFuncAttributeMaxDynamicSharedMemorySize, lds);
  gemm_kernel<MODE><<<C::NTM * C::NTN, C::WM * C::WN * 64 * (1 + C::LW), lds, stream>>>(p);
}

extern "C" void kernel_launch(void* const* d_in, const int* in_sizes, int n_in, void* d_out, int out_size,
                              void* d_ws, size_t ws_size, hipStream_t stream) {
  const u16* x = (const u16*)d_in[0];
  const u16* wqkv = (const u16*)d_in[1];
  const u16* wo = (const u16*)d_in[2];
  const u16* wg = (const u16*)d_in[4];
  const u16* wu = (const u16*)d_in[5];
  const u16* wd = (const u16*)d_in[6];
  const u16* c = (const u16*)d_in[7];
  const u16* wmod = (const u16*)d_in[8];
  u16* out = (u16*)d_out;

  char* ws = (char*)d_ws;
  float* mod = (float*)ws;
  u16* h = (u16*)(ws + (1 << 20));
  u16* qkv = (u16*)(ws + (8 << 20));
  u16* ao = (u16*)(ws + (24 << 20));
  u16* x1 = (u16*)(ws + (32 << 20));
  u16* hh = (u16*)(ws + (40 << 20));

  mod_kernel<<<256, 256, 0, stream>>>(c, wmod, mod, x);
  ln_kernel<<<256 + 256 + 192, 256, 0, stream>>>(x, mod, 0, h, c, wmod, mod, wqkv, 1);
  { GemmP p{h, wqkv, nullptr, nullptr, nullptr, qkv}; launch_gemm<0>(p, stream); }
  hipFuncSetAttribute((const void*)attn2_kernel, hipFuncAttributeMaxDynamicSharedMemorySize, A2_NS * A2_ST + 1024);
  attn2_kernel<<<256, 1024, A2_NS * A2_ST + 1024, stream>>>(qkv, ao, wo, wg, wu, wd);
  { GemmP p{ao, wo, nullptr, x, mod + 2 * 1024, x1}; launch_gemm<1>(p, stream); }
  ln_kernel<<<256, 256, 0, stream>>>(x1, mod, 3 * 1024, h, c, wmod, mod, wqkv, 5);
  { GemmP p{h, wg, wu, nullptr, nullptr, hh}; launch_gemm<2>(p, stream); }
  { GemmP p{hh, wd, nullptr, x1, mod + 5 * 1024, out}; launch_gemm<3>(p, stream); }
}
```

```cpp
#define NO_MIMIC
#include <hip/hip_runtime.h>
#include <hip/hip_bf16.h>
#include <stdint.h>

typedef unsigned short u16;
typedef short bf16x8 __attribute__((ext_vector_type(8)));
typedef short s16x4 __attribute__((ext_vector_type(4)));
typedef float f32x2 __attribute__((ext_vector_type(2)));
typedef float f32x4 __attribute__((ext_vector_type(4)));
typedef float f32x16 __attribute__((ext_vector_type(16)));
typedef unsigned u32x2 __attribute__((ext_vector_type(2)));
typedef unsigned u32x4 __attribute__((ext_vector_type(4)));
typedef __bf16 bf16x2_t __attribute__((ext_vector_type(2)));
#define LAS __attribute__((address_space(3)))
#define GAS __attribute__((address_space(1)))

constexpr int S_ = 2048, D_ = 1024, H_ = 32, DH_ = 32, F_ = 3584;

__device__ __forceinline__ float bf2f(u16 u) { return __uint_as_float((unsigned)u << 16); }
__device__ __forceinline__ unsigned pack2(float lo, float hi) {
  f32x2 v = {lo, hi};
  bf16x2_t b = __builtin_convertvector(v, bf16x2_t);
  return __builtin_bit_cast(unsigned, b);
}
__device__ __forceinline__ float rbf(float f) {
  return f;
  __bf16 b = (__bf16)f;
  return __uint_as_float((unsigned)__builtin_bit_cast(u16, b) << 16);
}
__device__ __forceinline__ float lo_f(unsigned u) { return __uint_as_float(u << 16); }
__device__ __forceinline__ float hi_f(unsigned u) { return __uint_as_float(u & 0xffff0000u); }

#define STAMP_BEGIN()
#define STAMP_END(kid)
#define STAMP_MID(kid, i)
#define WAIT_V(n) asm volatile("s_waitcnt vmcnt(%0)" ::"n"(n) : "memory")
#define WAIT_L0() asm volatile("s_waitcnt lgkmcnt(0)" ::: "memory")
#define BAR() __builtin_amdgcn_s_barrier()

__device__ __forceinline__ void glds16(const void* sbase, unsigned voff, unsigned ldsaddr) {
  asm volatile("s_mov_b32 m0, %2\n\ts_nop 0\n\tglobal_load_lds_dwordx4 %0, %1" ::"v"(voff), "s"(sbase), "s"(ldsaddr) : "memory");
}
#define MFMA16(c, a, b) c = __builtin_amdgcn_mfma_f32_16x16x32_bf16(a, b, c, 0, 0, 0)
#define ST16(p, v) asm volatile("global_store_dwordx4 %0, %1, off sc0 sc1\n\ts_nop 1" ::"v"(p), "v"(v) : "memory")
#define SBAR() __builtin_amdgcn_sched_barrier(0)
#define SGB(mask, n) __builtin_amdgcn_sched_group_barrier(mask, n, 0)
template <int I> struct IC { static constexpr int value = I; };
template <int I, int N, class F> __device__ __forceinline__ void sfor(F&& f) {
  if constexpr (I < N) { f(IC<I>{}); sfor<I + 1, N>(f); }
}
__device__ __forceinline__ s16x4 tr_read(const char* p) {
  return __builtin_amdgcn_ds_read_tr16_b64_v4i16((LAS s16x4*)p);
}
__device__ __forceinline__ bf16x8 lds_read16(const char* p) { return *(const LAS bf16x8*)p; }

__device__ __forceinline__ void mod_block(const u16* __restrict__ c, const u16* __restrict__ wmod, float* __restrict__ modp,
                                          int cg, int ks, float* sh) {
  const int tid = threadIdx.x;
  const int cl = tid & 7, rg = tid >> 3;
  const int col0 = cg * 64 + cl * 8;
  u32x4 w[8];
#pragma unroll
  for (int i = 0; i < 8; ++i)
    w[i] = __builtin_nontemporal_load((const u32x4*)(wmod + (size_t)(ks * 256 + rg + i * 32) * 6144 + col0));
  {
    const float v = bf2f(c[ks * 256 + tid]);
    sh[tid] = rbf(v / (1.0f + __expf(-v)));
  }
  __syncthreads();
  float acc[8];
#pragma unroll
  for (int j = 0; j < 8; ++j) acc[j] = 0.f;
#pragma unroll
  for (int i = 0; i < 8; ++i) {
    const float s = sh[rg + i * 32];
#pragma unroll
    for (int j = 0; j < 4; ++j) {
      acc[2 * j] += s * lo_f(w[i][j]);
      acc[2 * j + 1] += s * hi_f(w[i][j]);
    }
  }
  float* red = sh + 256;
#pragma unroll
  for (int j = 0; j < 8; ++j) red[rg * 65 + cl * 8 + j] = acc[j];
  __syncthreads();
  if (tid < 64) {
    float s = 0.f;
#pragma unroll 8
    for (int r = 0; r < 32; ++r) s += red[r * 65 + tid];
    modp[ks * 6144 + cg * 64 + tid] = s;
  }
}
__device__ __forceinline__ void touch_block(const char* src) {
  u32x4 v[8];
#pragma unroll
  for (int i = 0; i < 8; ++i) v[i] = *(const u32x4*)(src + i * 4096 + threadIdx.x * 16);
#pragma unroll
  for (int i = 0; i < 8; ++i) asm volatile("" ::"v"(v[i]));
}
__device__ __forceinline__ f32x4 ld_mod4(const float* modp, int idx) {
  f32x4 a = *(const f32x4*)(modp + idx), b = *(const f32x4*)(modp + 6144 + idx);
  f32x4 c = *(const f32x4*)(modp + 2 * 6144 + idx), d = *(const f32x4*)(modp + 3 * 6144 + idx);
  f32x4 s = (a + b) + (c + d);
  return (f32x4){rbf(s[0]), rbf(s[1]), rbf(s[2]), rbf(s[3])};
}

__global__ __launch_bounds__(256) void mod_kernel(const u16* __restrict__ c, const u16* __restrict__ wmod,
                                                  float* __restrict__ modp, const u16* __restrict__ pf0) {
  __shared__ float sh[256 + 32 * 65];
  STAMP_BEGIN();
  const int b = blockIdx.x;
  if (b < 128) mod_block(c, wmod, modp, b >> 2, b & 3, sh);
  else touch_block((const char*)pf0 + (size_t)(b - 128) * 32768);
  STAMP_END(0);
}

__global__ __launch_bounds__(256) void ln_kernel(const u16* __restrict__ x, const float* modp, int moff, u16* __restrict__ h,
                                                 const u16* __restrict__ c, const u16* __restrict__ wmod, float* modp_w,
                                                 const u16* __restrict__ pf, int kid) {
  __shared__ float sh[256 + 32 * 65];
  STAMP_BEGIN();
  const int tid = threadIdx.x;
  if (blockIdx.x >= 256) {
    const int b = blockIdx.x - 256;
    if (b < 256) mod_block(c, wmod, modp_w, 32 + (b >> 2), b & 3, sh);
    else touch_block((const char*)pf + (size_t)(b - 256) * 32768);
    return;
  }
  const int lane = tid & 63, wid = tid >> 6;
  const int row = blockIdx.x * 8 + wid * 2;
  const u16* xr = x + (size_t)row * D_;
  u32x4 v[2][2];
#pragma unroll
  for (int r = 0; r < 2; ++r) {
    v[r][0] = *(const u32x4*)(xr + r * D_ + lane * 8);
    v[r][1] = *(const u32x4*)(xr + r * D_ + 512 + lane * 8);
  }
  {
    const f32x4 s0 = ld_mod4(modp, moff + tid * 4);
    const f32x4 s1 = ld_mod4(modp, moff + 1024 + tid * 4);
    *(f32x4*)(sh + tid * 4) = s0;
    *(f32x4*)(sh + 1024 + tid * 4) = (f32x4){rbf(1.0f + s1[0]), rbf(1.0f + s1[1]), rbf(1.0f + s1[2]), rbf(1.0f + s1[3])};
  }
  __syncthreads();
#pragma unroll
  for (int r = 0; r < 2; ++r) {
    float f[16];
#pragma unroll
    for (int j = 0; j < 4; ++j) {
      f[2 * j] = lo_f(v[r][0][j]); f[2 * j + 1] = hi_f(v[r][0][j]);
      f[8 + 2 * j] = lo_f(v[r][1][j]); f[8 + 2 * j + 1] = hi_f(v[r][1][j]);
    }
    float s = 0.f;
#pragma unroll
    for (int j = 0; j < 16; ++j) s += f[j];
#pragma unroll
    for (int o = 32; o >= 1; o >>= 1) s += __shfl_xor(s, o);
    const float mean = s * (1.0f / D_);
    float q = 0.f;
#pragma unroll
    for (int j = 0; j < 16; ++j) { float d = f[j] - mean; q += d * d; }
#pragma unroll
    for (int o = 32; o >= 1; o >>= 1) q += __shfl_xor(q, o);
    const float mb = rbf(mean);
    const float vb = rbf(q * (1.0f / D_));
    const float sd = rbf(sqrtf(rbf(vb + 1e-5f)));
    const float inv = 1.0f / sd;
    float o[16];
#pragma unroll
    for (int half = 0; half < 2; ++half) {
      const int c0 = half * 512 + lane * 8;
      const f32x4 sa0 = *(const f32x4*)(sh + c0), sa1 = *(const f32x4*)(sh + c0 + 4);
      const f32x4 ca0 = *(const f32x4*)(sh + 1024 + c0), ca1 = *(const f32x4*)(sh + 1024 + c0 + 4);
#pragma unroll
      for (int j = 0; j < 8; ++j) {
        const float shf = j < 4 ? sa0[j & 3] : sa1[j & 3];
        const float sc = j < 4 ? ca0[j & 3] : ca1[j & 3];
        float t = rbf(f[half * 8 + j] - mb);
        t = rbf(t * inv);
        t = rbf(t * sc);
        o[half * 8 + j] = t + shf;
      }
    }
    u16* hr = h + (size_t)(row + r) * D_;
    u32x4 w0, w1;
#pragma unroll
    for (int j = 0; j < 4; ++j) {
      w0[j] = pack2(o[2 * j], o[2 * j + 1]);
      w1[j] = pack2(o[8 + 2 * j], o[8 + 2 * j + 1]);
    }
    ST16(hr + lane * 8, w0);
    ST16(hr + 512 + lane * 8, w1);
  }
  STAMP_END(kid);
}

struct GemmP {
  const u16* A; const u16* B0; const u16* B1; const u16* res; const float* gate; u16* out;
};

#define NS0 3
#define NS1 4
#define NS3 4
#define LW0 1
#define LW1 1
#define LW3 1
template <int MODE> struct GCfg;
template <> struct GCfg<0> { static constexpr int BM = 128, BN = 192, WM = 2, WN = 2, NS = NS0, K = 1024, LDA = 1024, LDB = 3072, NTM = 16, NTN = 16, RM = 8, RN = 4, LW = LW0; };
template <> struct GCfg<1> { static constexpr int BM = 128, BN = 64, WM = 2, WN = 2, NS = NS1, K = 1024, LDA = 1024, LDB = 1024, NTM = 16, NTN = 16, RM = 4, RN = 8, LW = LW1; };
template <> struct GCfg<2> { static constexpr int BM = 256, BN = 256, WM = 4, WN = 2, NS = 2, K = 1024, LDA = 1024, LDB = 3584, NTM = 8, NTN = 28, RM = 4, RN = 7, LW = 0; };
template <> struct GCfg<3> { static constexpr int BM = 128, BN = 64, WM = 2, WN = 2, NS = NS3, K = 3584, LDA = 3584, LDB = 1024, NTM = 16, NTN = 16, RM = 4, RN = 8, LW = LW3; };

template <int MODE>
__global__ __launch_bounds__(GCfg<MODE>::WM* GCfg<MODE>::WN * 64 * (1 + GCfg<MODE>::LW), 2) void gemm_kernel(GemmP p) {
  using C = GCfg<MODE>;
  constexpr int BM = C::BM, BN = C::BN, WM = C::WM, WN = C::WN, NS = C::NS, NW = WM * WN;
  constexpr int TM = BM / WM, TN = BN / WN, MI = TM / 16, NI = TN / 16;
  constexpr int A_BYTES = BM * 128, B_BYTES = BN * 128, ST_BYTES = A_BYTES + B_BYTES;
  constexpr int GA = BM / 8 / NW, GB = BN / 8 / NW, G = GA + GB;
  constexpr int NT = C::K / 64;
  static_assert(BM % (8 * NW) == 0 && BN % (8 * NW) == 0, "stage split");
  static_assert(NT >= NS + 1, "K too small");
  extern __shared__ __attribute__((aligned(16))) char smem[];
  STAMP_BEGIN();

  const int tid = threadIdx.x, lane = tid & 63;
  constexpr bool LW = C::LW != 0;
  const int wid_all = __builtin_amdgcn_readfirstlane(tid >> 6);
  const int wid = wid_all % NW;
  const int wm = wid / WN, wn = wid % WN;
  int tm, tn;
  {
    const int x = blockIdx.x & 7, idx = blockIdx.x >> 3;
    constexpr int NRM = C::NTM / C::RM;
    const int rm = x % NRM, rn = x / NRM;
    tm = rm * C::RM + idx % C::RM;
    tn = rn * C::RN + idx / C::RM;
  }
  const int m0 = tm * BM;

  const int par = wid & 1;
  const unsigned a_voff = (unsigned)(((lane >> 3) * C::LDA + 8 * ((lane & 7) ^ ((4 * par + (lane >> 4)) & 7))) * 2);
  const unsigned b_voff = (unsigned)(((lane >> 3) * C::LDB + 8 * ((lane & 7) ^ ((((lane >> 4) & 1) | (par << 1)) << 1))) * 2);
  const char* Abase = (const char*)(p.A + (size_t)m0 * C::LDA);
  const unsigned lds0 = (unsigned)(size_t)(LAS char*)smem;

  auto issue1 = [&](auto gi, int t, int bufoff) {
    constexpr int g = decltype(gi)::value;
    if constexpr (g < GA) {
      const int i = wid + NW * g;
      const char* gp = Abase + ((size_t)(8 * i) * C::LDA + (size_t)t * 64) * 2;
      glds16(gp, a_voff, lds0 + bufoff + i * 1024);
    } else {
      const int i = wid + NW * (g - GA);
      const int pn = i >> 3, kr = 8 * (i & 7);
      const u16* bp;
      if constexpr (MODE == 2) bp = (pn < 2 ? p.B0 : p.B1) + tn * 128 + (pn & 1) * 64;
      else bp = p.B0 + tn * BN + pn * 64;
      const char* gp = (const char*)(bp + ((size_t)t * 64 + kr) * C::LDB);
      glds16(gp, b_voff, lds0 + bufoff + A_BYTES + i * 1024);
    }
  };

  if constexpr (LW) {
    if (wid_all >= NW) {
      sfor<0, NS>([&](auto sc_) { sfor<0, G>([&](auto gc) { issue1(gc, decltype(sc_)::value, decltype(sc_)::value * ST_BYTES); }); });
      WAIT_V((NS - 1) * G);
      BAR();
      int lb = 0;
      for (int t = 0; t < NT - NS; ++t) {
        WAIT_V((NS - 2) * G);
        BAR();
        sfor<0, G>([&](auto gc) { issue1(gc, t + NS, lb); });
        lb = (lb + ST_BYTES == NS * ST_BYTES) ? 0 : lb + ST_BYTES;
      }
      sfor<0, NS - 1>([&](auto rc) { WAIT_V((NS - 2 - decltype(rc)::value) * G); BAR(); });
      return;
    }
  }

  const int fr = lane & 15, fq = lane >> 4;
  u32x4 rv[4];
  f32x4 gv[NI];
  if constexpr (MODE == 1 || MODE == 3) {
#pragma unroll
    for (int i = 0; i < 4; ++i) rv[i] = *(const u32x4*)(p.res + (size_t)(m0 + i * 32 + (tid >> 3)) * D_ + tn * BN + (tid & 7) * 8);
#pragma unroll
    for (int ni = 0; ni < NI; ++ni) gv[ni] = ld_mod4(p.gate, tn * BN + wn * TN + ni * 16 + 4 * fq);
  }
  const int a_rd0 = (wm * TM + fr) * 128 + ((fq ^ (fr >> 1)) << 4);
  const int a_rd1 = a_rd0 ^ 64;
  int b_rd[(MODE == 2) ? 4 : NI];
  {
    const int q = (lane & 15) >> 2, pp = lane & 3;
    const int gl = ((q >> 1) & 1) | ((fq & 1) << 1);
#pragma unroll
    for (int ni = 0; ni < ((MODE == 2) ? 4 : NI); ++ni) {
      int pn, un;
      if constexpr (MODE == 2) { pn = wn; un = ni & 3; }
      else { const int j = wn * NI + ni; pn = j >> 2; un = j & 3; }
      b_rd[ni] = A_BYTES + pn * 8192 + (8 * fq + q) * 128 + ((un ^ gl) << 5) + pp * 8;
    }
  }

  f32x4 acc[MI][NI];
#pragma unroll
  for (int mi = 0; mi < MI; ++mi)
#pragma unroll
    for (int ni = 0; ni < NI; ++ni) acc[mi][ni] = (f32x4){0.f, 0.f, 0.f, 0.f};

  constexpr int NSP = (MODE == 2) ? 2 : 1;
  constexpr int NH = NI / NSP, NM = MI * NH, NPH = 2 * NSP;
  constexpr int GW = (NS >= 3) ? NPH : NPH / 2;
  constexpr int GPP = (G + GW - 1) / GW;
  bf16x8 a0[MI], a1[MI], b0[NH], b1[NH];
  s16x4 blo[NH];

  auto rd_b = [&](auto ri, bf16x8* BF, const char* sb, int ks, int hf) {
    constexpr int r = decltype(ri)::value, ni = r >> 1;
    const int bo = (MODE == 2) ? b_rd[ni] + hf * 16384 : b_rd[ni];
    if constexpr ((r & 1) == 0) blo[ni] = tr_read(sb + bo + ks * 4096);
    else BF[ni] = __builtin_shufflevector(blo[ni], tr_read(sb + bo + ks * 4096 + 512), 0, 1, 2, 3, 4, 5, 6, 7);
  };
  auto rd_a = [&](auto ri, bf16x8* AF, const char* sb, int ks) {
    constexpr int mi = decltype(ri)::value;
    AF[mi] = lds_read16(sb + (ks ? a_rd1 : a_rd0) + mi * 2048);
  };
  auto phase = [&](auto hfc, auto rdm, auto g0c, auto g1c, bf16x8* AF, bf16x8* BF, bf16x8* AN_, bf16x8* BN_,
                   const char* rsb, int rks, int rhf, int gt, int gbuf) {
    constexpr int hf = decltype(hfc)::value, RDM = decltype(rdm)::value;
    constexpr int g0 = decltype(g0c)::value, g1 = decltype(g1c)::value, NG = g1 - g0;
    constexpr int NR = RDM == 0 ? 0 : (RDM == 1 ? 2 * NH : 2 * NH + MI);
    sfor<0, NM>([&](auto ic) {
      constexpr int i = decltype(ic)::value, mi = i / NH, ni = i % NH;
      MFMA16(acc[mi][hf * NH + ni], BF[ni], AF[mi]);
      sfor<0, NR>([&](auto rc) {
        constexpr int r = decltype(rc)::value;
        if constexpr ((r * (NM - NM / 4)) / (NR > 0 ? NR : 1) == i) {
          if constexpr (r < 2 * NH) rd_b(rc, BN_, rsb, rks, rhf);
          else rd_a(IC<r - 2 * NH>{}, AN_, rsb, rks);
        }
      });
      sfor<0, NG>([&](auto gc) {
        constexpr int g = decltype(gc)::value;
        if constexpr ((g * NM) / (NG > 0 ? NG : 1) == i) issue1(IC<g0 + g>{}, gt, gbuf);
      });
      SBAR();
    });
  };
  constexpr auto gmin = [](int a, int b) { return a < b ? a : b; };
  int cur = 0, prv = 0;
  auto ktile = [&](auto vmc, auto restc, auto firstc, int t) {
    constexpr int VM = decltype(vmc)::value;
    constexpr bool REST = decltype(restc)::value && !LW, FIRST = decltype(firstc)::value && !LW;
    const int nxt = (cur + ST_BYTES == NS * ST_BYTES) ? 0 : cur + ST_BYTES;
    const char* sc = smem + cur;
    const char* sn = smem + nxt;
    constexpr int R1 = REST ? gmin(G, GPP) : 0, R2 = REST ? gmin(G, 2 * GPP) : 0, R3 = REST ? gmin(G, 3 * GPP) : 0, R4 = REST ? gmin(G, 4 * GPP) : 0;
    constexpr int F1 = FIRST ? gmin(G, GPP) : 0;
    if constexpr (NSP == 1) {
      phase(IC<0>{}, IC<2>{}, IC<R1>{}, IC<R2>{}, a0, b0, a1, b1, sc, 1, 0, t - 1 + NS, prv);
      if constexpr (VM >= 0) {
        if constexpr (!LW) WAIT_V(VM);
        WAIT_L0(); BAR();
        phase(IC<0>{}, IC<2>{}, IC<0>{}, IC<F1>{}, a1, b1, a0, b0, sn, 0, 0, t + NS, cur);
      } else {
        phase(IC<0>{}, IC<0>{}, IC<0>{}, IC<0>{}, a1, b1, a0, b0, sn, 0, 0, 0, 0);
      }
    } else {
      phase(IC<0>{}, IC<1>{}, IC<R1>{}, IC<R2>{}, a0, b0, a1, b1, sc, 0, 1, t - 1 + NS, prv);
      phase(IC<1>{}, IC<2>{}, IC<R2>{}, IC<R3>{}, a0, b1, a1, b0, sc, 1, 0, t - 1 + NS, prv);
      phase(IC<0>{}, IC<1>{}, IC<R3>{}, IC<R4>{}, a1, b0, a1, b1, sc, 1, 1, t - 1 + NS, prv);
      if constexpr (VM >= 0) {
        WAIT_V(VM); WAIT_L0(); BAR();
        phase(IC<1>{}, IC<2>{}, IC<0>{}, IC<F1>{}, a1, b1, a0, b0, sn, 0, 0, t + NS, cur);
      } else {
        phase(IC<1>{}, IC<0>{}, IC<0>{}, IC<0>{}, a1, b1, a0, b0, sn, 0, 0, 0, 0);
      }
    }
    prv = cur; cur = nxt;
  };

  if constexpr (!LW) {
    sfor<0, NS>([&](auto sc_) { sfor<0, G>([&](auto gc) { issue1(gc, decltype(sc_)::value, decltype(sc_)::value * ST_BYTES); }); });
    WAIT_V((NS - 1) * G);
  }
  BAR();
  STAMP_MID(MODE == 0 ? 2 : MODE == 1 ? 4 : MODE == 2 ? 6 : 7, 0);
  sfor<0, 2 * NH>([&](auto rc) { rd_b(rc, b0, smem, 0, 0); });
  sfor<0, MI>([&](auto rc) { rd_a(rc, a0, smem, 0); });
  using TT = IC<1>; using FF = IC<0>;
  ktile(IC<(NS - 2) * G>{}, FF{}, TT{}, 0);
  for (int t = 1; t < NT - NS; ++t) ktile(IC<(NS - 2) * G>{}, TT{}, TT{}, t);
  ktile(IC<(NS - 2) * G>{}, TT{}, FF{}, NT - NS);
  sfor<0, NS - 2>([&](auto rc) { ktile(IC<(NS - 3 - decltype(rc)::value) * G>{}, FF{}, FF{}, 0); });
  ktile(IC<-1>{}, FF{}, FF{}, 0);

  STAMP_MID(MODE == 0 ? 2 : MODE == 1 ? 4 : MODE == 2 ? 6 : 7, 1);
  if constexpr (MODE == 0) {
    const float qs = 0.17677669529663687f * 1.4426950408889634f;
    BAR();
    char* st = smem + wid * (64 * 208);
#pragma unroll
    for (int ni = 0; ni < NI; ++ni) {
      const int n = tn * BN + wn * TN + ni * 16;
      const float sc = n < 1024 ? qs : 1.0f;
#pragma unroll
      for (int mi = 0; mi < MI; ++mi) {
        u32x2 w;
        w[0] = pack2(acc[mi][ni][0] * sc, acc[mi][ni][1] * sc);
        w[1] = pack2(acc[mi][ni][2] * sc, acc[mi][ni][3] * sc);
        *(LAS u32x2*)(st + (mi * 16 + fr) * 208 + ni * 32 + fq * 8) = w;
      }
    }
#pragma unroll
    for (int j = 0; j < 3; ++j) {
      const int hh = (tn * BN + wn * TN + j * 32) >> 5;
      u16* ob = p.out + ((size_t)hh * S_ + m0 + wm * TM) * 32 + lane * 8;
#pragma unroll
      for (int i = 0; i < 4; ++i) {
        const u32x4 v = *(const LAS u32x4*)(st + (i * 16 + (lane >> 2)) * 208 + j * 64 + (lane & 3) * 16);
        ST16(ob + i * 512, v);
      }
    }
  } else if constexpr (MODE == 1 || MODE == 3) {
    BAR();
#pragma unroll
    for (int ni = 0; ni < NI; ++ni) {
#pragma unroll
      for (int mi = 0; mi < MI; ++mi) {
        u32x2 w;
        w[0] = pack2(gv[ni][0] * rbf(acc[mi][ni][0]), gv[ni][1] * rbf(acc[mi][ni][1]));
        w[1] = pack2(gv[ni][2] * rbf(acc[mi][ni][2]), gv[ni][3] * rbf(acc[mi][ni][3]));
        *(LAS u32x2*)(smem + (wm * TM + mi * 16 + fr) * 144 + wn * 64 + ni * 32 + fq * 8) = w;
      }
    }
    WAIT_L0();
    BAR();
#pragma unroll
    for (int i = 0; i < 4; ++i) {
      const u32x4 v = *(const LAS u32x4*)(smem + (i * 32 + (tid >> 3)) * 144 + (tid & 7) * 16);
      u32x4 w;
#pragma unroll
      for (int j = 0; j < 4; ++j) w[j] = pack2(lo_f(rv[i][j]) + lo_f(v[j]), hi_f(rv[i][j]) + hi_f(v[j]));
      ST16(p.out + (size_t)(m0 + i * 32 + (tid >> 3)) * D_ + tn * BN + (tid & 7) * 8, w);
    }
  } else {
    BAR();
    char* st = smem + wid * (64 * 144);
#pragma unroll
    for (int ni = 0; ni < 4; ++ni)
#pragma unroll
      for (int mi = 0; mi < MI; ++mi) {
        float o[4];
#pragma unroll
        for (int r = 0; r < 4; ++r) o[r] = rbf(acc[mi][ni][r]) * rbf(acc[mi][ni + 4][r]);
        u32x2 w;
        w[0] = pack2(o[0], o[1]);
        w[1] = pack2(o[2], o[3]);
        *(LAS u32x2*)(st + (mi * 16 + fr) * 144 + ni * 32 + fq * 8) = w;
      }
    u16* ob = p.out + (size_t)(m0 + wm * TM + (lane >> 3)) * F_ + tn * 128 + wn * 64 + (lane & 7) * 8;
#pragma unroll
    for (int i = 0; i < 8; ++i) {
      const u32x4 v = *(const LAS u32x4*)(st + (i * 8 + (lane >> 3)) * 144 + (lane & 7) * 16);
      ST16(ob + (size_t)i * 8 * F_, v);
    }
  }
  STAMP_END(MODE == 0 ? 2 : MODE == 1 ? 4 : MODE == 2 ? 6 : 7);
}

#define AT_NSTAGE 6
constexpr int AT_KT = 128, AT_NS = AT_NSTAGE, AT_ST = 16384;
#define MFMA32(a, b, c) __builtin_amdgcn_mfma_f32_32x32x16_bf16(a, b, c, 0, 0, 0)
#define AEXP(x) __builtin_amdgcn_exp2f(x)
#define MX3(a, b, c) __builtin_fmaxf(__builtin_fmaxf((a), (b)), (c))

__device__ __forceinline__ float tile_rowmax(const f32x16& s0, const f32x16& s1) {
  float a = MX3(s0[0], s0[1], s1[0]), b = MX3(s0[2], s0[3], s1[1]);
  a = MX3(a, s1[2], s1[3]);
#pragma unroll
  for (int r = 4; r < 16; r += 4) {
    a = MX3(a, s0[r], s0[r + 1]); b = MX3(b, s0[r + 2], s0[r + 3]);
    a = MX3(a, s1[r], s1[r + 1]); b = MX3(b, s1[r + 2], s1[r + 3]);
  }
  float mx = __builtin_fmaxf(a, b);
  auto rr = __builtin_amdgcn_permlane32_swap(__float_as_uint(mx), __float_as_uint(mx), false, false);
  return __builtin_fmaxf(__uint_as_float(rr[0]), __uint_as_float(rr[1]));
}

__global__ __launch_bounds__(512, 2) void attn_kernel(const u16* __restrict__ qkv, u16* __restrict__ o) {
  extern __shared__ __attribute__((aligned(16))) char smem[];
  STAMP_BEGIN();
  const int tid = threadIdx.x, lane = tid & 63;
  const int wid = __builtin_amdgcn_readfirstlane(tid >> 6);
  const int x = blockIdx.x & 7, idx = blockIdx.x >> 3;
  const int hd = x * 4 + (idx >> 3), qb = idx & 7;
  const int r32 = lane & 31, hi = lane >> 5;
  const u16* Q = qkv + ((size_t)(0 * 32 + hd) * S_) * 32;
  const u16* Kp = qkv + ((size_t)(1 * 32 + hd) * S_) * 32;
  const u16* Vp = qkv + ((size_t)(2 * 32 + hd) * S_) * 32;
  const int q0 = qb * 256 + wid * 32;

  const unsigned k_voff = (unsigned)(((lane >> 2) * 32 + 8 * ((lane & 3) ^ ((lane >> 4) & 3))) * 2);
  const unsigned v_voff = (unsigned)(lane * 16);
  const unsigned lds0 = (unsigned)(size_t)(LAS char*)smem;
  auto issue = [&](int t, int bufoff) {
    const char* gk = (const char*)(Kp + ((size_t)t * AT_KT + wid * 16) * 32);
    const char* gv = (const char*)(Vp + ((size_t)t * AT_KT + wid * 16) * 32);
    glds16(gk, k_voff, lds0 + bufoff + wid * 1024);
    glds16(gv, v_voff, lds0 + bufoff + 8192 + wid * 1024);
  };
#pragma unroll
  for (int s = 0; s < AT_NS - 1; ++s) issue(s, s * AT_ST);
  const bf16x8 qf0 = *(const bf16x8*)(Q + (size_t)(q0 + r32) * 32 + hi * 8);
  const bf16x8 qf1 = *(const bf16x8*)(Q + (size_t)(q0 + r32) * 32 + 16 + hi * 8);

  const int k_rd0 = r32 * 64 + ((hi ^ ((r32 >> 2) & 3)) << 4);
  const int k_rd1 = k_rd0 ^ 32;
  const int v_rd = 8192 + (4 * hi + ((lane & 15) >> 2)) * 64 + ((lane >> 4) & 1) * 32 + (lane & 3) * 8;

  f32x16 oacc = {};
  f32x16 negm;
  float m, l = 0.f;
  f32x16 sa0, sa1, sb0, sb1;
  u32x4 pp0 = {}, pp1 = {}, pp2 = {}, pp3 = {};
  bf16x8 vf0, vf1, vf2, vf3;
#define AT_BIG 1.0e30f
  constexpr float BIG = AT_BIG;
  constexpr int NT = S_ / AT_KT;

#define SCORES(D0, D1, kb, CIN)                                                         \
  do {                                                                                  \
    bf16x8 k00 = lds_read16((kb) + k_rd0), k01 = lds_read16((kb) + k_rd1);              \
    bf16x8 k10 = lds_read16((kb) + 2048 + k_rd0), k11 = lds_read16((kb) + 2048 + k_rd1);\
    D0 = MFMA32(k00, qf0, CIN); D1 = MFMA32(k10, qf0, CIN);                             \
    D0 = MFMA32(k01, qf1, D0);  D1 = MFMA32(k11, qf1, D1);                              \
  } while (0)
#define VFRAG(vb, s) __builtin_shufflevector(tr_read((vb) + (s) * 1024), tr_read((vb) + (s) * 1024 + 512), 0, 1, 2, 3, 4, 5, 6, 7)
#define READV(vb) do { vf0 = VFRAG(vb, 0); vf1 = VFRAG(vb, 1); vf2 = VFRAG(vb, 2); vf3 = VFRAG(vb, 3); } while (0)
#define PV()                                                                            \
  do {                                                                                  \
    oacc = MFMA32(vf0, __builtin_bit_cast(bf16x8, pp0), oacc);                          \
    oacc = MFMA32(vf1, __builtin_bit_cast(bf16x8, pp1), oacc);                          \
    oacc = MFMA32(vf2, __builtin_bit_cast(bf16x8, pp2), oacc);                          \
    oacc = MFMA32(vf3, __builtin_bit_cast(bf16x8, pp3), oacc);                          \
  } while (0)
#define EXPSUM(C0, C1, LS)                                                              \
  do {                                                                                  \
    f32x2 ls2_ = {0.f, 0.f}, ls3_ = {0.f, 0.f};                                         \
    _Pragma("unroll") for (int r = 0; r < 16; r += 2) {                                 \
      C0[r] = AEXP(C0[r]); C0[r + 1] = AEXP(C0[r + 1]);                                 \
      C1[r] = AEXP(C1[r]); C1[r + 1] = AEXP(C1[r + 1]);                                 \
      ls2_ += (f32x2){C0[r], C0[r + 1]};                                                \
      ls3_ += (f32x2){C1[r], C1[r + 1]};                                                \
    }                                                                                   \
    ls2_ += ls3_;                                                                       \
    LS = ls2_[0] + ls2_[1];                                                             \
  } while (0)
#define PACK(C0, C1)                                                                    \
  do {                                                                                  \
    _Pragma("unroll") for (int j = 0; j < 4; ++j) {                                     \
      pp0[j] = pack2(C0[2 * j], C0[2 * j + 1]);                                         \
      pp1[j] = pack2(C0[8 + 2 * j], C0[8 + 2 * j + 1]);                                 \
      pp2[j] = pack2(C1[2 * j], C1[2 * j + 1]);                                         \
      pp3[j] = pack2(C1[8 + 2 * j], C1[8 + 2 * j + 1]);                                 \
    }                                                                                   \
  } while (0)
#define STEP(C0, C1, N0, N1, kcur, knext, vprev, HASN)                                  \
  do {                                                                                  \
    READV(vprev);                                                                       \
    if (HASN) SCORES(N0, N1, knext, negm);                                              \
    PV();                                                                               \
    float ls;                                                                           \
    EXPSUM(C0, C1, ls);                                                                 \
    if (__builtin_expect(__any(!(ls <= BIG)), 0)) {                                     \
                  \
      f32x16 z_ = {};                                                                   \
      SCORES(C0, C1, kcur, z_);                                                         \
      const float mn = __builtin_fmaxf(m, tile_rowmax(C0, C1));                         \
      const float dm = m - mn;                                                          \
      const float f = __builtin_amdgcn_exp2f(dm);                                       \
      m = mn; l *= f;                                                                   \
      _Pragma("unroll") for (int r = 0; r < 16; ++r) {                                  \
        oacc[r] *= f; negm[r] = -mn; C0[r] -= mn; C1[r] -= mn;                          \
        if (HASN) { N0[r] += dm; N1[r] += dm; }                                         \
      }                                                                                 \
      EXPSUM(C0, C1, ls);                                                               \
    }                                                                                   \
    l += ls;                                                                            \
    PACK(C0, C1);                                                                       \
  } while (0)

  WAIT_V(2 * (AT_NS - 2));
  BAR();
  {
    f32x16 z = {};
    SCORES(sa0, sa1, smem, z);
    m = tile_rowmax(sa0, sa1);
#pragma unroll
    for (int r = 0; r < 16; ++r) { negm[r] = -m; sa0[r] -= m; sa1[r] -= m; }
  }
  int cur = 0, prv = 0;
  for (int t = 0; t < NT - 1; ++t) {
    const char* sb = smem + cur;
    int nx = cur + AT_ST; if (nx >= AT_NS * AT_ST) nx = 0;
    int prv2 = cur - AT_ST; if (prv2 < 0) prv2 = (AT_NS - 1) * AT_ST;
    STEP(sa0, sa1, sb0, sb1, sb, sb + 4096, smem + prv + 4096 + v_rd, true);
    if (t + AT_NS - 1 < NT) {
      WAIT_V(2 * (AT_NS - 3));
      WAIT_L0();
      BAR();
      issue(t + AT_NS - 1, prv2);
    } else {
      WAIT_V(0);
      WAIT_L0();
      BAR();
    }
    STEP(sb0, sb1, sa0, sa1, sb + 4096, smem + nx, sb + v_rd, true);
    prv = cur; cur = nx;
  }
  {
    const char* sb = smem + cur;
    STEP(sa0, sa1, sb0, sb1, sb, sb + 4096, smem + prv + 4096 + v_rd, true);
    STEP(sb0, sb1, sa0, sa1, sb + 4096, sb, sb + v_rd, false);
    prv = cur;
  }
  READV(smem + prv + 4096 + v_rd);
  PV();
#undef SCORES
#undef VFRAG
#undef READV
#undef PV
#undef EXPSUM
#undef PACK
#undef STEP
  {
    auto rr = __builtin_amdgcn_permlane32_swap(__float_as_uint(l), __float_as_uint(l), false, false);
    l = __uint_as_float(rr[0]) + __uint_as_float(rr[1]);
  }
  const float inv = 1.0f / l;
  u16* op = o + (size_t)(q0 + r32) * D_ + hd * 32 + 4 * hi;
#pragma unroll
  for (int g = 0; g < 4; ++g) {
    u32x2 w;
    w[0] = pack2(oacc[4 * g] * inv, oacc[4 * g + 1] * inv);
    w[1] = pack2(oacc[4 * g + 2] * inv, oacc[4 * g + 3] * inv);
    *(u32x2*)(op + 8 * g) = w;
  }
  STAMP_END(3);
}

constexpr int A2_KT = 256, A2_NS = 3, A2_ST = 32768;
template <bool SAFE>
__device__ __forceinline__ void attn_pass(char* smem, const u16* Kp, const u16* Vp, bf16x8 qf0, bf16x8 qf1, int wid, int lane,
                                          f32x16& oacc, float& m, float& l,
                                          const u16* pw0, const u16* pw1, const u16* pw2, const u16* pw3) {
  const int wk = wid >> 3, r32 = lane & 31, hi = lane >> 5;
  const unsigned k_voff = (unsigned)(((lane >> 2) * 32 + 8 * ((lane & 3) ^ ((lane >> 4) & 3))) * 2);
  const unsigned v_voff = (unsigned)(lane * 16);
  const unsigned lds0 = (unsigned)(size_t)(LAS char*)smem;
  auto issue = [&](int t, int bufoff) {
    const char* gk = (const char*)(Kp + ((size_t)t * A2_KT + wid * 16) * 32);
    const char* gv = (const char*)(Vp + ((size_t)t * A2_KT + wid * 16) * 32);
    glds16(gk, k_voff, lds0 + bufoff + wid * 1024);
    glds16(gv, v_voff, lds0 + bufoff + 16384 + wid * 1024);
  };
  issue(0, 0);
  issue(1, A2_ST);
  const int k_rd0 = wk * 8192 + r32 * 64 + ((hi ^ ((r32 >> 2) & 3)) << 4);
  const int k_rd1 = k_rd0 ^ 32;
  const int v_rd = 16384 + wk * 8192 + (4 * hi + ((lane & 15) >> 2)) * 64 + ((lane >> 4) & 1) * 32 + (lane & 3) * 8;
  f32x16 negm = {};
  f32x2 lsa = {0.f, 0.f}, lsb = {0.f, 0.f};
  oacc = (f32x16){};
  m = 0.f; l = 0.f;
  constexpr int NT = S_ / A2_KT;
  int cur = 0;
  for (int t = 0; t < NT; ++t) {
    if (t == 0) WAIT_V(2); else if (t + 1 < NT) WAIT_V(3); else WAIT_V(0);
    BAR();
    {
      int ck = (blockIdx.x * NT + t) * 16 + wid;
      if (ck >= 2048 + 3 * 7168) ck -= 16384;
      const u16* pb = pw0;
      if (ck >= 2048) { pb = pw1; ck -= 2048; if (ck >= 7168) { pb = pw2; ck -= 7168; if (ck >= 7168) { pb = pw3; ck -= 7168; } } }
      glds16((const char*)pb + (size_t)ck * 1024, v_voff, lds0 + A2_NS * A2_ST);
      int nb = cur + 2 * A2_ST; if (nb >= A2_NS * A2_ST) nb -= A2_NS * A2_ST;
      if (t + 2 < NT) issue(t + 2, nb);
    }
#pragma unroll
    for (int hb = 0; hb < 2; ++hb) {
      const char* kb = smem + cur + hb * 4096;
      const bf16x8 k00 = lds_read16(kb + k_rd0), k01 = lds_read16(kb + k_rd1);
      const bf16x8 k10 = lds_read16(kb + 2048 + k_rd0), k11 = lds_read16(kb + 2048 + k_rd1);
      f32x16 s0, s1;
      if (SAFE || (t == 0 && hb == 0)) {
        f32x16 z_ = {};
        s0 = MFMA32(k00, qf0, z_); s1 = MFMA32(k10, qf0, z_);
        s0 = MFMA32(k01, qf1, s0); s1 = MFMA32(k11, qf1, s1);
        const float tm = tile_rowmax(s0, s1);
        const bool first = (t == 0 && hb == 0);
        const float mn = first ? tm : __builtin_fmaxf(m, tm);
        if (SAFE) {
          const float f = first ? 0.f : __builtin_amdgcn_exp2f(m - mn);
          lsa *= f; lsb *= f;
          oacc *= f;
        }
        m = mn;
#pragma unroll
        for (int r = 0; r < 16; ++r) { negm[r] = -mn; s0[r] -= mn; s1[r] -= mn; }
      } else {
        s0 = MFMA32(k00, qf0, negm); s1 = MFMA32(k10, qf0, negm);
        s0 = MFMA32(k01, qf1, s0); s1 = MFMA32(k11, qf1, s1);
      }
#pragma unroll
      for (int r = 0; r < 16; r += 2) {
        s0[r] = AEXP(s0[r]); s0[r + 1] = AEXP(s0[r + 1]);
        s1[r] = AEXP(s1[r]); s1[r + 1] = AEXP(s1[r + 1]);
        lsa += (f32x2){s0[r], s0[r + 1]};
        lsb += (f32x2){s1[r], s1[r + 1]};
      }
      u32x4 p0, p1, p2, p3;
#pragma unroll
      for (int j = 0; j < 4; ++j) {
        p0[j] = pack2(s0[2 * j], s0[2 * j + 1]);
        p1[j] = pack2(s0[8 + 2 * j], s0[8 + 2 * j + 1]);
        p2[j] = pack2(s1[2 * j], s1[2 * j + 1]);
        p3[j] = pack2(s1[8 + 2 * j], s1[8 + 2 * j + 1]);
      }
      const char* vb = smem + cur + hb * 4096 + v_rd;
#define VFRAG(s) __builtin_shufflevector(tr_read(vb + (s) * 1024), tr_read(vb + (s) * 1024 + 512), 0, 1, 2, 3, 4, 5, 6, 7)
      oacc = MFMA32(VFRAG(0), __builtin_bit_cast(bf16x8, p0), oacc);
      oacc = MFMA32(VFRAG(1), __builtin_bit_cast(bf16x8, p1), oacc);
      oacc = MFMA32(VFRAG(2), __builtin_bit_cast(bf16x8, p2), oacc);
      oacc = MFMA32(VFRAG(3), __builtin_bit_cast(bf16x8, p3), oacc);
#undef VFRAG
    }
    WAIT_L0();
    cur += A2_ST; if (cur >= A2_NS * A2_ST) cur = 0;
  }
  lsa += lsb;
  l = lsa[0] + lsa[1];
  {
    auto rr = __builtin_amdgcn_permlane32_swap(__float_as_uint(l), __float_as_uint(l), false, false);
    l = __uint_as_float(rr[0]) + __uint_as_float(rr[1]);
  }
  WAIT_V(0);
}

__global__ __launch_bounds__(1024, 4) void attn2_kernel(const u16* __restrict__ qkv, u16* __restrict__ o,
                                                        const u16* pw0, const u16* pw1, const u16* pw2, const u16* pw3) {
  extern __shared__ __attribute__((aligned(16))) char smem[];
  STAMP_BEGIN();
  const int tid = threadIdx.x, lane = tid & 63;
  const int wid = __builtin_amdgcn_readfirstlane(tid >> 6);
  const int wq = wid & 7, wk = wid >> 3;
  const int x = blockIdx.x & 7, idx = blockIdx.x >> 3;
  const int hd = x * 4 + (idx >> 3), qb = idx & 7;
  const int r32 = lane & 31, hi = lane >> 5;
  const u16* Q = qkv + ((size_t)(0 * 32 + hd) * S_) * 32;
  const u16* Kp = qkv + ((size_t)(1 * 32 + hd) * S_) * 32;
  const u16* Vp = qkv + ((size_t)(2 * 32 + hd) * S_) * 32;
  const int q0 = qb * 256 + wq * 32;
  const bf16x8 qf0 = *(const bf16x8*)(Q + (size_t)(q0 + r32) * 32 + hi * 8);
  const bf16x8 qf1 = *(const bf16x8*)(Q + (size_t)(q0 + r32) * 32 + 16 + hi * 8);

  f32x16 oacc;
  float m, l;
  attn_pass<false>(smem, Kp, Vp, qf0, qf1, wid, lane, oacc, m, l, pw0, pw1, pw2, pw3);
  {
    float chk = l;
#pragma unroll
    for (int r = 0; r < 16; ++r) chk += __builtin_fabsf(oacc[r]);
    const int bad = !(chk < 1.0e37f);
    if (__syncthreads_or(bad)) attn_pass<true>(smem, Kp, Vp, qf0, qf1, wid, lane, oacc, m, l, pw0, pw1, pw2, pw3);
  }
  __syncthreads();
  float* xb = (float*)smem + wq * (18 * 64);
  if (wk == 1) {
    xb[lane] = m; xb[64 + lane] = l;
#pragma unroll
    for (int r = 0; r < 16; ++r) xb[(2 + r) * 64 + lane] = oacc[r];
  }
  __syncthreads();
  if (wk == 0) {
    const float m2 = xb[lane], l2 = xb[64 + lane];
    const float mn = __builtin_fmaxf(m, m2);
    const float e1 = (m - mn) * 0.5f, e2 = (m2 - mn) * 0.5f;
    const float f1 = __builtin_amdgcn_exp2f(e1), f2 = __builtin_amdgcn_exp2f(e2);
    const float lt = (l * f1) * f1 + (l2 * f2) * f2;
    const float inv = 1.0f / lt;
    u16* op = o + (size_t)(q0 + r32) * D_ + hd * 32 + 4 * hi;
#pragma unroll
    for (int g = 0; g < 4; ++g) {
      float v[4];
#pragma unroll
      for (int e = 0; e < 4; ++e) v[e] = ((oacc[4 * g + e] * f1) * f1 + (xb[(2 + 4 * g + e) * 64 + lane] * f2) * f2) * inv;
      u32x2 w;
      w[0] = pack2(v[0], v[1]);
      w[1] = pack2(v[2], v[3]);
      *(u32x2*)(op + 8 * g) = w;
    }
  }
  STAMP_END(3);
}

template <int MODE> static void launch_gemm(const GemmP& p, hipStream_t stream) {
  using C = GCfg<MODE>;
  constexpr int lds = C::NS * (C::BM + C::BN) * 128;
  hipFuncSetAttribute((const void*)gemm_kernel<MODE>, hipFuncAttributeMaxDynamicSharedMemorySize, lds);
  gemm_kernel<MODE><<<C::NTM * C::NTN, C::WM * C::WN * 64 * (1 + C::LW), lds, stream>>>(p);
}

extern "C" void kernel_launch(void* const* d_in, const int* in_sizes, int n_in, void* d_out, int out_size,
                              void* d_ws, size_t ws_size, hipStream_t stream) {
  const u16* x = (const u16*)d_in[0];
  const u16* wqkv = (const u16*)d_in[1];
  const u16* wo = (const u16*)d_in[2];
  const u16* wg = (const u16*)d_in[4];
  const u16* wu = (const u16*)d_in[5];
  const u16* wd = (const u16*)d_in[6];
  const u16* c = (const u16*)d_in[7];
  const u16* wmod = (const u16*)d_in[8];
  u16* out = (u16*)d_out;

  char* ws = (char*)d_ws;
  float* mod = (float*)ws;
  u16* h = (u16*)(ws + (1 << 20));
  u16* qkv = (u16*)(ws + (8 << 20));
  u16* ao = (u16*)(ws + (24 << 20));
  u16* x1 = (u16*)(ws + (32 << 20));
  u16* hh = (u16*)(ws + (40 << 20));

  mod_kernel<<<256, 256, 0, stream>>>(c, wmod, mod, x);
  ln_kernel<<<256 + 256 + 192, 256, 0, stream>>>(x, mod, 0, h, c, wmod, mod, wqkv, 1);
  { GemmP p{h, wqkv, nullptr, nullptr, nullptr, qkv}; launch_gemm<0>(p, stream); }
  hipFuncSetAttribute((const void*)attn2_kernel, hipFuncAttributeMaxDynamicSharedMemorySize, A2_NS * A2_ST + 1024);
  attn2_kernel<<<256, 1024, A2_NS * A2_ST + 1024, stream>>>(qkv, ao, wo, wg, wu, wd);
  { GemmP p{ao, wo, nullptr, x, mod + 2 * 1024, x1}; launch_gemm<1>(p, stream); }
  ln_kernel<<<256, 256, 0, stream>>>(x1, mod, 3 * 1024, h, c, wmod, mod, wqkv, 5);
  { GemmP p{h, wg, wu, nullptr, nullptr, hh}; launch_gemm<2>(p, stream); }
  { GemmP p{hh, wd, nullptr, x1, mod + 5 * 1024, out}; launch_gemm<3>(p, stream); }
}
```

```cpp
#include <hip/hip_runtime.h>
#include <hip/hip_bf16.h>
#include <stdint.h>

typedef unsigned short u16;
typedef short bf16x8 __attribute__((ext_vector_type(8)));
typedef short s16x4 __attribute__((ext_vector_type(4)));
typedef float f32x2 __attribute__((ext_vector_type(2)));
typedef float f32x4 __attribute__((ext_vector_type(4)));
typedef float f32x16 __attribute__((ext_vector_type(16)));
typedef unsigned u32x2 __attribute__((ext_vector_type(2)));
typedef unsigned u32x4 __attribute__((ext_vector_type(4)));
typedef __bf16 bf16x2_t __attribute__((ext_vector_type(2)));
#define LAS __attribute__((address_space(3)))
#define GAS __attribute__((address_space(1)))

constexpr int S_ = 2048, D_ = 1024, H_ = 32, DH_ = 32, F_ = 3584;

__device__ __forceinline__ float bf2f(u16 u) { return __uint_as_float((unsigned)u << 16); }
__device__ __forceinline__ unsigned pack2(float lo, float hi) {
  f32x2 v = {lo, hi};
  bf16x2_t b = __builtin_convertvector(v, bf16x2_t);
  return __builtin_bit_cast(unsigned, b);
}
__device__ __forceinline__ float rbf(float f) {
  __bf16 b = (__bf16)f;
  return __uint_as_float((unsigned)__builtin_bit_cast(u16, b) << 16);
}
__device__ __forceinline__ float lo_f(unsigned u) { return __uint_as_float(u << 16); }
__device__ __forceinline__ float hi_f(unsigned u) { return __uint_as_float(u & 0xffff0000u); }

#define STAMP_BEGIN()
#define STAMP_END(kid)
#define STAMP_MID(kid, i)
#define WAIT_V(n) asm volatile("s_waitcnt vmcnt(%0)" ::"n"(n) : "memory")
#define WAIT_L0() asm volatile("s_waitcnt lgkmcnt(0)" ::: "memory")
#define BAR() __builtin_amdgcn_s_barrier()

__device__ __forceinline__ void glds16(const void* sbase, unsigned voff, unsigned ldsaddr) {
  asm volatile("s_mov_b32 m0, %2\n\ts_nop 0\n\tglobal_load_lds_dwordx4 %0, %1" ::"v"(voff), "s"(sbase), "s"(ldsaddr) : "memory");
}
#define MFMA16(c, a, b) c = __builtin_amdgcn_mfma_f32_16x16x32_bf16(a, b, c, 0, 0, 0)
#define ST16(p, v) asm volatile("global_store_dwordx4 %0, %1, off sc0 sc1\n\ts_nop 1" ::"v"(p), "v"(v) : "memory")
#define SBAR() __builtin_amdgcn_sched_barrier(0)
#define SGB(mask, n) __builtin_amdgcn_sched_group_barrier(mask, n, 0)
template <int I> struct IC { static constexpr int value = I; };
template <int I, int N, class F> __device__ __forceinline__ void sfor(F&& f) {
  if constexpr (I < N) { f(IC<I>{}); sfor<I + 1, N>(f); }
}
__device__ __forceinline__ s16x4 tr_read(const char* p) {
  return __builtin_amdgcn_ds_read_tr16_b64_v4i16((LAS s16x4*)p);
}
__device__ __forceinline__ bf16x8 lds_read16(const char* p) { return *(const LAS bf16x8*)p; }

__device__ __forceinline__ void mod_block(const u16* __restrict__ c, const u16* __restrict__ wmod, float* __restrict__ modp,
                                          int cg, int ks, float* sh) {
  const int tid = threadIdx.x;
  const int cl = tid & 7, rg = tid >> 3;
  const int col0 = cg * 64 + cl * 8;
  u32x4 w[8];
#pragma unroll
  for (int i = 0; i < 8; ++i)
    w[i] = __builtin_nontemporal_load((const u32x4*)(wmod + (size_t)(ks * 256 + rg + i * 32) * 6144 + col0));
  {
    const float v = bf2f(c[ks * 256 + tid]);
    sh[tid] = rbf(v / (1.0f + __expf(-v)));
  }
  __syncthreads();
  float acc[8];
#pragma unroll
  for (int j = 0; j < 8; ++j) acc[j] = 0.f;
#pragma unroll
  for (int i = 0; i < 8; ++i) {
    const float s = sh[rg + i * 32];
#pragma unroll
    for (int j = 0; j < 4; ++j) {
      acc[2 * j] += s * lo_f(w[i][j]);
      acc[2 * j + 1] += s * hi_f(w[i][j]);
    }
  }
  float* red = sh + 256;
#pragma unroll
  for (int j = 0; j < 8; ++j) red[rg * 65 + cl * 8 + j] = acc[j];
  __syncthreads();
  if (tid < 64) {
    float s = 0.f;
#pragma unroll 8
    for (int r = 0; r < 32; ++r) s += red[r * 65 + tid];
    modp[ks * 6144 + cg * 64 + tid] = s;
  }
}
__device__ __forceinline__ void touch_block(const char* src) {
  u32x4 v[8];
#pragma unroll
  for (int i = 0; i < 8; ++i) v[i] = *(const u32x4*)(src + i * 4096 + threadIdx.x * 16);
#pragma unroll
  for (int i = 0; i < 8; ++i) asm volatile("" ::"v"(v[i]));
}
__device__ __forceinline__ f32x4 ld_mod4(const float* modp, int idx) {
  f32x4 a = *(const f32x4*)(modp + idx), b = *(const f32x4*)(modp + 6144 + idx);
  f32x4 c = *(const f32x4*)(modp + 2 * 6144 + idx), d = *(const f32x4*)(modp + 3 * 6144 + idx);
  f32x4 s = (a + b) + (c + d);
  return (f32x4){rbf(s[0]), rbf(s[1]), rbf(s[2]), rbf(s[3])};
}

__global__ __launch_bounds__(256) void mod_kernel(const u16* __restrict__ c, const u16* __restrict__ wmod,
                                                  float* __restrict__ modp, const u16* __restrict__ pf0, unsigned* flags) {
  __shared__ float sh[256 + 32 * 65];
  STAMP_BEGIN();
  const int b = blockIdx.x;
  if (threadIdx.x == 0) flags[b * 32] = 0u;
  if (b < 128) mod_block(c, wmod, modp, b >> 2, b & 3, sh);
  else touch_block((const char*)pf0 + (size_t)(b - 128) * 32768);
  STAMP_END(0);
}

__global__ __launch_bounds__(256) void ln_kernel(const u16* __restrict__ x, const float* modp, int moff, u16* __restrict__ h,
                                                 const u16* __restrict__ c, const u16* __restrict__ wmod, float* modp_w,
                                                 const u16* __restrict__ pf, int kid) {
  __shared__ float sh[256 + 32 * 65];
  STAMP_BEGIN();
  const int tid = threadIdx.x;
  if (blockIdx.x >= 256) {
    const int b = blockIdx.x - 256;
    if (b < 256) mod_block(c, wmod, modp_w, 32 + (b >> 2), b & 3, sh);
    else touch_block((const char*)pf + (size_t)(b - 256) * 32768);
    return;
  }
  const int lane = tid & 63, wid = tid >> 6;
  const int row = blockIdx.x * 8 + wid * 2;
  const u16* xr = x + (size_t)row * D_;
  u32x4 v[2][2];
#pragma unroll
  for (int r = 0; r < 2; ++r) {
    v[r][0] = *(const u32x4*)(xr + r * D_ + lane * 8);
    v[r][1] = *(const u32x4*)(xr + r * D_ + 512 + lane * 8);
  }
  {
    const f32x4 s0 = ld_mod4(modp, moff + tid * 4);
    const f32x4 s1 = ld_mod4(modp, moff + 1024 + tid * 4);
    *(f32x4*)(sh + tid * 4) = s0;
    *(f32x4*)(sh + 1024 + tid * 4) = (f32x4){rbf(1.0f + s1[0]), rbf(1.0f + s1[1]), rbf(1.0f + s1[2]), rbf(1.0f + s1[3])};
  }
  __syncthreads();
#pragma unroll
  for (int r = 0; r < 2; ++r) {
    float f[16];
#pragma unroll
    for (int j = 0; j < 4; ++j) {
      f[2 * j] = lo_f(v[r][0][j]); f[2 * j + 1] = hi_f(v[r][0][j]);
      f[8 + 2 * j] = lo_f(v[r][1][j]); f[8 + 2 * j + 1] = hi_f(v[r][1][j]);
    }
    float s = 0.f;
#pragma unroll
    for (int j = 0; j < 16; ++j) s += f[j];
#pragma unroll
    for (int o = 32; o >= 1; o >>= 1) s += __shfl_xor(s, o);
    const float mean = s * (1.0f / D_);
    float q = 0.f;
#pragma unroll
    for (int j = 0; j < 16; ++j) { float d = f[j] - mean; q += d * d; }
#pragma unroll
    for (int o = 32; o >= 1; o >>= 1) q += __shfl_xor(q, o);
    const float mb = rbf(mean);
    const float vb = rbf(q * (1.0f / D_));
    const float sd = rbf(sqrtf(rbf(vb + 1e-5f)));
    const float inv = 1.0f / sd;
    float o[16];
#pragma unroll
    for (int half = 0; half < 2; ++half) {
      const int c0 = half * 512 + lane * 8;
      const f32x4 sa0 = *(const f32x4*)(sh + c0), sa1 = *(const f32x4*)(sh + c0 + 4);
      const f32x4 ca0 = *(const f32x4*)(sh + 1024 + c0), ca1 = *(const f32x4*)(sh + 1024 + c0 + 4);
#pragma unroll
      for (int j = 0; j < 8; ++j) {
        const float shf = j < 4 ? sa0[j & 3] : sa1[j & 3];
        const float sc = j < 4 ? ca0[j & 3] : ca1[j & 3];
        float t = rbf(f[half * 8 + j] - mb);
        t = rbf(t * inv);
        t = rbf(t * sc);
        o[half * 8 + j] = t + shf;
      }
    }
    u16* hr = h + (size_t)(row + r) * D_;
    u32x4 w0, w1;
#pragma unroll
    for (int j = 0; j < 4; ++j) {
      w0[j] = pack2(o[2 * j], o[2 * j + 1]);
      w1[j] = pack2(o[8 + 2 * j], o[8 + 2 * j + 1]);
    }
    ST16(hr + lane * 8, w0);
    ST16(hr + 512 + lane * 8, w1);
  }
  STAMP_END(kid);
}

struct GemmP {
  const u16* A; const u16* B0; const u16* B1; const u16* res; const float* gate; u16* out; float* slab; unsigned* flags;
};

#define NS0 3
#define NS1 4
#define NS3 4
#define LW0 1
#define LW1 1
#define LW3 1
template <int MODE> struct GCfg;
template <> struct GCfg<0> { static constexpr int BM = 128, BN = 192, WM = 2, WN = 2, NS = NS0, K = 1024, LDA = 1024, LDB = 3072, NTM = 16, NTN = 16, RM = 8, RN = 4, LW = LW0; };
template <> struct GCfg<1> { static constexpr int BM = 128, BN = 64, WM = 2, WN = 2, NS = NS1, K = 1024, LDA = 1024, LDB = 1024, NTM = 16, NTN = 16, RM = 4, RN = 8, LW = LW1; };
template <> struct GCfg<2> { static constexpr int BM = 256, BN = 256, WM = 4, WN = 2, NS = 2, K = 1024, LDA = 1024, LDB = 3584, NTM = 8, NTN = 28, RM = 4, RN = 7, LW = 0; };
template <> struct GCfg<3> { static constexpr int BM = 128, BN = 128, WM = 2, WN = 2, NS = NS3, K = 1792, LDA = 3584, LDB = 1024, NTM = 16, NTN = 16, RM = 4, RN = 8, LW = LW3; };

template <int MODE>
__global__ __launch_bounds__(GCfg<MODE>::WM* GCfg<MODE>::WN * 64 * (1 + GCfg<MODE>::LW), 2) void gemm_kernel(GemmP p) {
  using C = GCfg<MODE>;
  constexpr int BM = C::BM, BN = C::BN, WM = C::WM, WN = C::WN, NS = C::NS, NW = WM * WN;
  constexpr int TM = BM / WM, TN = BN / WN, MI = TM / 16, NI = TN / 16;
  constexpr int A_BYTES = BM * 128, B_BYTES = BN * 128, ST_BYTES = A_BYTES + B_BYTES;
  constexpr int GA = BM / 8 / NW, GB = BN / 8 / NW, G = GA + GB;
  constexpr int NT = C::K / 64;
  static_assert(BM % (8 * NW) == 0 && BN % (8 * NW) == 0, "stage split");
  static_assert(NT >= NS + 1, "K too small");
  extern __shared__ __attribute__((aligned(16))) char smem[];
  STAMP_BEGIN();

  const int tid = threadIdx.x, lane = tid & 63;
  constexpr bool LW = C::LW != 0;
  const int wid_all = __builtin_amdgcn_readfirstlane(tid >> 6);
  const int wid = wid_all % NW;
  const int wm = wid / WN, wn = wid % WN;
  int tm, tn;
  {
    const int x = blockIdx.x & 7, idx = blockIdx.x >> 3;
    constexpr int NRM = C::NTM / C::RM;
    const int rm = x % NRM, rn = x / NRM;
    tm = rm * C::RM + idx % C::RM;
    tn = rn * C::RN + idx / C::RM;
  }
  int kh = 0;
  if constexpr (MODE == 3) { kh = tn & 1; tn >>= 1; }
  const int m0 = tm * BM;

  const int par = wid & 1;
  const unsigned a_voff = (unsigned)(((lane >> 3) * C::LDA + 8 * ((lane & 7) ^ ((4 * par + (lane >> 4)) & 7))) * 2);
  const unsigned b_voff = (unsigned)(((lane >> 3) * C::LDB + 8 * ((lane & 7) ^ ((((lane >> 4) & 1) | (par << 1)) << 1))) * 2);
  const char* Abase = (const char*)(p.A + (size_t)m0 * C::LDA + kh * C::K);
  const unsigned lds0 = (unsigned)(size_t)(LAS char*)smem;

  auto issue1 = [&](auto gi, int t, int bufoff) {
    constexpr int g = decltype(gi)::value;
    if constexpr (g < GA) {
      const int i = wid + NW * g;
      const char* gp = Abase + ((size_t)(8 * i) * C::LDA + (size_t)t * 64) * 2;
      glds16(gp, a_voff, lds0 + bufoff + i * 1024);
    } else {
      const int i = wid + NW * (g - GA);
      const int pn = i >> 3, kr = 8 * (i & 7);
      const u16* bp;
      if constexpr (MODE == 2) bp = (pn < 2 ? p.B0 : p.B1) + tn * 128 + (pn & 1) * 64;
      else bp = p.B0 + (size_t)kh * C::K * C::LDB + tn * BN + pn * 64;
      const char* gp = (const char*)(bp + ((size_t)t * 64 + kr) * C::LDB);
      glds16(gp, b_voff, lds0 + bufoff + A_BYTES + i * 1024);
    }
  };

  if constexpr (LW) {
    if (wid_all >= NW) {
      sfor<0, NS>([&](auto sc_) { sfor<0, G>([&](auto gc) { issue1(gc, decltype(sc_)::value, decltype(sc_)::value * ST_BYTES); }); });
      WAIT_V((NS - 1) * G);
      BAR();
      int lb = 0;
      for (int t = 0; t < NT - NS; ++t) {
        WAIT_V((NS - 2) * G);
        BAR();
        sfor<0, G>([&](auto gc) { issue1(gc, t + NS, lb); });
        lb = (lb + ST_BYTES == NS * ST_BYTES) ? 0 : lb + ST_BYTES;
      }
      sfor<0, NS - 1>([&](auto rc) { WAIT_V((NS - 2 - decltype(rc)::value) * G); BAR(); });
      return;
    }
  }

  const int fr = lane & 15, fq = lane >> 4;
  u32x4 rv[4];
  f32x4 gv[NI];
  if constexpr (MODE == 1 || MODE == 3) {
#pragma unroll
    for (int i = 0; i < 4; ++i) {
      if constexpr (MODE == 1) rv[i] = *(const u32x4*)(p.res + (size_t)(m0 + i * 32 + (tid >> 3)) * D_ + tn * BN + (tid & 7) * 8);
      else rv[i] = *(const u32x4*)(p.res + (size_t)(m0 + kh * 64 + i * 16 + (tid >> 4)) * D_ + tn * BN + (tid & 15) * 8);
    }
#pragma unroll
    for (int ni = 0; ni < NI; ++ni) gv[ni] = ld_mod4(p.gate, tn * BN + wn * TN + ni * 16 + 4 * fq);
  }
  const int a_rd0 = (wm * TM + fr) * 128 + ((fq ^ (fr >> 1)) << 4);
  const int a_rd1 = a_rd0 ^ 64;
  int b_rd[(MODE == 2) ? 4 : NI];
  {
    const int q = (lane & 15) >> 2, pp = lane & 3;
    const int gl = ((q >> 1) & 1) | ((fq & 1) << 1);
#pragma unroll
    for (int ni = 0; ni < ((MODE == 2) ? 4 : NI); ++ni) {
      int pn, un;
      if constexpr (MODE == 2) { pn = wn; un = ni & 3; }
      else { const int j = wn * NI + ni; pn = j >> 2; un = j & 3; }
      b_rd[ni] = A_BYTES + pn * 8192 + (8 * fq + q) * 128 + ((un ^ gl) << 5) + pp * 8;
    }
  }

  f32x4 acc[MI][NI];
#pragma unroll
  for (int mi = 0; mi < MI; ++mi)
#pragma unroll
    for (int ni = 0; ni < NI; ++ni) acc[mi][ni] = (f32x4){0.f, 0.f, 0.f, 0.f};

  constexpr int NSP = (MODE == 2) ? 2 : 1;
  constexpr int NH = NI / NSP, NM = MI * NH, NPH = 2 * NSP;
  constexpr int GW = (NS >= 3) ? NPH : NPH / 2;
  constexpr int GPP = (G + GW - 1) / GW;
  bf16x8 a0[MI], a1[MI], b0[NH], b1[NH];
  s16x4 blo[NH];

  auto rd_b = [&](auto ri, bf16x8* BF, const char* sb, int ks, int hf) {
    constexpr int r = decltype(ri)::value, ni = r >> 1;
    const int bo = (MODE == 2) ? b_rd[ni] + hf * 16384 : b_rd[ni];
    if constexpr ((r & 1) == 0) blo[ni] = tr_read(sb + bo + ks * 4096);
    else BF[ni] = __builtin_shufflevector(blo[ni], tr_read(sb + bo + ks * 4096 + 512), 0, 1, 2, 3, 4, 5, 6, 7);
  };
  auto rd_a = [&](auto ri, bf16x8* AF, const char* sb, int ks) {
    constexpr int mi = decltype(ri)::value;
    AF[mi] = lds_read16(sb + (ks ? a_rd1 : a_rd0) + mi * 2048);
  };
  auto phase = [&](auto hfc, auto rdm, auto g0c, auto g1c, bf16x8* AF, bf16x8* BF, bf16x8* AN_, bf16x8* BN_,
                   const char* rsb, int rks, int rhf, int gt, int gbuf) {
    constexpr int hf = decltype(hfc)::value, RDM = decltype(rdm)::value;
    constexpr int g0 = decltype(g0c)::value, g1 = decltype(g1c)::value, NG = g1 - g0;
    constexpr int NR = RDM == 0 ? 0 : (RDM == 1 ? 2 * NH : 2 * NH + MI);
    sfor<0, NM>([&](auto ic) {
      constexpr int i = decltype(ic)::value, mi = i / NH, ni = i % NH;
      MFMA16(acc[mi][hf * NH + ni], BF[ni], AF[mi]);
      sfor<0, NR>([&](auto rc) {
        constexpr int r = decltype(rc)::value;
        if constexpr ((r * (NM - NM / 4)) / (NR > 0 ? NR : 1) == i) {
          if constexpr (r < 2 * NH) rd_b(rc, BN_, rsb, rks, rhf);
          else rd_a(IC<r - 2 * NH>{}, AN_, rsb, rks);
        }
      });
      sfor<0, NG>([&](auto gc) {
        constexpr int g = decltype(gc)::value;
        if constexpr ((g * NM) / (NG > 0 ? NG : 1) == i) issue1(IC<g0 + g>{}, gt, gbuf);
      });
      SBAR();
    });
  };
  constexpr auto gmin = [](int a, int b) { return a < b ? a : b; };
  int cur = 0, prv = 0;
  auto ktile = [&](auto vmc, auto restc, auto firstc, int t) {
    constexpr int VM = decltype(vmc)::value;
    constexpr bool REST = decltype(restc)::value && !LW, FIRST = decltype(firstc)::value && !LW;
    const int nxt = (cur + ST_BYTES == NS * ST_BYTES) ? 0 : cur + ST_BYTES;
    const char* sc = smem + cur;
    const char* sn = smem + nxt;
    constexpr int R1 = REST ? gmin(G, GPP) : 0, R2 = REST ? gmin(G, 2 * GPP) : 0, R3 = REST ? gmin(G, 3 * GPP) : 0, R4 = REST ? gmin(G, 4 * GPP) : 0;
    constexpr int F1 = FIRST ? gmin(G, GPP) : 0;
    if constexpr (NSP == 1) {
      phase(IC<0>{}, IC<2>{}, IC<R1>{}, IC<R2>{}, a0, b0, a1, b1, sc, 1, 0, t - 1 + NS, prv);
      if constexpr (VM >= 0) {
        if constexpr (!LW) WAIT_V(VM);
        WAIT_L0(); BAR();
        phase(IC<0>{}, IC<2>{}, IC<0>{}, IC<F1>{}, a1, b1, a0, b0, sn, 0, 0, t + NS, cur);
      } else {
        phase(IC<0>{}, IC<0>{}, IC<0>{}, IC<0>{}, a1, b1, a0, b0, sn, 0, 0, 0, 0);
      }
    } else {
      phase(IC<0>{}, IC<1>{}, IC<R1>{}, IC<R2>{}, a0, b0, a1, b1, sc, 0, 1, t - 1 + NS, prv);
      phase(IC<1>{}, IC<2>{}, IC<R2>{}, IC<R3>{}, a0, b1, a1, b0, sc, 1, 0, t - 1 + NS, prv);
      phase(IC<0>{}, IC<1>{}, IC<R3>{}, IC<R4>{}, a1, b0, a1, b1, sc, 1, 1, t - 1 + NS, prv);
      if constexpr (VM >= 0) {
        WAIT_V(VM); WAIT_L0(); BAR();
        phase(IC<1>{}, IC<2>{}, IC<0>{}, IC<F1>{}, a1, b1, a0, b0, sn, 0, 0, t + NS, cur);
      } else {
        phase(IC<1>{}, IC<0>{}, IC<0>{}, IC<0>{}, a1, b1, a0, b0, sn, 0, 0, 0, 0);
      }
    }
    prv = cur; cur = nxt;
  };

  if constexpr (!LW) {
    sfor<0, NS>([&](auto sc_) { sfor<0, G>([&](auto gc) { issue1(gc, decltype(sc_)::value, decltype(sc_)::value * ST_BYTES); }); });
    WAIT_V((NS - 1) * G);
  }
  BAR();
  STAMP_MID(MODE == 0 ? 2 : MODE == 1 ? 4 : MODE == 2 ? 6 : 7, 0);
  sfor<0, 2 * NH>([&](auto rc) { rd_b(rc, b0, smem, 0, 0); });
  sfor<0, MI>([&](auto rc) { rd_a(rc, a0, smem, 0); });
  using TT = IC<1>; using FF = IC<0>;
  ktile(IC<(NS - 2) * G>{}, FF{}, TT{}, 0);
  for (int t = 1; t < NT - NS; ++t) ktile(IC<(NS - 2) * G>{}, TT{}, TT{}, t);
  ktile(IC<(NS - 2) * G>{}, TT{}, FF{}, NT - NS);
  sfor<0, NS - 2>([&](auto rc) { ktile(IC<(NS - 3 - decltype(rc)::value) * G>{}, FF{}, FF{}, 0); });
  ktile(IC<-1>{}, FF{}, FF{}, 0);

  STAMP_MID(MODE == 0 ? 2 : MODE == 1 ? 4 : MODE == 2 ? 6 : 7, 1);
  if constexpr (MODE == 0) {
    const float qs = 0.17677669529663687f * 1.4426950408889634f;
    BAR();
    char* st = smem + wid * (64 * 208);
#pragma unroll
    for (int ni = 0; ni < NI; ++ni) {
      const int n = tn * BN + wn * TN + ni * 16;
      const float sc = n < 1024 ? qs : 1.0f;
#pragma unroll
      for (int mi = 0; mi < MI; ++mi) {
        u32x2 w;
        w[0] = pack2(acc[mi][ni][0] * sc, acc[mi][ni][1] * sc);
        w[1] = pack2(acc[mi][ni][2] * sc, acc[mi][ni][3] * sc);
        *(LAS u32x2*)(st + (mi * 16 + fr) * 208 + ni * 32 + fq * 8) = w;
      }
    }
#pragma unroll
    for (int j = 0; j < 3; ++j) {
      const int hh = (tn * BN + wn * TN + j * 32) >> 5;
      u16* ob = p.out + ((size_t)hh * S_ + m0 + wm * TM) * 32 + lane * 8;
#pragma unroll
      for (int i = 0; i < 4; ++i) {
        const u32x4 v = *(const LAS u32x4*)(st + (i * 16 + (lane >> 2)) * 208 + j * 64 + (lane & 3) * 16);
        ST16(ob + i * 512, v);
      }
    }
  } else if constexpr (MODE == 3) {
    const int tile = tm * 8 + tn;
    if (wm != kh) {
      float* dst = p.slab + ((size_t)(tile * 2 + kh) * 2 + wn) * 4096 + lane * 4;
#pragma unroll
      for (int mi = 0; mi < MI; ++mi)
#pragma unroll
        for (int ni = 0; ni < NI; ++ni) ST16(dst + (mi * NI + ni) * 256, __builtin_bit_cast(u32x4, acc[mi][ni]));
      WAIT_V(0);
      if (lane == 0) __hip_atomic_fetch_add(p.flags + (tile * 2 + kh) * 32, 1u, __ATOMIC_RELAXED, __HIP_MEMORY_SCOPE_AGENT);
      BAR();
    } else {
      const unsigned* fl = p.flags + (tile * 2 + (kh ^ 1)) * 32;
      for (int spin = 0; spin < (1 << 22); ++spin) {
        if (__builtin_amdgcn_readfirstlane(__hip_atomic_load(fl, __ATOMIC_RELAXED, __HIP_MEMORY_SCOPE_AGENT)) >= 2u) break;
        __builtin_amdgcn_s_sleep(1);
      }
      const float* src = p.slab + ((size_t)(tile * 2 + (kh ^ 1)) * 2 + wn) * 4096 + lane * 4;
      f32x4 pv[MI][NI];
#pragma unroll
      for (int mi = 0; mi < MI; ++mi)
#pragma unroll
        for (int ni = 0; ni < NI; ++ni)
          asm volatile("global_load_dwordx4 %0, %1, off sc0 sc1" : "=v"(pv[mi][ni]) : "v"(src + (mi * NI + ni) * 256) : "memory");
      BAR();
      asm volatile("s_waitcnt vmcnt(0)" ::: "memory");
      SBAR();
#pragma unroll
      for (int ni = 0; ni < NI; ++ni)
#pragma unroll
        for (int mi = 0; mi < MI; ++mi) {
          asm volatile("" : "+v"(pv[mi][ni]));
          const f32x4 a = acc[mi][ni] + pv[mi][ni];
          u32x2 w;
          w[0] = pack2(gv[ni][0] * rbf(a[0]), gv[ni][1] * rbf(a[1]));
          w[1] = pack2(gv[ni][2] * rbf(a[2]), gv[ni][3] * rbf(a[3]));
          *(LAS u32x2*)(smem + (mi * 16 + fr) * 272 + wn * 128 + ni * 32 + fq * 8) = w;
        }
    }
    WAIT_L0();
    BAR();
#pragma unroll
    for (int i = 0; i < 4; ++i) {
      const u32x4 v = *(const LAS u32x4*)(smem + (i * 16 + (tid >> 4)) * 272 + (tid & 15) * 16);
      u32x4 w;
#pragma unroll
      for (int j = 0; j < 4; ++j) w[j] = pack2(lo_f(rv[i][j]) + lo_f(v[j]), hi_f(rv[i][j]) + hi_f(v[j]));
      ST16(p.out + (size_t)(m0 + kh * 64 + i * 16 + (tid >> 4)) * D_ + tn * BN + (tid & 15) * 8, w);
    }
  } else if constexpr (MODE == 1) {
    BAR();
#pragma unroll
    for (int ni = 0; ni < NI; ++ni) {
#pragma unroll
      for (int mi = 0; mi < MI; ++mi) {
        u32x2 w;
        w[0] = pack2(gv[ni][0] * rbf(acc[mi][ni][0]), gv[ni][1] * rbf(acc[mi][ni][1]));
        w[1] = pack2(gv[ni][2] * rbf(acc[mi][ni][2]), gv[ni][3] * rbf(acc[mi][ni][3]));
        *(LAS u32x2*)(smem + (wm * TM + mi * 16 + fr) * 144 + wn * 64 + ni * 32 + fq * 8) = w;
      }
    }
    WAIT_L0();
    BAR();
#pragma unroll
    for (int i = 0; i < 4; ++i) {
      const u32x4 v = *(const LAS u32x4*)(smem + (i * 32 + (tid >> 3)) * 144 + (tid & 7) * 16);
      u32x4 w;
#pragma unroll
      for (int j = 0; j < 4; ++j) w[j] = pack2(lo_f(rv[i][j]) + lo_f(v[j]), hi_f(rv[i][j]) + hi_f(v[j]));
      ST16(p.out + (size_t)(m0 + i * 32 + (tid >> 3)) * D_ + tn * BN + (tid & 7) * 8, w);
    }
  } else {
    BAR();
    char* st = smem + wid * (64 * 144);
#pragma unroll
    for (int ni = 0; ni < 4; ++ni)
#pragma unroll
      for (int mi = 0; mi < MI; ++mi) {
        float o[4];
#pragma unroll
        for (int r = 0; r < 4; ++r) o[r] = rbf(acc[mi][ni][r]) * rbf(acc[mi][ni + 4][r]);
        u32x2 w;
        w[0] = pack2(o[0], o[1]);
        w[1] = pack2(o[2], o[3]);
        *(LAS u32x2*)(st + (mi * 16 + fr) * 144 + ni * 32 + fq * 8) = w;
      }
    u16* ob = p.out + (size_t)(m0 + wm * TM + (lane >> 3)) * F_ + tn * 128 + wn * 64 + (lane & 7) * 8;
#pragma unroll
    for (int i = 0; i < 8; ++i) {
      const u32x4 v = *(const LAS u32x4*)(st + (i * 8 + (lane >> 3)) * 144 + (lane & 7) * 16);
      ST16(ob + (size_t)i * 8 * F_, v);
    }
  }
  STAMP_END(MODE == 0 ? 2 : MODE == 1 ? 4 : MODE == 2 ? 6 : 7);
}

#define AT_NSTAGE 6
constexpr int AT_KT = 128, AT_NS = AT_NSTAGE, AT_ST = 16384;
#define MFMA32(a, b, c) __builtin_amdgcn_mfma_f32_32x32x16_bf16(a, b, c, 0, 0, 0)
#define AEXP(x) __builtin_amdgcn_exp2f(x)
#define MX3(a, b, c) __builtin_fmaxf(__builtin_fmaxf((a), (b)), (c))

__device__ __forceinline__ float tile_rowmax(const f32x16& s0, const f32x16& s1) {
  float a = MX3(s0[0], s0[1], s1[0]), b = MX3(s0[2], s0[3], s1[1]);
  a = MX3(a, s1[2], s1[3]);
#pragma unroll
  for (int r = 4; r < 16; r += 4) {
    a = MX3(a, s0[r], s0[r + 1]); b = MX3(b, s0[r + 2], s0[r + 3]);
    a = MX3(a, s1[r], s1[r + 1]); b = MX3(b, s1[r + 2], s1[r + 3]);
  }
  float mx = __builtin_fmaxf(a, b);
  auto rr = __builtin_amdgcn_permlane32_swap(__float_as_uint(mx), __float_as_uint(mx), false, false);
  return __builtin_fmaxf(__uint_as_float(rr[0]), __uint_as_float(rr[1]));
}

__global__ __launch_bounds__(512, 2) void attn_kernel(const u16* __restrict__ qkv, u16* __restrict__ o) {
  extern __shared__ __attribute__((aligned(16))) char smem[];
  STAMP_BEGIN();
  const int tid = threadIdx.x, lane = tid & 63;
  const int wid = __builtin_amdgcn_readfirstlane(tid >> 6);
  const int x = blockIdx.x & 7, idx = blockIdx.x >> 3;
  const int hd = x * 4 + (idx >> 3), qb = idx & 7;
  const int r32 = lane & 31, hi = lane >> 5;
  const u16* Q = qkv + ((size_t)(0 * 32 + hd) * S_) * 32;
  const u16* Kp = qkv + ((size_t)(1 * 32 + hd) * S_) * 32;
  const u16* Vp = qkv + ((size_t)(2 * 32 + hd) * S_) * 32;
  const int q0 = qb * 256 + wid * 32;

  const unsigned k_voff = (unsigned)(((lane >> 2) * 32 + 8 * ((lane & 3) ^ ((lane >> 4) & 3))) * 2);
  const unsigned v_voff = (unsigned)(lane * 16);
  const unsigned lds0 = (unsigned)(size_t)(LAS char*)smem;
  auto issue = [&](int t, int bufoff) {
    const char* gk = (const char*)(Kp + ((size_t)t * AT_KT + wid * 16) * 32);
    const char* gv = (const char*)(Vp + ((size_t)t * AT_KT + wid * 16) * 32);
    glds16(gk, k_voff, lds0 + bufoff + wid * 1024);
    glds16(gv, v_voff, lds0 + bufoff + 8192 + wid * 1024);
  };
#pragma unroll
  for (int s = 0; s < AT_NS - 1; ++s) issue(s, s * AT_ST);
  const bf16x8 qf0 = *(const bf16x8*)(Q + (size_t)(q0 + r32) * 32 + hi * 8);
  const bf16x8 qf1 = *(const bf16x8*)(Q + (size_t)(q0 + r32) * 32 + 16 + hi * 8);

  const int k_rd0 = r32 * 64 + ((hi ^ ((r32 >> 2) & 3)) << 4);
  const int k_rd1 = k_rd0 ^ 32;
  const int v_rd = 8192 + (4 * hi + ((lane & 15) >> 2)) * 64 + ((lane >> 4) & 1) * 32 + (lane & 3) * 8;

  f32x16 oacc = {};
  f32x16 negm;
  float m, l = 0.f;
  f32x16 sa0, sa1, sb0, sb1;
  u32x4 pp0 = {}, pp1 = {}, pp2 = {}, pp3 = {};
  bf16x8 vf0, vf1, vf2, vf3;
#define AT_BIG 1.0e30f
  constexpr float BIG = AT_BIG;
  constexpr int NT = S_ / AT_KT;

#define SCORES(D0, D1, kb, CIN)                                                         \
  do {                                                                                  \
    bf16x8 k00 = lds_read16((kb) + k_rd0), k01 = lds_read16((kb) + k_rd1);              \
    bf16x8 k10 = lds_read16((kb) + 2048 + k_rd0), k11 = lds_read16((kb) + 2048 + k_rd1);\
    D0 = MFMA32(k00, qf0, CIN); D1 = MFMA32(k10, qf0, CIN);                             \
    D0 = MFMA32(k01, qf1, D0);  D1 = MFMA32(k11, qf1, D1);                              \
  } while (0)
#define VFRAG(vb, s) __builtin_shufflevector(tr_read((vb) + (s) * 1024), tr_read((vb) + (s) * 1024 + 512), 0, 1, 2, 3, 4, 5, 6, 7)
#define READV(vb) do { vf0 = VFRAG(vb, 0); vf1 = VFRAG(vb, 1); vf2 = VFRAG(vb, 2); vf3 = VFRAG(vb, 3); } while (0)
#define PV()                                                                            \
  do {                                                                                  \
    oacc = MFMA32(vf0, __builtin_bit_cast(bf16x8, pp0), oacc);                          \
    oacc = MFMA32(vf1, __builtin_bit_cast(bf16x8, pp1), oacc);                          \
    oacc = MFMA32(vf2, __builtin_bit_cast(bf16x8, pp2), oacc);                          \
    oacc = MFMA32(vf3, __builtin_bit_cast(bf16x8, pp3), oacc);                          \
  } while (0)
#define EXPSUM(C0, C1, LS)                                                              \
  do {                                                                                  \
    f32x2 ls2_ = {0.f, 0.f}, ls3_ = {0.f, 0.f};                                         \
    _Pragma("unroll") for (int r = 0; r < 16; r += 2) {                                 \
      C0[r] = AEXP(C0[r]); C0[r + 1] = AEXP(C0[r + 1]);                                 \
      C1[r] = AEXP(C1[r]); C1[r + 1] = AEXP(C1[r + 1]);                                 \
      ls2_ += (f32x2){C0[r], C0[r + 1]};                                                \
      ls3_ += (f32x2){C1[r], C1[r + 1]};                                                \
    }                                                                                   \
    ls2_ += ls3_;                                                                       \
    LS = ls2_[0] + ls2_[1];                                                             \
  } while (0)
#define PACK(C0, C1)                                                                    \
  do {                                                                                  \
    _Pragma("unroll") for (int j = 0; j < 4; ++j) {                                     \
      pp0[j] = pack2(C0[2 * j], C0[2 * j + 1]);                                         \
      pp1[j] = pack2(C0[8 + 2 * j], C0[8 + 2 * j + 1]);                                 \
      pp2[j] = pack2(C1[2 * j], C1[2 * j + 1]);                                         \
      pp3[j] = pack2(C1[8 + 2 * j], C1[8 + 2 * j + 1]);                                 \
    }                                                                                   \
  } while (0)
#define STEP(C0, C1, N0, N1, kcur, knext, vprev, HASN)                                  \
  do {                                                                                  \
    READV(vprev);                                                                       \
    if (HASN) SCORES(N0, N1, knext, negm);                                              \
    PV();                                                                               \
    float ls;                                                                           \
    EXPSUM(C0, C1, ls);                                                                 \
    if (__builtin_expect(__any(!(ls <= BIG)), 0)) {                                     \
                  \
      f32x16 z_ = {};                                                                   \
      SCORES(C0, C1, kcur, z_);                                                         \
      const float mn = __builtin_fmaxf(m, tile_rowmax(C0, C1));                         \
      const float dm = m - mn;                                                          \
      const float f = __builtin_amdgcn_exp2f(dm);                                       \
      m = mn; l *= f;                                                                   \
      _Pragma("unroll") for (int r = 0; r < 16; ++r) {                                  \
        oacc[r] *= f; negm[r] = -mn; C0[r] -= mn; C1[r] -= mn;                          \
        if (HASN) { N0[r] += dm; N1[r] += dm; }                                         \
      }                                                                                 \
      EXPSUM(C0, C1, ls);                                                               \
    }                                                                                   \
    l += ls;                                                                            \
    PACK(C0, C1);                                                                       \
  } while (0)

  WAIT_V(2 * (AT_NS - 2));
  BAR();
  {
    f32x16 z = {};
    SCORES(sa0, sa1, smem, z);
    m = tile_rowmax(sa0, sa1);
#pragma unroll
    for (int r = 0; r < 16; ++r) { negm[r] = -m; sa0[r] -= m; sa1[r] -= m; }
  }
  int cur = 0, prv = 0;
  for (int t = 0; t < NT - 1; ++t) {
    const char* sb = smem + cur;
    int nx = cur + AT_ST; if (nx >= AT_NS * AT_ST) nx = 0;
    int prv2 = cur - AT_ST; if (prv2 < 0) prv2 = (AT_NS - 1) * AT_ST;
    STEP(sa0, sa1, sb0, sb1, sb, sb + 4096, smem + prv + 4096 + v_rd, true);
    if (t + AT_NS - 1 < NT) {
      WAIT_V(2 * (AT_NS - 3));
      WAIT_L0();
      BAR();
      issue(t + AT_NS - 1, prv2);
    } else {
      WAIT_V(0);
      WAIT_L0();
      BAR();
    }
    STEP(sb0, sb1, sa0, sa1, sb + 4096, smem + nx, sb + v_rd, true);
    prv = cur; cur = nx;
  }
  {
    const char* sb = smem + cur;
    STEP(sa0, sa1, sb0, sb1, sb, sb + 4096, smem + prv + 4096 + v_rd, true);
    STEP(sb0, sb1, sa0, sa1, sb + 4096, sb, sb + v_rd, false);
    prv = cur;
  }
  READV(smem + prv + 4096 + v_rd);
  PV();
#undef SCORES
#undef VFRAG
#undef READV
#undef PV
#undef EXPSUM
#undef PACK
#undef STEP
  {
    auto rr = __builtin_amdgcn_permlane32_swap(__float_as_uint(l), __float_as_uint(l), false, false);
    l = __uint_as_float(rr[0]) + __uint_as_float(rr[1]);
  }
  const float inv = 1.0f / l;
  u16* op = o + (size_t)(q0 + r32) * D_ + hd * 32 + 4 * hi;
#pragma unroll
  for (int g = 0; g < 4; ++g) {
    u32x2 w;
    w[0] = pack2(oacc[4 * g] * inv, oacc[4 * g + 1] * inv);
    w[1] = pack2(oacc[4 * g + 2] * inv, oacc[4 * g + 3] * inv);
    *(u32x2*)(op + 8 * g) = w;
  }
  STAMP_END(3);
}

constexpr int A2_KT = 256, A2_NS = 3, A2_ST = 32768;
template <bool SAFE>
__device__ __forceinline__ void attn_pass(char* smem, const u16* Kp, const u16* Vp, bf16x8 qf0, bf16x8 qf1, int wid, int lane,
                                          f32x16& oacc, float& m, float& l,
                                          const u16* pw0, const u16* pw1, const u16* pw2, const u16* pw3) {
  const int wk = wid >> 3, r32 = lane & 31, hi = lane >> 5;
  const unsigned k_voff = (unsigned)(((lane >> 2) * 32 + 8 * ((lane & 3) ^ ((lane >> 4) & 3))) * 2);
  const unsigned v_voff = (unsigned)(lane * 16);
  const unsigned lds0 = (unsigned)(size_t)(LAS char*)smem;
  auto issue = [&](int t, int bufoff) {
    const char* gk = (const char*)(Kp + ((size_t)t * A2_KT + wid * 16) * 32);
    const char* gv = (const char*)(Vp + ((size_t)t * A2_KT + wid * 16) * 32);
    glds16(gk, k_voff, lds0 + bufoff + wid * 1024);
    glds16(gv, v_voff, lds0 + bufoff + 16384 + wid * 1024);
  };
  issue(0, 0);
  issue(1, A2_ST);
  const int k_rd0 = wk * 8192 + r32 * 64 + ((hi ^ ((r32 >> 2) & 3)) << 4);
  const int k_rd1 = k_rd0 ^ 32;
  const int v_rd = 16384 + wk * 8192 + (4 * hi + ((lane & 15) >> 2)) * 64 + ((lane >> 4) & 1) * 32 + (lane & 3) * 8;
  f32x16 negm = {};
  f32x2 lsa = {0.f, 0.f}, lsb = {0.f, 0.f};
  oacc = (f32x16){};
  m = 0.f; l = 0.f;
  constexpr int NT = S_ / A2_KT;
  int cur = 0;
  for (int t = 0; t < NT; ++t) {
    if (t == 0) WAIT_V(2); else if (t + 1 < NT) WAIT_V(3); else WAIT_V(0);
    BAR();
    {
      int ck = (blockIdx.x * NT + t) * 16 + wid;
      if (ck >= 2048 + 3 * 7168) ck -= 16384;
      const u16* pb = pw0;
      if (ck >= 2048) { pb = pw1; ck -= 2048; if (ck >= 7168) { pb = pw2; ck -= 7168; if (ck >= 7168) { pb = pw3; ck -= 7168; } } }
      glds16((const char*)pb + (size_t)ck * 1024, v_voff, lds0 + A2_NS * A2_ST);
      int nb = cur + 2 * A2_ST; if (nb >= A2_NS * A2_ST) nb -= A2_NS * A2_ST;
      if (t + 2 < NT) issue(t + 2, nb);
    }
#pragma unroll
    for (int hb = 0; hb < 2; ++hb) {
      const char* kb = smem + cur + hb * 4096;
      const bf16x8 k00 = lds_read16(kb + k_rd0), k01 = lds_read16(kb + k_rd1);
      const bf16x8 k10 = lds_read16(kb + 2048 + k_rd0), k11 = lds_read16(kb + 2048 + k_rd1);
      f32x16 s0, s1;
      if (SAFE || (t == 0 && hb == 0)) {
        f32x16 z_ = {};
        s0 = MFMA32(k00, qf0, z_); s1 = MFMA32(k10, qf0, z_);
        s0 = MFMA32(k01, qf1, s0); s1 = MFMA32(k11, qf1, s1);
        const float tm = tile_rowmax(s0, s1);
        const bool first = (t == 0 && hb == 0);
        const float mn = first ? tm : __builtin_fmaxf(m, tm);
        if (SAFE) {
          const float f = first ? 0.f : __builtin_amdgcn_exp2f(m - mn);
          lsa *= f; lsb *= f;
          oacc *= f;
        }
        m = mn;
#pragma unroll
        for (int r = 0; r < 16; ++r) { negm[r] = -mn; s0[r] -= mn; s1[r] -= mn; }
      } else {
        s0 = MFMA32(k00, qf0, negm); s1 = MFMA32(k10, qf0, negm);
        s0 = MFMA32(k01, qf1, s0); s1 = MFMA32(k11, qf1, s1);
      }
#pragma unroll
      for (int r = 0; r < 16; r += 2) {
        s0[r] = AEXP(s0[r]); s0[r + 1] = AEXP(s0[r + 1]);
        s1[r] = AEXP(s1[r]); s1[r + 1] = AEXP(s1[r + 1]);
        lsa += (f32x2){s0[r], s0[r + 1]};
        lsb += (f32x2){s1[r], s1[r + 1]};
      }
      u32x4 p0, p1, p2, p3;
#pragma unroll
      for (int j = 0; j < 4; ++j) {
        p0[j] = pack2(s0[2 * j], s0[2 * j + 1]);
        p1[j] = pack2(s0[8 + 2 * j], s0[8 + 2 * j + 1]);
        p2[j] = pack2(s1[2 * j], s1[2 * j + 1]);
        p3[j] = pack2(s1[8 + 2 * j], s1[8 + 2 * j + 1]);
      }
      const char* vb = smem + cur + hb * 4096 + v_rd;
#define VFRAG(s) __builtin_shufflevector(tr_read(vb + (s) * 1024), tr_read(vb + (s) * 1024 + 512), 0, 1, 2, 3, 4, 5, 6, 7)
      oacc = MFMA32(VFRAG(0), __builtin_bit_cast(bf16x8, p0), oacc);
      oacc = MFMA32(VFRAG(1), __builtin_bit_cast(bf16x8, p1), oacc);
      oacc = MFMA32(VFRAG(2), __builtin_bit_cast(bf16x8, p2), oacc);
      oacc = MFMA32(VFRAG(3), __builtin_bit_cast(bf16x8, p3), oacc);
#undef VFRAG
    }
    WAIT_L0();
    cur += A2_ST; if (cur >= A2_NS * A2_ST) cur = 0;
  }
  lsa += lsb;
  l = lsa[0] + lsa[1];
  {
    auto rr = __builtin_amdgcn_permlane32_swap(__float_as_uint(l), __float_as_uint(l), false, false);
    l = __uint_as_float(rr[0]) + __uint_as_float(rr[1]);
  }
  WAIT_V(0);
}

__global__ __launch_bounds__(1024, 4) void attn2_kernel(const u16* __restrict__ qkv, u16* __restrict__ o,
                                                        const u16* pw0, const u16* pw1, const u16* pw2, const u16* pw3) {
  extern __shared__ __attribute__((aligned(16))) char smem[];
  STAMP_BEGIN();
  const int tid = threadIdx.x, lane = tid & 63;
  const int wid = __builtin_amdgcn_readfirstlane(tid >> 6);
  const int wq = wid & 7, wk = wid >> 3;
  const int x = blockIdx.x & 7, idx = blockIdx.x >> 3;
  const int hd = x * 4 + (idx >> 3), qb = idx & 7;
  const int r32 = lane & 31, hi = lane >> 5;
  const u16* Q = qkv + ((size_t)(0 * 32 + hd) * S_) * 32;
  const u16* Kp = qkv + ((size_t)(1 * 32 + hd) * S_) * 32;
  const u16* Vp = qkv + ((size_t)(2 * 32 + hd) * S_) * 32;
  const int q0 = qb * 256 + wq * 32;
  const bf16x8 qf0 = *(const bf16x8*)(Q + (size_t)(q0 + r32) * 32 + hi * 8);
  const bf16x8 qf1 = *(const bf16x8*)(Q + (size_t)(q0 + r32) * 32 + 16 + hi * 8);

  f32x16 oacc;
  float m, l;
  attn_pass<false>(smem, Kp, Vp, qf0, qf1, wid, lane, oacc, m, l, pw0, pw1, pw2, pw3);
  {
    float chk = l;
#pragma unroll
    for (int r = 0; r < 16; ++r) chk += __builtin_fabsf(oacc[r]);
    const int bad = !(chk < 1.0e37f);
    if (__syncthreads_or(bad)) attn_pass<true>(smem, Kp, Vp, qf0, qf1, wid, lane, oacc, m, l, pw0, pw1, pw2, pw3);
  }
  __syncthreads();
  float* xb = (float*)smem + wq * (18 * 64);
  if (wk == 1) {
    xb[lane] = m; xb[64 + lane] = l;
#pragma unroll
    for (int r = 0; r < 16; ++r) xb[(2 + r) * 64 + lane] = oacc[r];
  }
  __syncthreads();
  if (wk == 0) {
    const float m2 = xb[lane], l2 = xb[64 + lane];
    const float mn = __builtin_fmaxf(m, m2);
    const float e1 = (m - mn) * 0.5f, e2 = (m2 - mn) * 0.5f;
    const float f1 = __builtin_amdgcn_exp2f(e1), f2 = __builtin_amdgcn_exp2f(e2);
    const float lt = (l * f1) * f1 + (l2 * f2) * f2;
    const float inv = 1.0f / lt;
    u16* op = o + (size_t)(q0 + r32) * D_ + hd * 32 + 4 * hi;
#pragma unroll
    for (int g = 0; g < 4; ++g) {
      float v[4];
#pragma unroll
      for (int e = 0; e < 4; ++e) v[e] = ((oacc[4 * g + e] * f1) * f1 + (xb[(2 + 4 * g + e) * 64 + lane] * f2) * f2) * inv;
      u32x2 w;
      w[0] = pack2(v[0], v[1]);
      w[1] = pack2(v[2], v[3]);
      *(u32x2*)(op + 8 * g) = w;
    }
  }
  STAMP_END(3);
}

template <int MODE> static void launch_gemm(const GemmP& p, hipStream_t stream) {
  using C = GCfg<MODE>;
  constexpr int lds = C::NS * (C::BM + C::BN) * 128;
  hipFuncSetAttribute((const void*)gemm_kernel<MODE>, hipFuncAttributeMaxDynamicSharedMemorySize, lds);
  gemm_kernel<MODE><<<C::NTM * C::NTN, C::WM * C::WN * 64 * (1 + C::LW), lds, stream>>>(p);
}

extern "C" void kernel_launch(void* const* d_in, const int* in_sizes, int n_in, void* d_out, int out_size,
                              void* d_ws, size_t ws_size, hipStream_t stream) {
  const u16* x = (const u16*)d_in[0];
  const u16* wqkv = (const u16*)d_in[1];
  const u16* wo = (const u16*)d_in[2];
  const u16* wg = (const u16*)d_in[4];
  const u16* wu = (const u16*)d_in[5];
  const u16* wd = (const u16*)d_in[6];
  const u16* c = (const u16*)d_in[7];
  const u16* wmod = (const u16*)d_in[8];
  u16* out = (u16*)d_out;

  char* ws = (char*)d_ws;
  float* mod = (float*)ws;
  u16* h = (u16*)(ws + (1 << 20));
  u16* qkv = (u16*)(ws + (8 << 20));
  u16* ao = (u16*)(ws + (24 << 20));
  u16* x1 = (u16*)(ws + (32 << 20));
  u16* hh = (u16*)(ws + (40 << 20));
  float* slab = (float*)(ws + (64 << 20));
  unsigned* flags = (unsigned*)(ws + (512 << 10));

  mod_kernel<<<256, 256, 0, stream>>>(c, wmod, mod, x, flags);
  ln_kernel<<<256 + 256 + 192, 256, 0, stream>>>(x, mod, 0, h, c, wmod, mod, wqkv, 1);
  { GemmP p{h, wqkv, nullptr, nullptr, nullptr, qkv, nullptr, nullptr}; launch_gemm<0>(p, stream); }
  hipFuncSetAttribute((const void*)attn2_kernel, hipFuncAttributeMaxDynamicSharedMemorySize, A2_NS * A2_ST + 1024);
  attn2_kernel<<<256, 1024, A2_NS * A2_ST + 1024, stream>>>(qkv, ao, wo, wg, wu, wd);
  { GemmP p{ao, wo, nullptr, x, mod + 2 * 1024, x1, nullptr, nullptr}; launch_gemm<1>(p, stream); }
  ln_kernel<<<256, 256, 0, stream>>>(x1, mod, 3 * 1024, h, c, wmod, mod, wqkv, 5);
  { GemmP p{h, wg, wu, nullptr, nullptr, hh, nullptr, nullptr}; launch_gemm<2>(p, stream); }
  { GemmP p{hh, wd, nullptr, x1, mod + 5 * 1024, out, slab, flags}; launch_gemm<3>(p, stream); }
}
```

```cpp
#include <hip/hip_runtime.h>
#include <hip/hip_bf16.h>
#include <stdint.h>

typedef unsigned short u16;
typedef short bf16x8 __attribute__((ext_vector_type(8)));
typedef short s16x4 __attribute__((ext_vector_type(4)));
typedef float f32x2 __attribute__((ext_vector_type(2)));
typedef float f32x4 __attribute__((ext_vector_type(4)));
typedef float f32x16 __attribute__((ext_vector_type(16)));
typedef unsigned u32x2 __attribute__((ext_vector_type(2)));
typedef unsigned u32x4 __attribute__((ext_vector_type(4)));
typedef __bf16 bf16x2_t __attribute__((ext_vector_type(2)));
#define LAS __attribute__((address_space(3)))
#define GAS __attribute__((address_space(1)))

constexpr int S_ = 2048, D_ = 1024, H_ = 32, DH_ = 32, F_ = 3584;

__device__ __forceinline__ float bf2f(u16 u) { return __uint_as_float((unsigned)u << 16); }
__device__ __forceinline__ unsigned pack2(float lo, float hi) {
  f32x2 v = {lo, hi};
  bf16x2_t b = __builtin_convertvector(v, bf16x2_t);
  return __builtin_bit_cast(unsigned, b);
}
__device__ __forceinline__ float rbf(float f) {
  __bf16 b = (__bf16)f;
  return __uint_as_float((unsigned)__builtin_bit_cast(u16, b) << 16);
}
__device__ __forceinline__ float lo_f(unsigned u) { return __uint_as_float(u << 16); }
__device__ __forceinline__ float hi_f(unsigned u) { return __uint_as_float(u & 0xffff0000u); }

#define STAMP_BEGIN()
#define STAMP_END(kid)
#define STAMP_MID(kid, i)
#define WAIT_V(n) asm volatile("s_waitcnt vmcnt(%0)" ::"n"(n) : "memory")
#define WAIT_L0() asm volatile("s_waitcnt lgkmcnt(0)" ::: "memory")
#define BAR() __builtin_amdgcn_s_barrier()

__device__ __forceinline__ void glds16(const void* sbase, unsigned voff, unsigned ldsaddr) {
  asm volatile("s_mov_b32 m0, %2\n\ts_nop 0\n\tglobal_load_lds_dwordx4 %0, %1" ::"v"(voff), "s"(sbase), "s"(ldsaddr) : "memory");
}
#define MFMA16(c, a, b) c = __builtin_amdgcn_mfma_f32_16x16x32_bf16(a, b, c, 0, 0, 0)
#define ST16(p, v) asm volatile("global_store_dwordx4 %0, %1, off sc0 sc1\n\ts_nop 1" ::"v"(p), "v"(v) : "memory")
#define SBAR() __builtin_amdgcn_sched_barrier(0)
#define SGB(mask, n) __builtin_amdgcn_sched_group_barrier(mask, n, 0)
template <int I> struct IC { static constexpr int value = I; };
template <int I, int N, class F> __device__ __forceinline__ void sfor(F&& f) {
  if constexpr (I < N) { f(IC<I>{}); sfor<I + 1, N>(f); }
}
__device__ __forceinline__ s16x4 tr_read(const char* p) {
  return __builtin_amdgcn_ds_read_tr16_b64_v4i16((LAS s16x4*)p);
}
__device__ __forceinline__ bf16x8 lds_read16(const char* p) { return *(const LAS bf16x8*)p; }

__device__ __forceinline__ void mod_block(const u16* __restrict__ c, const u16* __restrict__ wmod, float* __restrict__ modp,
                                          int cg, int ks, float* sh) {
  const int tid = threadIdx.x;
  const int cl = tid & 7, rg = tid >> 3;
  const int col0 = cg * 64 + cl * 8;
  u32x4 w[8];
#pragma unroll
  for (int i = 0; i < 8; ++i)
    w[i] = __builtin_nontemporal_load((const u32x4*)(wmod + (size_t)(ks * 256 + rg + i * 32) * 6144 + col0));
  {
    const float v = bf2f(c[ks * 256 + tid]);
    sh[tid] = rbf(v / (1.0f + __expf(-v)));
  }
  __syncthreads();
  float acc[8];
#pragma unroll
  for (int j = 0; j < 8; ++j) acc[j] = 0.f;
#pragma unroll
  for (int i = 0; i < 8; ++i) {
    const float s = sh[rg + i * 32];
#pragma unroll
    for (int j = 0; j < 4; ++j) {
      acc[2 * j] += s * lo_f(w[i][j]);
      acc[2 * j + 1] += s * hi_f(w[i][j]);
    }
  }
  float* red = sh + 256;
#pragma unroll
  for (int j = 0; j < 8; ++j) red[rg * 65 + cl * 8 + j] = acc[j];
  __syncthreads();
  if (tid < 64) {
    float s = 0.f;
#pragma unroll 8
    for (int r = 0; r < 32; ++r) s += red[r * 65 + tid];
    modp[ks * 6144 + cg * 64 + tid] = s;
  }
}
__device__ __forceinline__ void touch_block(const char* src) {
  u32x4 v[8];
#pragma unroll
  for (int i = 0; i < 8; ++i) v[i] = *(const u32x4*)(src + i * 4096 + threadIdx.x * 16);
#pragma unroll
  for (int i = 0; i < 8; ++i) asm volatile("" ::"v"(v[i]));
}
__device__ __forceinline__ f32x4 ld_mod4(const float* modp, int idx) {
  f32x4 a = *(const f32x4*)(modp + idx), b = *(const f32x4*)(modp + 6144 + idx);
  f32x4 c = *(const f32x4*)(modp + 2 * 6144 + idx), d = *(const f32x4*)(modp + 3 * 6144 + idx);
  f32x4 s = (a + b) + (c + d);
  return (f32x4){rbf(s[0]), rbf(s[1]), rbf(s[2]), rbf(s[3])};
}

__global__ __launch_bounds__(256) void mod_kernel(const u16* __restrict__ c, const u16* __restrict__ wmod,
                                                  float* __restrict__ modp, const u16* __restrict__ pf0,
                                                  const u16* __restrict__ pf1, unsigned* flags) {
  __shared__ float sh[256 + 32 * 65];
  STAMP_BEGIN();
  const int b = blockIdx.x;
  if (threadIdx.x == 0 && b < 256) flags[b * 32] = 0u;
  if (b < 128) mod_block(c, wmod, modp, b >> 2, b & 3, sh);
  else if (b < 256) touch_block((const char*)pf0 + (size_t)(b - 128) * 32768);
  else touch_block((const char*)pf1 + (size_t)(b - 256) * 32768);
  STAMP_END(0);
}

__global__ __launch_bounds__(256) void ln_kernel(const u16* __restrict__ x, const float* modp, int moff, u16* __restrict__ h, int kid) {
  __shared__ float sh[2048];
  STAMP_BEGIN();
  const int tid = threadIdx.x;
  const int lane = tid & 63, wid = tid >> 6;
  const int row = blockIdx.x * 4 + wid;
  const u16* xr = x + (size_t)row * D_;
  const u32x4 v0 = *(const u32x4*)(xr + lane * 8);
  const u32x4 v1 = *(const u32x4*)(xr + 512 + lane * 8);
  {
    const f32x4 s0 = ld_mod4(modp, moff + tid * 4);
    const f32x4 s1 = ld_mod4(modp, moff + 1024 + tid * 4);
    *(f32x4*)(sh + tid * 4) = s0;
    *(f32x4*)(sh + 1024 + tid * 4) = (f32x4){rbf(1.0f + s1[0]), rbf(1.0f + s1[1]), rbf(1.0f + s1[2]), rbf(1.0f + s1[3])};
  }
  float f[16];
#pragma unroll
  for (int j = 0; j < 4; ++j) {
    f[2 * j] = lo_f(v0[j]); f[2 * j + 1] = hi_f(v0[j]);
    f[8 + 2 * j] = lo_f(v1[j]); f[8 + 2 * j + 1] = hi_f(v1[j]);
  }
  float s = 0.f;
#pragma unroll
  for (int j = 0; j < 16; ++j) s += f[j];
#pragma unroll
  for (int o = 32; o >= 1; o >>= 1) s += __shfl_xor(s, o);
  const float mean = s * (1.0f / D_);
  float q = 0.f;
#pragma unroll
  for (int j = 0; j < 16; ++j) { float d = f[j] - mean; q += d * d; }
#pragma unroll
  for (int o = 32; o >= 1; o >>= 1) q += __shfl_xor(q, o);
  const float mb = rbf(mean);
  const float vb = rbf(q * (1.0f / D_));
  const float sd = rbf(sqrtf(rbf(vb + 1e-5f)));
  const float inv = 1.0f / sd;
  __syncthreads();
  float o[16];
#pragma unroll
  for (int half = 0; half < 2; ++half) {
    const int c0 = half * 512 + lane * 8;
    const f32x4 sa0 = *(const f32x4*)(sh + c0), sa1 = *(const f32x4*)(sh + c0 + 4);
    const f32x4 ca0 = *(const f32x4*)(sh + 1024 + c0), ca1 = *(const f32x4*)(sh + 1024 + c0 + 4);
#pragma unroll
    for (int j = 0; j < 8; ++j) {
      const float shf = j < 4 ? sa0[j & 3] : sa1[j & 3];
      const float sc = j < 4 ? ca0[j & 3] : ca1[j & 3];
      float t = rbf(f[half * 8 + j] - mb);
      t = rbf(t * inv);
      t = rbf(t * sc);
      o[half * 8 + j] = t + shf;
    }
  }
  u16* hr = h + (size_t)row * D_;
  u32x4 w0, w1;
#pragma unroll
  for (int j = 0; j < 4; ++j) {
    w0[j] = pack2(o[2 * j], o[2 * j + 1]);
    w1[j] = pack2(o[8 + 2 * j], o[8 + 2 * j + 1]);
  }
  ST16(hr + lane * 8, w0);
  ST16(hr + 512 + lane * 8, w1);
  STAMP_END(kid);
}

struct GemmP {
  const u16* A; const u16* B0; const u16* B1; const u16* res; const float* gate; u16* out; float* slab; unsigned* flags;
};

#define NS0 3
#define NS1 4
#define NS3 4
#define LW0 1
#define LW1 1
#define LW3 1
template <int MODE> struct GCfg;
template <> struct GCfg<0> { static constexpr int BM = 128, BN = 192, WM = 2, WN = 2, NS = NS0, K = 1024, LDA = 1024, LDB = 3072, NTM = 16, NTN = 16, RM = 8, RN = 4, LW = LW0; };
template <> struct GCfg<1> { static constexpr int BM = 128, BN = 64, WM = 2, WN = 2, NS = NS1, K = 1024, LDA = 1024, LDB = 1024, NTM = 16, NTN = 16, RM = 4, RN = 8, LW = LW1; };
template <> struct GCfg<2> { static constexpr int BM = 256, BN = 256, WM = 4, WN = 2, NS = 2, K = 1024, LDA = 1024, LDB = 3584, NTM = 8, NTN = 28, RM = 4, RN = 7, LW = 0; };
template <> struct GCfg<3> { static constexpr int BM = 128, BN = 128, WM = 2, WN = 2, NS = NS3, K = 1792, LDA = 3584, LDB = 1024, NTM = 16, NTN = 16, RM = 4, RN = 8, LW = LW3; };

template <> struct GCfg<4> { static constexpr int BM = 128, BN = 64, WM = 2, WN = 2, NS = NS3, K = 3584, LDA = 3584, LDB = 1024, NTM = 16, NTN = 16, RM = 4, RN = 8, LW = LW3; };

template <int MODE>
__global__ __launch_bounds__(GCfg<MODE>::WM* GCfg<MODE>::WN * 64 * (GCfg<MODE>::LW ? 2 : 1), 2) void gemm_kernel(GemmP p) {
  using C = GCfg<MODE>;
  constexpr int BM = C::BM, BN = C::BN, WM = C::WM, WN = C::WN, NS = C::NS, NW = WM * WN;
  constexpr int TM = BM / WM, TN = BN / WN, MI = TM / 16, NI = TN / 16;
  constexpr int A_BYTES = BM * 128, B_BYTES = BN * 128, ST_BYTES = A_BYTES + B_BYTES;
  constexpr int GA = BM / 8 / NW, GB = BN / 8 / NW, G = GA + GB;
  constexpr int NT = C::K / 64;
  static_assert(BM % (8 * NW) == 0 && BN % (8 * NW) == 0, "stage split");
  static_assert(NT >= NS + 1, "K too small");
  extern __shared__ __attribute__((aligned(16))) char smem[];
  STAMP_BEGIN();

  const int tid = threadIdx.x, lane = tid & 63;
  constexpr bool LW = C::LW != 0;
  const int wid_all = __builtin_amdgcn_readfirstlane(tid >> 6);
  const int wid = wid_all % NW;
  const int wm = wid / WN, wn = wid % WN;
  int tm, tn;
  {
    const int x = blockIdx.x & 7, idx = blockIdx.x >> 3;
    constexpr int NRM = C::NTM / C::RM;
    const int rm = x % NRM, rn = x / NRM;
    tm = rm * C::RM + idx % C::RM;
    tn = rn * C::RN + idx / C::RM;
  }
  int kh = 0;
  if constexpr (MODE == 3) { kh = tn & 1; tn >>= 1; }
  const int m0 = tm * BM;

  const int par = wid & 1;
  const unsigned a_voff = (unsigned)(((lane >> 3) * C::LDA + 8 * ((lane & 7) ^ ((4 * par + (lane >> 4)) & 7))) * 2);
  const unsigned b_voff = (unsigned)(((lane >> 3) * C::LDB + 8 * ((lane & 7) ^ ((((lane >> 4) & 1) | (par << 1)) << 1))) * 2);
  const char* Abase = (const char*)(p.A + (size_t)m0 * C::LDA + kh * C::K);
  const unsigned lds0 = (unsigned)(size_t)(LAS char*)smem;

  auto issue1 = [&](auto gi, int t, int bufoff) {
    constexpr int g = decltype(gi)::value;
    if constexpr (g < GA) {
      const int i = wid + NW * g;
      const char* gp = Abase + ((size_t)(8 * i) * C::LDA + (size_t)t * 64) * 2;
      glds16(gp, a_voff, lds0 + bufoff + i * 1024);
    } else {
      const int i = wid + NW * (g - GA);
      const int pn = i >> 3, kr = 8 * (i & 7);
      const u16* bp;
      if constexpr (MODE == 2) bp = (pn < 2 ? p.B0 : p.B1) + tn * 128 + (pn & 1) * 64;
      else bp = p.B0 + (size_t)kh * C::K * C::LDB + tn * BN + pn * 64;
      const char* gp = (const char*)(bp + ((size_t)t * 64 + kr) * C::LDB);
      glds16(gp, b_voff, lds0 + bufoff + A_BYTES + i * 1024);
    }
  };

  if constexpr (C::LW == 1) {
    if (wid_all >= NW) {
      sfor<0, NS>([&](auto sc_) { sfor<0, G>([&](auto gc) { issue1(gc, decltype(sc_)::value, decltype(sc_)::value * ST_BYTES); }); });
      WAIT_V((NS - 1) * G);
      BAR();
      int lb = 0;
      for (int t = 0; t < NT - NS; ++t) {
        WAIT_V((NS - 2) * G);
        BAR();
        sfor<0, G>([&](auto gc) { issue1(gc, t + NS, lb); });
        lb = (lb + ST_BYTES == NS * ST_BYTES) ? 0 : lb + ST_BYTES;
      }
      sfor<0, NS - 1>([&](auto rc) { WAIT_V((NS - 2 - decltype(rc)::value) * G); BAR(); });
      return;
    }
  }
  if constexpr (C::LW == 2) {
    if (wid_all >= NW) {
      static_assert(NS == 3 && (NT & 1) == 0, "reg loader");
      u32x4 R0[G], R1[G];
      auto ld = [&](u32x4* R, int t) {
        sfor<0, G>([&](auto gi) {
          constexpr int g = decltype(gi)::value;
          if constexpr (g < GA) {
            const int i = wid + NW * g;
            R[g] = *(const u32x4*)(Abase + ((size_t)(8 * i) * C::LDA + (size_t)t * 64) * 2 + a_voff);
          } else {
            const int i = wid + NW * (g - GA);
            const int pn = i >> 3, kr = 8 * (i & 7);
            const u16* bp = p.B0 + (size_t)kh * C::K * C::LDB + tn * BN + pn * 64;
            R[g] = *(const u32x4*)((const char*)(bp + ((size_t)t * 64 + kr) * C::LDB) + b_voff);
          }
        });
      };
      auto st = [&](const u32x4* R, int bufoff) {
        sfor<0, G>([&](auto gi) {
          constexpr int g = decltype(gi)::value;
          const int off = g < GA ? (wid + NW * g) * 1024 : A_BYTES + (wid + NW * (g - GA)) * 1024;
          *(LAS u32x4*)(smem + bufoff + off + lane * 16) = R[g];
        });
      };
      ld(R0, 0); ld(R1, 1);
      st(R0, 0); ld(R0, 2);
      WAIT_L0(); BAR();
      st(R1, ST_BYTES); ld(R1, 3);
      st(R0, 2 * ST_BYTES); ld(R0, 4);
      int lb = 0;
      for (int t = 0; t < NT - 4; t += 2) {
        WAIT_L0(); BAR();
        st(R1, lb); if (t + 5 < NT) ld(R1, t + 5);
        lb = (lb + ST_BYTES == NS * ST_BYTES) ? 0 : lb + ST_BYTES;
        WAIT_L0(); BAR();
        st(R0, lb); if (t + 6 < NT) ld(R0, t + 6);
        lb = (lb + ST_BYTES == NS * ST_BYTES) ? 0 : lb + ST_BYTES;
      }
      WAIT_L0(); BAR();
      st(R1, lb);
      WAIT_L0(); BAR();
      BAR();
      return;
    }
  }

  const int fr = lane & 15, fq = lane >> 4;
  u32x4 rv[4];
  f32x4 gv[NI];
  if constexpr (MODE == 1 || MODE == 3 || MODE == 4) {
#pragma unroll
    for (int i = 0; i < 4; ++i) {
      if constexpr (MODE != 3) rv[i] = *(const u32x4*)(p.res + (size_t)(m0 + i * 32 + (tid >> 3)) * D_ + tn * BN + (tid & 7) * 8);
      else rv[i] = *(const u32x4*)(p.res + (size_t)(m0 + kh * 64 + i * 16 + (tid >> 4)) * D_ + tn * BN + (tid & 15) * 8);
    }
#pragma unroll
    for (int ni = 0; ni < NI; ++ni) gv[ni] = ld_mod4(p.gate, tn * BN + wn * TN + ni * 16 + 4 * fq);
  }
  const int a_rd0 = (wm * TM + fr) * 128 + ((fq ^ (fr >> 1)) << 4);
  const int a_rd1 = a_rd0 ^ 64;
  int b_rd[(MODE == 2) ? 4 : NI];
  {
    const int q = (lane & 15) >> 2, pp = lane & 3;
    const int gl = ((q >> 1) & 1) | ((fq & 1) << 1);
#pragma unroll
    for (int ni = 0; ni < ((MODE == 2) ? 4 : NI); ++ni) {
      int pn, un;
      if constexpr (MODE == 2) { pn = wn; un = ni & 3; }
      else { const int j = wn * NI + ni; pn = j >> 2; un = j & 3; }
      b_rd[ni] = A_BYTES + pn * 8192 + (8 * fq + q) * 128 + ((un ^ gl) << 5) + pp * 8;
    }
  }

  f32x4 acc[MI][NI];
#pragma unroll
  for (int mi = 0; mi < MI; ++mi)
#pragma unroll
    for (int ni = 0; ni < NI; ++ni) acc[mi][ni] = (f32x4){0.f, 0.f, 0.f, 0.f};

  constexpr int NSP = (MODE == 2) ? 2 : 1;
  constexpr int NH = NI / NSP, NM = MI * NH, NPH = 2 * NSP;
  constexpr int GW = (NS >= 3) ? NPH : NPH / 2;
  constexpr int GPP = (G + GW - 1) / GW;
  bf16x8 a0[MI], a1[MI], b0[NH], b1[NH];
  s16x4 blo[NH];

  auto rd_b = [&](auto ri, bf16x8* BF, const char* sb, int ks, int hf) {
    constexpr int r = decltype(ri)::value, ni = r >> 1;
    const int bo = (MODE == 2) ? b_rd[ni] + hf * 16384 : b_rd[ni];
    if constexpr ((r & 1) == 0) blo[ni] = tr_read(sb + bo + ks * 4096);
    else BF[ni] = __builtin_shufflevector(blo[ni], tr_read(sb + bo + ks * 4096 + 512), 0, 1, 2, 3, 4, 5, 6, 7);
  };
  auto rd_a = [&](auto ri, bf16x8* AF, const char* sb, int ks) {
    constexpr int mi = decltype(ri)::value;
    AF[mi] = lds_read16(sb + (ks ? a_rd1 : a_rd0) + mi * 2048);
  };
  auto phase = [&](auto hfc, auto rdm, auto g0c, auto g1c, bf16x8* AF, bf16x8* BF, bf16x8* AN_, bf16x8* BN_,
                   const char* rsb, int rks, int rhf, int gt, int gbuf) {
    constexpr int hf = decltype(hfc)::value, RDM = decltype(rdm)::value;
    constexpr int g0 = decltype(g0c)::value, g1 = decltype(g1c)::value, NG = g1 - g0;
    constexpr int NR = RDM == 0 ? 0 : (RDM == 1 ? 2 * NH : 2 * NH + MI);
    sfor<0, NM>([&](auto ic) {
      constexpr int i = decltype(ic)::value, mi = i / NH, ni = i % NH;
      MFMA16(acc[mi][hf * NH + ni], BF[ni], AF[mi]);
      sfor<0, NR>([&](auto rc) {
        constexpr int r = decltype(rc)::value;
        if constexpr ((r * (NM - NM / 4)) / (NR > 0 ? NR : 1) == i) {
          if constexpr (r < 2 * NH) rd_b(rc, BN_, rsb, rks, rhf);
          else rd_a(IC<r - 2 * NH>{}, AN_, rsb, rks);
        }
      });
      sfor<0, NG>([&](auto gc) {
        constexpr int g = decltype(gc)::value;
        if constexpr ((g * NM) / (NG > 0 ? NG : 1) == i) issue1(IC<g0 + g>{}, gt, gbuf);
      });
      SBAR();
    });
  };
  constexpr auto gmin = [](int a, int b) { return a < b ? a : b; };
  int cur = 0, prv = 0;
  auto ktile = [&](auto vmc, auto restc, auto firstc, int t) {
    constexpr int VM = decltype(vmc)::value;
    constexpr bool REST = decltype(restc)::value && !LW, FIRST = decltype(firstc)::value && !LW;
    const int nxt = (cur + ST_BYTES == NS * ST_BYTES) ? 0 : cur + ST_BYTES;
    const char* sc = smem + cur;
    const char* sn = smem + nxt;
    constexpr int R1 = REST ? gmin(G, GPP) : 0, R2 = REST ? gmin(G, 2 * GPP) : 0, R3 = REST ? gmin(G, 3 * GPP) : 0, R4 = REST ? gmin(G, 4 * GPP) : 0;
    constexpr int F1 = FIRST ? gmin(G, GPP) : 0;
    if constexpr (NSP == 1) {
      phase(IC<0>{}, IC<2>{}, IC<R1>{}, IC<R2>{}, a0, b0, a1, b1, sc, 1, 0, t - 1 + NS, prv);
      if constexpr (VM >= 0) {
        if constexpr (!LW) WAIT_V(VM);
        WAIT_L0(); BAR();
        phase(IC<0>{}, IC<2>{}, IC<0>{}, IC<F1>{}, a1, b1, a0, b0, sn, 0, 0, t + NS, cur);
      } else {
        phase(IC<0>{}, IC<0>{}, IC<0>{}, IC<0>{}, a1, b1, a0, b0, sn, 0, 0, 0, 0);
      }
    } else {
      phase(IC<0>{}, IC<1>{}, IC<R1>{}, IC<R2>{}, a0, b0, a1, b1, sc, 0, 1, t - 1 + NS, prv);
      phase(IC<1>{}, IC<2>{}, IC<R2>{}, IC<R3>{}, a0, b1, a1, b0, sc, 1, 0, t - 1 + NS, prv);
      phase(IC<0>{}, IC<1>{}, IC<R3>{}, IC<R4>{}, a1, b0, a1, b1, sc, 1, 1, t - 1 + NS, prv);
      if constexpr (VM >= 0) {
        WAIT_V(VM); WAIT_L0(); BAR();
        phase(IC<1>{}, IC<2>{}, IC<0>{}, IC<F1>{}, a1, b1, a0, b0, sn, 0, 0, t + NS, cur);
      } else {
        phase(IC<1>{}, IC<0>{}, IC<0>{}, IC<0>{}, a1, b1, a0, b0, sn, 0, 0, 0, 0);
      }
    }
    prv = cur; cur = nxt;
  };

  if constexpr (!LW) {
    sfor<0, NS>([&](auto sc_) { sfor<0, G>([&](auto gc) { issue1(gc, decltype(sc_)::value, decltype(sc_)::value * ST_BYTES); }); });
    WAIT_V((NS - 1) * G);
  }
  BAR();
  STAMP_MID(MODE == 0 ? 2 : MODE == 1 ? 4 : MODE == 2 ? 6 : 7, 0);
  sfor<0, 2 * NH>([&](auto rc) { rd_b(rc, b0, smem, 0, 0); });
  sfor<0, MI>([&](auto rc) { rd_a(rc, a0, smem, 0); });
  using TT = IC<1>; using FF = IC<0>;
  ktile(IC<(NS - 2) * G>{}, FF{}, TT{}, 0);
  for (int t = 1; t < NT - NS; ++t) ktile(IC<(NS - 2) * G>{}, TT{}, TT{}, t);
  ktile(IC<(NS - 2) * G>{}, TT{}, FF{}, NT - NS);
  sfor<0, NS - 2>([&](auto rc) { ktile(IC<(NS - 3 - decltype(rc)::value) * G>{}, FF{}, FF{}, 0); });
  ktile(IC<-1>{}, FF{}, FF{}, 0);

  STAMP_MID(MODE == 0 ? 2 : MODE == 1 ? 4 : MODE == 2 ? 6 : 7, 1);
  if constexpr (MODE == 0) {
    const float qs = 0.17677669529663687f * 1.4426950408889634f;
    BAR();
    char* st = smem + wid * (64 * 208);
#pragma unroll
    for (int ni = 0; ni < NI; ++ni) {
      const int n = tn * BN + wn * TN + ni * 16;
      const float sc = n < 1024 ? qs : 1.0f;
#pragma unroll
      for (int mi = 0; mi < MI; ++mi) {
        u32x2 w;
        w[0] = pack2(acc[mi][ni][0] * sc, acc[mi][ni][1] * sc);
        w[1] = pack2(acc[mi][ni][2] * sc, acc[mi][ni][3] * sc);
        *(LAS u32x2*)(st + (mi * 16 + fr) * 208 + ni * 32 + fq * 8) = w;
      }
    }
#pragma unroll
    for (int j = 0; j < 3; ++j) {
      const int hh = (tn * BN + wn * TN + j * 32) >> 5;
      u16* ob = p.out + ((size_t)hh * S_ + m0 + wm * TM) * 32 + lane * 8;
#pragma unroll
      for (int i = 0; i < 4; ++i) {
        const u32x4 v = *(const LAS u32x4*)(st + (i * 16 + (lane >> 2)) * 208 + j * 64 + (lane & 3) * 16);
        ST16(ob + i * 512, v);
      }
    }
  } else if constexpr (MODE == 3) {
    const int tile = tm * 8 + tn;
    if (wm != kh) {
      float* dst = p.slab + ((size_t)(tile * 2 + kh) * 2 + wn) * 4096 + lane * 4;
#pragma unroll
      for (int mi = 0; mi < MI; ++mi)
#pragma unroll
        for (int ni = 0; ni < NI; ++ni) ST16(dst + (mi * NI + ni) * 256, __builtin_bit_cast(u32x4, acc[mi][ni]));
      WAIT_V(0);
      if (lane == 0) __hip_atomic_fetch_add(p.flags + (tile * 2 + kh) * 32, 1u, __ATOMIC_RELAXED, __HIP_MEMORY_SCOPE_AGENT);
      BAR();
    } else {
      const unsigned* fl = p.flags + (tile * 2 + (kh ^ 1)) * 32;
      for (int spin = 0; spin < (1 << 22); ++spin) {
        if (__builtin_amdgcn_readfirstlane(__hip_atomic_load(fl, __ATOMIC_RELAXED, __HIP_MEMORY_SCOPE_AGENT)) >= 2u) break;
        __builtin_amdgcn_s_sleep(1);
      }
      const float* src = p.slab + ((size_t)(tile * 2 + (kh ^ 1)) * 2 + wn) * 4096 + lane * 4;
      f32x4 pv[MI][NI];
#pragma unroll
      for (int mi = 0; mi < MI; ++mi)
#pragma unroll
        for (int ni = 0; ni < NI; ++ni)
          asm volatile("global_load_dwordx4 %0, %1, off sc0 sc1" : "=v"(pv[mi][ni]) : "v"(src + (mi * NI + ni) * 256) : "memory");
      BAR();
      asm volatile("s_waitcnt vmcnt(0)" ::: "memory");
      SBAR();
#pragma unroll
      for (int ni = 0; ni < NI; ++ni)
#pragma unroll
        for (int mi = 0; mi < MI; ++mi) {
          asm volatile("" : "+v"(pv[mi][ni]));
          const f32x4 a = acc[mi][ni] + pv[mi][ni];
          u32x2 w;
          w[0] = pack2(gv[ni][0] * rbf(a[0]), gv[ni][1] * rbf(a[1]));
          w[1] = pack2(gv[ni][2] * rbf(a[2]), gv[ni][3] * rbf(a[3]));
          *(LAS u32x2*)(smem + (mi * 16 + fr) * 272 + wn * 128 + ni * 32 + fq * 8) = w;
        }
    }
    WAIT_L0();
    BAR();
#pragma unroll
    for (int i = 0; i < 4; ++i) {
      const u32x4 v = *(const LAS u32x4*)(smem + (i * 16 + (tid >> 4)) * 272 + (tid & 15) * 16);
      u32x4 w;
#pragma unroll
      for (int j = 0; j < 4; ++j) w[j] = pack2(lo_f(rv[i][j]) + lo_f(v[j]), hi_f(rv[i][j]) + hi_f(v[j]));
      ST16(p.out + (size_t)(m0 + kh * 64 + i * 16 + (tid >> 4)) * D_ + tn * BN + (tid & 15) * 8, w);
    }
  } else if constexpr (MODE == 1 || MODE == 4) {
    BAR();
#pragma unroll
    for (int ni = 0; ni < NI; ++ni) {
#pragma unroll
      for (int mi = 0; mi < MI; ++mi) {
        u32x2 w;
        w[0] = pack2(gv[ni][0] * rbf(acc[mi][ni][0]), gv[ni][1] * rbf(acc[mi][ni][1]));
        w[1] = pack2(gv[ni][2] * rbf(acc[mi][ni][2]), gv[ni][3] * rbf(acc[mi][ni][3]));
        *(LAS u32x2*)(smem + (wm * TM + mi * 16 + fr) * 144 + wn * 64 + ni * 32 + fq * 8) = w;
      }
    }
    WAIT_L0();
    BAR();
#pragma unroll
    for (int i = 0; i < 4; ++i) {
      const u32x4 v = *(const LAS u32x4*)(smem + (i * 32 + (tid >> 3)) * 144 + (tid & 7) * 16);
      u32x4 w;
#pragma unroll
      for (int j = 0; j < 4; ++j) w[j] = pack2(lo_f(rv[i][j]) + lo_f(v[j]), hi_f(rv[i][j]) + hi_f(v[j]));
      ST16(p.out + (size_t)(m0 + i * 32 + (tid >> 3)) * D_ + tn * BN + (tid & 7) * 8, w);
    }
  } else {
    BAR();
    char* st = smem + wid * (64 * 144);
#pragma unroll
    for (int ni = 0; ni < 4; ++ni)
#pragma unroll
      for (int mi = 0; mi < MI; ++mi) {
        float o[4];
#pragma unroll
        for (int r = 0; r < 4; ++r) o[r] = rbf(acc[mi][ni][r]) * rbf(acc[mi][ni + 4][r]);
        u32x2 w;
        w[0] = pack2(o[0], o[1]);
        w[1] = pack2(o[2], o[3]);
        *(LAS u32x2*)(st + (mi * 16 + fr) * 144 + ni * 32 + fq * 8) = w;
      }
    u16* ob = p.out + (size_t)(m0 + wm * TM + (lane >> 3)) * F_ + tn * 128 + wn * 64 + (lane & 7) * 8;
#pragma unroll
    for (int i = 0; i < 8; ++i) {
      const u32x4 v = *(const LAS u32x4*)(st + (i * 8 + (lane >> 3)) * 144 + (lane & 7) * 16);
      ST16(ob + (size_t)i * 8 * F_, v);
    }
  }
  STAMP_END(MODE == 0 ? 2 : MODE == 1 ? 4 : MODE == 2 ? 6 : 7);
}

#define AT_NSTAGE 6
constexpr int AT_KT = 128, AT_NS = AT_NSTAGE, AT_ST = 16384;
#define MFMA32(a, b, c) __builtin_amdgcn_mfma_f32_32x32x16_bf16(a, b, c, 0, 0, 0)
#define AEXP(x) __builtin_amdgcn_exp2f(x)
#define MX3(a, b, c) __builtin_fmaxf(__builtin_fmaxf((a), (b)), (c))

__device__ __forceinline__ float tile_rowmax(const f32x16& s0, const f32x16& s1) {
  float a = MX3(s0[0], s0[1], s1[0]), b = MX3(s0[2], s0[3], s1[1]);
  a = MX3(a, s1[2], s1[3]);
#pragma unroll
  for (int r = 4; r < 16; r += 4) {
    a = MX3(a, s0[r], s0[r + 1]); b = MX3(b, s0[r + 2], s0[r + 3]);
    a = MX3(a, s1[r], s1[r + 1]); b = MX3(b, s1[r + 2], s1[r + 3]);
  }
  float mx = __builtin_fmaxf(a, b);
  auto rr = __builtin_amdgcn_permlane32_swap(__float_as_uint(mx), __float_as_uint(mx), false, false);
  return __builtin_fmaxf(__uint_as_float(rr[0]), __uint_as_float(rr[1]));
}

__global__ __launch_bounds__(512, 2) void attn_kernel(const u16* __restrict__ qkv, u16* __restrict__ o) {
  extern __shared__ __attribute__((aligned(16))) char smem[];
  STAMP_BEGIN();
  const int tid = threadIdx.x, lane = tid & 63;
  const int wid = __builtin_amdgcn_readfirstlane(tid >> 6);
  const int x = blockIdx.x & 7, idx = blockIdx.x >> 3;
  const int hd = x * 4 + (idx >> 3), qb = idx & 7;
  const int r32 = lane & 31, hi = lane >> 5;
  const u16* Q = qkv + ((size_t)(0 * 32 + hd) * S_) * 32;
  const u16* Kp = qkv + ((size_t)(1 * 32 + hd) * S_) * 32;
  const u16* Vp = qkv + ((size_t)(2 * 32 + hd) * S_) * 32;
  const int q0 = qb * 256 + wid * 32;

  const unsigned k_voff = (unsigned)(((lane >> 2) * 32 + 8 * ((lane & 3) ^ ((lane >> 4) & 3))) * 2);
  const unsigned v_voff = (unsigned)(lane * 16);
  const unsigned lds0 = (unsigned)(size_t)(LAS char*)smem;
  auto issue = [&](int t, int bufoff) {
    const char* gk = (const char*)(Kp + ((size_t)t * AT_KT + wid * 16) * 32);
    const char* gv = (const char*)(Vp + ((size_t)t * AT_KT + wid * 16) * 32);
    glds16(gk, k_voff, lds0 + bufoff + wid * 1024);
    glds16(gv, v_voff, lds0 + bufoff + 8192 + wid * 1024);
  };
#pragma unroll
  for (int s = 0; s < AT_NS - 1; ++s) issue(s, s * AT_ST);
  const bf16x8 qf0 = *(const bf16x8*)(Q + (size_t)(q0 + r32) * 32 + hi * 8);
  const bf16x8 qf1 = *(const bf16x8*)(Q + (size_t)(q0 + r32) * 32 + 16 + hi * 8);

  const int k_rd0 = r32 * 64 + ((hi ^ ((r32 >> 2) & 3)) << 4);
  const int k_rd1 = k_rd0 ^ 32;
  const int v_rd = 8192 + (4 * hi + ((lane & 15) >> 2)) * 64 + ((lane >> 4) & 1) * 32 + (lane & 3) * 8;

  f32x16 oacc = {};
  f32x16 negm;
  float m, l = 0.f;
  f32x16 sa0, sa1, sb0, sb1;
  u32x4 pp0 = {}, pp1 = {}, pp2 = {}, pp3 = {};
  bf16x8 vf0, vf1, vf2, vf3;
#define AT_BIG 1.0e30f
  constexpr float BIG = AT_BIG;
  constexpr int NT = S_ / AT_KT;

#define SCORES(D0, D1, kb, CIN)                                                         \
  do {                                                                                  \
    bf16x8 k00 = lds_read16((kb) + k_rd0), k01 = lds_read16((kb) + k_rd1);              \
    bf16x8 k10 = lds_read16((kb) + 2048 + k_rd0), k11 = lds_read16((kb) + 2048 + k_rd1);\
    D0 = MFMA32(k00, qf0, CIN); D1 = MFMA32(k10, qf0, CIN);                             \
    D0 = MFMA32(k01, qf1, D0);  D1 = MFMA32(k11, qf1, D1);                              \
  } while (0)
#define VFRAG(vb, s) __builtin_shufflevector(tr_read((vb) + (s) * 1024), tr_read((vb) + (s) * 1024 + 512), 0, 1, 2, 3, 4, 5, 6, 7)
#define READV(vb) do { vf0 = VFRAG(vb, 0); vf1 = VFRAG(vb, 1); vf2 = VFRAG(vb, 2); vf3 = VFRAG(vb, 3); } while (0)
#define PV()                                                                            \
  do {                                                                                  \
    oacc = MFMA32(vf0, __builtin_bit_cast(bf16x8, pp0), oacc);                          \
    oacc = MFMA32(vf1, __builtin_bit_cast(bf16x8, pp1), oacc);                          \
    oacc = MFMA32(vf2, __builtin_bit_cast(bf16x8, pp2), oacc);                          \
    oacc = MFMA32(vf3, __builtin_bit_cast(bf16x8, pp3), oacc);                          \
  } while (0)
#define EXPSUM(C0, C1, LS)                                                              \
  do {                                                                                  \
    f32x2 ls2_ = {0.f, 0.f}, ls3_ = {0.f, 0.f};                                         \
    _Pragma("unroll") for (int r = 0; r < 16; r += 2) {                                 \
      C0[r] = AEXP(C0[r]); C0[r + 1] = AEXP(C0[r + 1]);                                 \
      C1[r] = AEXP(C1[r]); C1[r + 1] = AEXP(C1[r + 1]);                                 \
      ls2_ += (f32x2){C0[r], C0[r + 1]};                                                \
      ls3_ += (f32x2){C1[r], C1[r + 1]};                                                \
    }                                                                                   \
    ls2_ += ls3_;                                                                       \
    LS = ls2_[0] + ls2_[1];                                                             \
  } while (0)
#define PACK(C0, C1)                                                                    \
  do {                                                                                  \
    _Pragma("unroll") for (int j = 0; j < 4; ++j) {                                     \
      pp0[j] = pack2(C0[2 * j], C0[2 * j + 1]);                                         \
      pp1[j] = pack2(C0[8 + 2 * j], C0[8 + 2 * j + 1]);                                 \
      pp2[j] = pack2(C1[2 * j], C1[2 * j + 1]);                                         \
      pp3[j] = pack2(C1[8 + 2 * j], C1[8 + 2 * j + 1]);                                 \
    }                                                                                   \
  } while (0)
#define STEP(C0, C1, N0, N1, kcur, knext, vprev, HASN)                                  \
  do {                                                                                  \
    READV(vprev);                                                                       \
    if (HASN) SCORES(N0, N1, knext, negm);                                              \
    PV();                                                                               \
    float ls;                                                                           \
    EXPSUM(C0, C1, ls);                                                                 \
    if (__builtin_expect(__any(!(ls <= BIG)), 0)) {                                     \
                  \
      f32x16 z_ = {};                                                                   \
      SCORES(C0, C1, kcur, z_);                                                         \
      const float mn = __builtin_fmaxf(m, tile_rowmax(C0, C1));                         \
      const float dm = m - mn;                                                          \
      const float f = __builtin_amdgcn_exp2f(dm);                                       \
      m = mn; l *= f;                                                                   \
      _Pragma("unroll") for (int r = 0; r < 16; ++r) {                                  \
        oacc[r] *= f; negm[r] = -mn; C0[r] -= mn; C1[r] -= mn;                          \
        if (HASN) { N0[r] += dm; N1[r] += dm; }                                         \
      }                                                                                 \
      EXPSUM(C0, C1, ls);                                                               \
    }                                                                                   \
    l += ls;                                                                            \
    PACK(C0, C1);                                                                       \
  } while (0)

  WAIT_V(2 * (AT_NS - 2));
  BAR();
  {
    f32x16 z = {};
    SCORES(sa0, sa1, smem, z);
    m = tile_rowmax(sa0, sa1);
#pragma unroll
    for (int r = 0; r < 16; ++r) { negm[r] = -m; sa0[r] -= m; sa1[r] -= m; }
  }
  int cur = 0, prv = 0;
  for (int t = 0; t < NT - 1; ++t) {
    const char* sb = smem + cur;
    int nx = cur + AT_ST; if (nx >= AT_NS * AT_ST) nx = 0;
    int prv2 = cur - AT_ST; if (prv2 < 0) prv2 = (AT_NS - 1) * AT_ST;
    STEP(sa0, sa1, sb0, sb1, sb, sb + 4096, smem + prv + 4096 + v_rd, true);
    if (t + AT_NS - 1 < NT) {
      WAIT_V(2 * (AT_NS - 3));
      WAIT_L0();
      BAR();
      issue(t + AT_NS - 1, prv2);
    } else {
      WAIT_V(0);
      WAIT_L0();
      BAR();
    }
    STEP(sb0, sb1, sa0, sa1, sb + 4096, smem + nx, sb + v_rd, true);
    prv = cur; cur = nx;
  }
  {
    const char* sb = smem + cur;
    STEP(sa0, sa1, sb0, sb1, sb, sb + 4096, smem + prv + 4096 + v_rd, true);
    STEP(sb0, sb1, sa0, sa1, sb + 4096, sb, sb + v_rd, false);
    prv = cur;
  }
  READV(smem + prv + 4096 + v_rd);
  PV();
#undef SCORES
#undef VFRAG
#undef READV
#undef PV
#undef EXPSUM
#undef PACK
#undef STEP
  {
    auto rr = __builtin_amdgcn_permlane32_swap(__float_as_uint(l), __float_as_uint(l), false, false);
    l = __uint_as_float(rr[0]) + __uint_as_float(rr[1]);
  }
  const float inv = 1.0f / l;
  u16* op = o + (size_t)(q0 + r32) * D_ + hd * 32 + 4 * hi;
#pragma unroll
  for (int g = 0; g < 4; ++g) {
    u32x2 w;
    w[0] = pack2(oacc[4 * g] * inv, oacc[4 * g + 1] * inv);
    w[1] = pack2(oacc[4 * g + 2] * inv, oacc[4 * g + 3] * inv);
    *(u32x2*)(op + 8 * g) = w;
  }
  STAMP_END(3);
}

constexpr int A2_KT = 256, A2_NS = 3, A2_ST = 32768, A2_MOD = A2_NS * A2_ST + 1024, A2_LDS = A2_MOD + 32768;
template <bool SAFE>
__device__ __forceinline__ void attn_pass(char* smem, const u16* Kp, const u16* Vp, bf16x8 qf0, bf16x8 qf1, int wid, int lane,
                                          f32x16& oacc, float& m, float& l,
                                          const u16* pw0, const u16* pw1, const u16* pw2, const u16* pw3, const u16* wm) {
  const int wk = wid >> 3, r32 = lane & 31, hi = lane >> 5;
  const unsigned k_voff = (unsigned)(((lane >> 2) * 32 + 8 * ((lane & 3) ^ ((lane >> 4) & 3))) * 2);
  const unsigned v_voff = (unsigned)(lane * 16);
  const unsigned lds0 = (unsigned)(size_t)(LAS char*)smem;
  auto issue = [&](int t, int bufoff) {
    const char* gk = (const char*)(Kp + ((size_t)t * A2_KT + wid * 16) * 32);
    const char* gv = (const char*)(Vp + ((size_t)t * A2_KT + wid * 16) * 32);
    glds16(gk, k_voff, lds0 + bufoff + wid * 1024);
    glds16(gv, v_voff, lds0 + bufoff + 16384 + wid * 1024);
  };
  issue(0, 0);
  issue(1, A2_ST);
  if (!SAFE) {
    const unsigned m_voff = (unsigned)(((lane >> 3) * 6144 + (lane & 7) * 8) * 2);
    glds16(wm + (size_t)(wid * 16) * 6144, m_voff, lds0 + A2_MOD + wid * 2048);
    glds16(wm + (size_t)(wid * 16 + 8) * 6144, m_voff, lds0 + A2_MOD + wid * 2048 + 1024);
  }
  const int k_rd0 = wk * 8192 + r32 * 64 + ((hi ^ ((r32 >> 2) & 3)) << 4);
  const int k_rd1 = k_rd0 ^ 32;
  const int v_rd = 16384 + wk * 8192 + (4 * hi + ((lane & 15) >> 2)) * 64 + ((lane >> 4) & 1) * 32 + (lane & 3) * 8;
  f32x16 negm = {};
  f32x2 lsa = {0.f, 0.f}, lsb = {0.f, 0.f};
  oacc = (f32x16){};
  m = 0.f; l = 0.f;
  constexpr int NT = S_ / A2_KT;
  int cur = 0;
  for (int t = 0; t < NT; ++t) {
    if (t == 0) WAIT_V(SAFE ? 2 : 4); else if (t == 1) WAIT_V(SAFE ? 3 : 5); else if (t + 1 < NT) WAIT_V(3); else WAIT_V(0);
    BAR();
    {
      int ck = (blockIdx.x * NT + t) * 16 + wid;
      if (ck >= 2048 + 3 * 7168) ck -= 16384;
      const u16* pb = pw0;
      if (ck >= 2048) { pb = pw1; ck -= 2048; if (ck >= 7168) { pb = pw2; ck -= 7168; if (ck >= 7168) { pb = pw3; ck -= 7168; } } }
      glds16((const char*)pb + (size_t)ck * 1024, v_voff, lds0 + A2_NS * A2_ST);
      int nb = cur + 2 * A2_ST; if (nb >= A2_NS * A2_ST) nb -= A2_NS * A2_ST;
      if (t + 2 < NT) issue(t + 2, nb);
    }
#pragma unroll
    for (int hb = 0; hb < 2; ++hb) {
      const char* kb = smem + cur + hb * 4096;
      const bf16x8 k00 = lds_read16(kb + k_rd0), k01 = lds_read16(kb + k_rd1);
      const bf16x8 k10 = lds_read16(kb + 2048 + k_rd0), k11 = lds_read16(kb + 2048 + k_rd1);
      f32x16 s0, s1;
      if (SAFE || (t == 0 && hb == 0)) {
        f32x16 z_ = {};
        s0 = MFMA32(k00, qf0, z_); s1 = MFMA32(k10, qf0, z_);
        s0 = MFMA32(k01, qf1, s0); s1 = MFMA32(k11, qf1, s1);
        const float tm = tile_rowmax(s0, s1);
        const bool first = (t == 0 && hb == 0);
        const float mn = first ? tm : __builtin_fmaxf(m, tm);
        if (SAFE) {
          const float f = first ? 0.f : __builtin_amdgcn_exp2f(m - mn);
          lsa *= f; lsb *= f;
          oacc *= f;
        }
        m = mn;
#pragma unroll
        for (int r = 0; r < 16; ++r) { negm[r] = -mn; s0[r] -= mn; s1[r] -= mn; }
      } else {
        s0 = MFMA32(k00, qf0, negm); s1 = MFMA32(k10, qf0, negm);
        s0 = MFMA32(k01, qf1, s0); s1 = MFMA32(k11, qf1, s1);
      }
#pragma unroll
      for (int r = 0; r < 16; r += 2) {
        s0[r] = AEXP(s0[r]); s0[r + 1] = AEXP(s0[r + 1]);
        s1[r] = AEXP(s1[r]); s1[r + 1] = AEXP(s1[r + 1]);
        lsa += (f32x2){s0[r], s0[r + 1]};
        lsb += (f32x2){s1[r], s1[r + 1]};
      }
      u32x4 p0, p1, p2, p3;
#pragma unroll
      for (int j = 0; j < 4; ++j) {
        p0[j] = pack2(s0[2 * j], s0[2 * j + 1]);
        p1[j] = pack2(s0[8 + 2 * j], s0[8 + 2 * j + 1]);
        p2[j] = pack2(s1[2 * j], s1[2 * j + 1]);
        p3[j] = pack2(s1[8 + 2 * j], s1[8 + 2 * j + 1]);
      }
      const char* vb = smem + cur + hb * 4096 + v_rd;
#define VFRAG(s) __builtin_shufflevector(tr_read(vb + (s) * 1024), tr_read(vb + (s) * 1024 + 512), 0, 1, 2, 3, 4, 5, 6, 7)
      oacc = MFMA32(VFRAG(0), __builtin_bit_cast(bf16x8, p0), oacc);
      oacc = MFMA32(VFRAG(1), __builtin_bit_cast(bf16x8, p1), oacc);
      oacc = MFMA32(VFRAG(2), __builtin_bit_cast(bf16x8, p2), oacc);
      oacc = MFMA32(VFRAG(3), __builtin_bit_cast(bf16x8, p3), oacc);
#undef VFRAG
    }
    WAIT_L0();
    cur += A2_ST; if (cur >= A2_NS * A2_ST) cur = 0;
  }
  lsa += lsb;
  l = lsa[0] + lsa[1];
  {
    auto rr = __builtin_amdgcn_permlane32_swap(__float_as_uint(l), __float_as_uint(l), false, false);
    l = __uint_as_float(rr[0]) + __uint_as_float(rr[1]);
  }
  WAIT_V(0);
}

__global__ __launch_bounds__(1024, 4) void attn2_kernel(const u16* __restrict__ qkv, u16* __restrict__ o,
                                                        const u16* pw0, const u16* pw1, const u16* pw2, const u16* pw3,
                                                        const u16* __restrict__ cvec, const u16* wmod, float* __restrict__ modp) {
  extern __shared__ __attribute__((aligned(16))) char smem[];
  STAMP_BEGIN();
  const int tid = threadIdx.x, lane = tid & 63;
  const int wid = __builtin_amdgcn_readfirstlane(tid >> 6);
  const int wq = wid & 7, wk = wid >> 3;
  const int x = blockIdx.x & 7, idx = blockIdx.x >> 3;
  const int hd = x * 4 + (idx >> 3), qb = idx & 7;
  const int r32 = lane & 31, hi = lane >> 5;
  const u16* Q = qkv + ((size_t)(0 * 32 + hd) * S_) * 32;
  const u16* Kp = qkv + ((size_t)(1 * 32 + hd) * S_) * 32;
  const u16* Vp = qkv + ((size_t)(2 * 32 + hd) * S_) * 32;
  const int q0 = qb * 256 + wq * 32;
  const bf16x8 qf0 = *(const bf16x8*)(Q + (size_t)(q0 + r32) * 32 + hi * 8);
  const bf16x8 qf1 = *(const bf16x8*)(Q + (size_t)(q0 + r32) * 32 + 16 + hi * 8);

  f32x16 oacc;
  float m, l;
  const int mcg = 32 + (blockIdx.x >> 2), mks = blockIdx.x & 3;
  const u16* wm = wmod + (size_t)(mks * 256) * 6144 + mcg * 64;
  attn_pass<false>(smem, Kp, Vp, qf0, qf1, wid, lane, oacc, m, l, pw0, pw1, pw2, pw3, wm);
  {
    float chk = l;
#pragma unroll
    for (int r = 0; r < 16; ++r) chk += __builtin_fabsf(oacc[r]);
    const int bad = !(chk < 1.0e37f);
    if (__syncthreads_or(bad)) attn_pass<true>(smem, Kp, Vp, qf0, qf1, wid, lane, oacc, m, l, pw0, pw1, pw2, pw3, wm);
  }
  __syncthreads();
  float* xb = (float*)smem + wq * (18 * 64);
  float* mred = (float*)(smem + 65536);
  if (wk == 1) {
    xb[lane] = m; xb[64 + lane] = l;
#pragma unroll
    for (int r = 0; r < 16; ++r) xb[(2 + r) * 64 + lane] = oacc[r];
  }
  {
    const int rg = tid >> 3, cl = tid & 7;
    const unsigned cc = *(const unsigned*)(cvec + mks * 256 + 2 * rg);
    const float c0 = lo_f(cc), c1 = hi_f(cc);
    const float s0 = rbf(c0 / (1.0f + __expf(-c0))), s1 = rbf(c1 / (1.0f + __expf(-c1)));
    const u32x4 w0 = *(const LAS u32x4*)(smem + A2_MOD + (2 * rg) * 128 + cl * 16);
    const u32x4 w1 = *(const LAS u32x4*)(smem + A2_MOD + (2 * rg + 1) * 128 + cl * 16);
    float a[8];
#pragma unroll
    for (int j = 0; j < 4; ++j) {
      a[2 * j] = s0 * lo_f(w0[j]) + s1 * lo_f(w1[j]);
      a[2 * j + 1] = s0 * hi_f(w0[j]) + s1 * hi_f(w1[j]);
    }
#pragma unroll
    for (int j = 0; j < 8; ++j) {
      a[j] += __shfl_xor(a[j], 8); a[j] += __shfl_xor(a[j], 16); a[j] += __shfl_xor(a[j], 32);
    }
    if (lane < 8) {
#pragma unroll
      for (int j = 0; j < 8; ++j) mred[wid * 64 + lane * 8 + j] = a[j];
    }
  }
  __syncthreads();
  if (wid == 15) {
    float sacc = 0.f;
#pragma unroll
    for (int w = 0; w < 16; ++w) sacc += mred[w * 64 + lane];
    modp[mks * 6144 + mcg * 64 + lane] = sacc;
  }
  if (wk == 0) {
    const float m2 = xb[lane], l2 = xb[64 + lane];
    const float mn = __builtin_fmaxf(m, m2);
    const float e1 = (m - mn) * 0.5f, e2 = (m2 - mn) * 0.5f;
    const float f1 = __builtin_amdgcn_exp2f(e1), f2 = __builtin_amdgcn_exp2f(e2);
    const float lt = (l * f1) * f1 + (l2 * f2) * f2;
    const float inv = 1.0f / lt;
    u16* op = o + (size_t)(q0 + r32) * D_ + hd * 32 + 4 * hi;
#pragma unroll
    for (int g = 0; g < 4; ++g) {
      float v[4];
#pragma unroll
      for (int e = 0; e < 4; ++e) v[e] = ((oacc[4 * g + e] * f1) * f1 + (xb[(2 + 4 * g + e) * 64 + lane] * f2) * f2) * inv;
      u32x2 w;
      w[0] = pack2(v[0], v[1]);
      w[1] = pack2(v[2], v[3]);
      *(u32x2*)(op + 8 * g) = w;
    }
  }
  STAMP_END(3);
}

template <int MODE> static void launch_gemm(const GemmP& p, hipStream_t stream) {
  using C = GCfg<MODE>;
  constexpr int lds = C::NS * (C::BM + C::BN) * 128;
  hipFuncSetAttribute((const void*)gemm_kernel<MODE>, hipFuncAttributeMaxDynamicSharedMemorySize, lds);
  gemm_kernel<MODE><<<C::NTM * C::NTN, C::WM * C::WN * 64 * (C::LW ? 2 : 1), lds, stream>>>(p);
}

extern "C" void kernel_launch(void* const* d_in, const int* in_sizes, int n_in, void* d_out, int out_size,
                              void* d_ws, size_t ws_size, hipStream_t stream) {
  const u16* x = (const u16*)d_in[0];
  const u16* wqkv = (const u16*)d_in[1];
  const u16* wo = (const u16*)d_in[2];
  const u16* wg = (const u16*)d_in[4];
  const u16* wu = (const u16*)d_in[5];
  const u16* wd = (const u16*)d_in[6];
  const u16* c = (const u16*)d_in[7];
  const u16* wmod = (const u16*)d_in[8];
  u16* out = (u16*)d_out;

  char* ws = (char*)d_ws;
  float* mod = (float*)ws;
  u16* h = (u16*)(ws + (1 << 20));
  u16* qkv = (u16*)(ws + (8 << 20));
  u16* ao = (u16*)(ws + (24 << 20));
  u16* x1 = (u16*)(ws + (32 << 20));
  u16* hh = (u16*)(ws + (40 << 20));
  float* slab = (float*)(ws + (64 << 20));
  unsigned* flags = (unsigned*)(ws + (512 << 10));

  mod_kernel<<<448, 256, 0, stream>>>(c, wmod, mod, x, wqkv, flags);
  ln_kernel<<<512, 256, 0, stream>>>(x, mod, 0, h, 1);
  { GemmP p{h, wqkv, nullptr, nullptr, nullptr, qkv, nullptr, nullptr}; launch_gemm<0>(p, stream); }
  hipFuncSetAttribute((const void*)attn2_kernel, hipFuncAttributeMaxDynamicSharedMemorySize, A2_LDS);
  attn2_kernel<<<256, 1024, A2_LDS, stream>>>(qkv, ao, wo, wg, wu, wd, c, wmod, mod);
  { GemmP p{ao, wo, nullptr, x, mod + 2 * 1024, x1, nullptr, nullptr}; launch_gemm<1>(p, stream);
  }
  ln_kernel<<<512, 256, 0, stream>>>(x1, mod, 3 * 1024, h, 5);
  { GemmP p{h, wg, wu, nullptr, nullptr, hh, nullptr, nullptr}; launch_gemm<2>(p, stream); }
  { GemmP p{hh, wd, nullptr, x1, mod + 5 * 1024, out, slab, flags}; launch_gemm<4>(p, stream); }
}
```

```cpp
#include <hip/hip_runtime.h>
#include <hip/hip_bf16.h>
#include <stdint.h>

typedef unsigned short u16;
typedef short bf16x8 __attribute__((ext_vector_type(8)));
typedef short s16x4 __attribute__((ext_vector_type(4)));
typedef float f32x2 __attribute__((ext_vector_type(2)));
typedef float f32x4 __attribute__((ext_vector_type(4)));
typedef float f32x16 __attribute__((ext_vector_type(16)));
typedef unsigned u32x2 __attribute__((ext_vector_type(2)));
typedef unsigned u32x4 __attribute__((ext_vector_type(4)));
typedef __bf16 bf16x2_t __attribute__((ext_vector_type(2)));
#define LAS __attribute__((address_space(3)))
#define GAS __attribute__((address_space(1)))

constexpr int S_ = 2048, D_ = 1024, H_ = 32, DH_ = 32, F_ = 3584;

__device__ __forceinline__ float bf2f(u16 u) { return __uint_as_float((unsigned)u << 16); }
__device__ __forceinline__ unsigned pack2(float lo, float hi) {
  f32x2 v = {lo, hi};
  bf16x2_t b = __builtin_convertvector(v, bf16x2_t);
  return __builtin_bit_cast(unsigned, b);
}
__device__ __forceinline__ float rbf(float f) {
  __bf16 b = (__bf16)f;
  return __uint_as_float((unsigned)__builtin_bit_cast(u16, b) << 16);
}
__device__ __forceinline__ float lo_f(unsigned u) { return __uint_as_float(u << 16); }
__device__ __forceinline__ float hi_f(unsigned u) { return __uint_as_float(u & 0xffff0000u); }

#define STAMP_BEGIN()
#define STAMP_END(kid)
#define STAMP_MID(kid, i)
#define WAIT_V(n) asm volatile("s_waitcnt vmcnt(%0)" ::"n"(n) : "memory")
#define WAIT_L0() asm volatile("s_waitcnt lgkmcnt(0)" ::: "memory")
#define BAR() __builtin_amdgcn_s_barrier()

__device__ __forceinline__ void glds16(const void* sbase, unsigned voff, unsigned ldsaddr) {
  asm volatile("s_mov_b32 m0, %2\n\ts_nop 0\n\tglobal_load_lds_dwordx4 %0, %1" ::"v"(voff), "s"(sbase), "s"(ldsaddr) : "memory");
}
#define MFMA16(c, a, b) c = __builtin_amdgcn_mfma_f32_16x16x32_bf16(a, b, c, 0, 0, 0)
#define ST16(p, v) asm volatile("global_store_dwordx4 %0, %1, off sc0 sc1\n\ts_nop 1" ::"v"(p), "v"(v) : "memory")
#define SBAR() __builtin_amdgcn_sched_barrier(0)
#define SGB(mask, n) __builtin_amdgcn_sched_group_barrier(mask, n, 0)
template <int I> struct IC { static constexpr int value = I; };
template <int I, int N, class F> __device__ __forceinline__ void sfor(F&& f) {
  if constexpr (I < N) { f(IC<I>{}); sfor<I + 1, N>(f); }
}
__device__ __forceinline__ s16x4 tr_read(const char* p) {
  return __builtin_amdgcn_ds_read_tr16_b64_v4i16((LAS s16x4*)p);
}
__device__ __forceinline__ bf16x8 lds_read16(const char* p) { return *(const LAS bf16x8*)p; }

__device__ __forceinline__ void mod_block(const u16* __restrict__ c, const u16* __restrict__ wmod, float* __restrict__ modp,
                                          int cg, int ks, float* sh) {
  const int tid = threadIdx.x;
  const int cl = tid & 7, rg = tid >> 3;
  const int col0 = cg * 64 + cl * 8;
  u32x4 w[8];
#pragma unroll
  for (int i = 0; i < 8; ++i)
    w[i] = __builtin_nontemporal_load((const u32x4*)(wmod + (size_t)(ks * 256 + rg + i * 32) * 6144 + col0));
  {
    const float v = bf2f(c[ks * 256 + tid]);
    sh[tid] = rbf(v / (1.0f + __expf(-v)));
  }
  __syncthreads();
  float acc[8];
#pragma unroll
  for (int j = 0; j < 8; ++j) acc[j] = 0.f;
#pragma unroll
  for (int i = 0; i < 8; ++i) {
    const float s = sh[rg + i * 32];
#pragma unroll
    for (int j = 0; j < 4; ++j) {
      acc[2 * j] += s * lo_f(w[i][j]);
      acc[2 * j + 1] += s * hi_f(w[i][j]);
    }
  }
  float* red = sh + 256;
#pragma unroll
  for (int j = 0; j < 8; ++j) red[rg * 65 + cl * 8 + j] = acc[j];
  __syncthreads();
  if (tid < 64) {
    float s = 0.f;
#pragma unroll 8
    for (int r = 0; r < 32; ++r) s += red[r * 65 + tid];
    modp[ks * 6144 + cg * 64 + tid] = s;
  }
}
__device__ __forceinline__ void touch_block(const char* src) {
  u32x4 v[8];
#pragma unroll
  for (int i = 0; i < 8; ++i) v[i] = *(const u32x4*)(src + i * 4096 + threadIdx.x * 16);
#pragma unroll
  for (int i = 0; i < 8; ++i) asm volatile("" ::"v"(v[i]));
}
__device__ __forceinline__ f32x4 ld_mod4(const float* modp, int idx) {
  f32x4 a = *(const f32x4*)(modp + idx), b = *(const f32x4*)(modp + 6144 + idx);
  f32x4 c = *(const f32x4*)(modp + 2 * 6144 + idx), d = *(const f32x4*)(modp + 3 * 6144 + idx);
  f32x4 s = (a + b) + (c + d);
  return (f32x4){rbf(s[0]), rbf(s[1]), rbf(s[2]), rbf(s[3])};
}

__global__ __launch_bounds__(256) void mod_kernel(const u16* __restrict__ c, const u16* __restrict__ wmod,
                                                  float* __restrict__ modp, const u16* __restrict__ pf0,
                                                  const u16* __restrict__ pf1, unsigned* flags) {
  __shared__ float sh[256 + 32 * 65];
  STAMP_BEGIN();
  const int b = blockIdx.x;
  if (threadIdx.x == 0 && b < 256) flags[b * 32] = 0u;
  if (b < 128) mod_block(c, wmod, modp, b >> 2, b & 3, sh);
  else if (b < 256) touch_block((const char*)pf0 + (size_t)(b - 128) * 32768);
  else touch_block((const char*)pf1 + (size_t)(b - 256) * 32768);
  STAMP_END(0);
}

__global__ __launch_bounds__(256) void ln_kernel(const u16* __restrict__ x, const float* modp, int moff, u16* __restrict__ h,
                                                 const u16* __restrict__ pf, int kid) {
  __shared__ float sh[2048];
  STAMP_BEGIN();
  const int tid = threadIdx.x;
  if (blockIdx.x >= 512) { touch_block((const char*)pf + (size_t)(blockIdx.x - 512) * 32768); return; }
  const int lane = tid & 63, wid = tid >> 6;
  const int row = blockIdx.x * 4 + wid;
  const u16* xr = x + (size_t)row * D_;
  const u32x4 v0 = *(const u32x4*)(xr + lane * 8);
  const u32x4 v1 = *(const u32x4*)(xr + 512 + lane * 8);
  {
    const f32x4 s0 = ld_mod4(modp, moff + tid * 4);
    const f32x4 s1 = ld_mod4(modp, moff + 1024 + tid * 4);
    *(f32x4*)(sh + tid * 4) = s0;
    *(f32x4*)(sh + 1024 + tid * 4) = (f32x4){rbf(1.0f + s1[0]), rbf(1.0f + s1[1]), rbf(1.0f + s1[2]), rbf(1.0f + s1[3])};
  }
  float f[16];
#pragma unroll
  for (int j = 0; j < 4; ++j) {
    f[2 * j] = lo_f(v0[j]); f[2 * j + 1] = hi_f(v0[j]);
    f[8 + 2 * j] = lo_f(v1[j]); f[8 + 2 * j + 1] = hi_f(v1[j]);
  }
  float s = 0.f;
#pragma unroll
  for (int j = 0; j < 16; ++j) s += f[j];
#pragma unroll
  for (int o = 32; o >= 1; o >>= 1) s += __shfl_xor(s, o);
  const float mean = s * (1.0f / D_);
  float q = 0.f;
#pragma unroll
  for (int j = 0; j < 16; ++j) { float d = f[j] - mean; q += d * d; }
#pragma unroll
  for (int o = 32; o >= 1; o >>= 1) q += __shfl_xor(q, o);
  const float mb = rbf(mean);
  const float vb = rbf(q * (1.0f / D_));
  const float sd = rbf(sqrtf(rbf(vb + 1e-5f)));
  const float inv = 1.0f / sd;
  __syncthreads();
  float o[16];
#pragma unroll
  for (int half = 0; half < 2; ++half) {
    const int c0 = half * 512 + lane * 8;
    const f32x4 sa0 = *(const f32x4*)(sh + c0), sa1 = *(const f32x4*)(sh + c0 + 4);
    const f32x4 ca0 = *(const f32x4*)(sh + 1024 + c0), ca1 = *(const f32x4*)(sh + 1024 + c0 + 4);
#pragma unroll
    for (int j = 0; j < 8; ++j) {
      const float shf = j < 4 ? sa0[j & 3] : sa1[j & 3];
      const float sc = j < 4 ? ca0[j & 3] : ca1[j & 3];
      float t = rbf(f[half * 8 + j] - mb);
      t = rbf(t * inv);
      t = rbf(t * sc);
      o[half * 8 + j] = t + shf;
    }
  }
  u16* hr = h + (size_t)row * D_;
  u32x4 w0, w1;
#pragma unroll
  for (int j = 0; j < 4; ++j) {
    w0[j] = pack2(o[2 * j], o[2 * j + 1]);
    w1[j] = pack2(o[8 + 2 * j], o[8 + 2 * j + 1]);
  }
  ST16(hr + lane * 8, w0);
  ST16(hr + 512 + lane * 8, w1);
  STAMP_END(kid);
}

struct GemmP {
  const u16* A; const u16* B0; const u16* B1; const u16* res; const float* gate; u16* out; float* slab; unsigned* flags;
};

#define NS0 3
#define NS1 4
#define NS3 4
#define LW0 1
#define LW1 1
#define LW3 1
template <int MODE> struct GCfg;
template <> struct GCfg<0> { static constexpr int BM = 128, BN = 192, WM = 2, WN = 2, NS = NS0, K = 1024, LDA = 1024, LDB = 3072, NTM = 16, NTN = 16, RM = 8, RN = 4, LW = LW0; };
template <> struct GCfg<1> { static constexpr int BM = 128, BN = 64, WM = 2, WN = 2, NS = NS1, K = 1024, LDA = 1024, LDB = 1024, NTM = 16, NTN = 16, RM = 4, RN = 8, LW = LW1; };
template <> struct GCfg<2> { static constexpr int BM = 256, BN = 256, WM = 4, WN = 2, NS = 2, K = 1024, LDA = 1024, LDB = 3584, NTM = 8, NTN = 28, RM = 4, RN = 7, LW = 0; };
template <> struct GCfg<3> { static constexpr int BM = 128, BN = 128, WM = 2, WN = 2, NS = NS3, K = 1792, LDA = 3584, LDB = 1024, NTM = 16, NTN = 16, RM = 4, RN = 8, LW = LW3; };

template <> struct GCfg<4> { static constexpr int BM = 128, BN = 64, WM = 2, WN = 2, NS = NS3, K = 3584, LDA = 3584, LDB = 1024, NTM = 16, NTN = 16, RM = 4, RN = 8, LW = LW3; };

template <int MODE>
__global__ __launch_bounds__(GCfg<MODE>::WM* GCfg<MODE>::WN * 64 * (GCfg<MODE>::LW ? 2 : 1), 2) void gemm_kernel(GemmP p) {
  using C = GCfg<MODE>;
  constexpr int BM = C::BM, BN = C::BN, WM = C::WM, WN = C::WN, NS = C::NS, NW = WM * WN;
  constexpr int TM = BM / WM, TN = BN / WN, MI = TM / 16, NI = TN / 16;
  constexpr int A_BYTES = BM * 128, B_BYTES = BN * 128, ST_BYTES = A_BYTES + B_BYTES;
  constexpr int GA = BM / 8 / NW, GB = BN / 8 / NW, G = GA + GB;
  constexpr int NT = C::K / 64;
  static_assert(BM % (8 * NW) == 0 && BN % (8 * NW) == 0, "stage split");
  static_assert(NT >= NS + 1, "K too small");
  extern __shared__ __attribute__((aligned(16))) char smem[];
  STAMP_BEGIN();

  const int tid = threadIdx.x, lane = tid & 63;
  constexpr bool LW = C::LW != 0;
  const int wid_all = __builtin_amdgcn_readfirstlane(tid >> 6);
  const int wid = wid_all % NW;
  const int wm = wid / WN, wn = wid % WN;
  int tm, tn;
  {
    const int x = blockIdx.x & 7, idx = blockIdx.x >> 3;
    constexpr int NRM = C::NTM / C::RM;
    const int rm = x % NRM, rn = x / NRM;
    tm = rm * C::RM + idx % C::RM;
    tn = rn * C::RN + idx / C::RM;
  }
  int kh = 0;
  if constexpr (MODE == 3) { kh = tn & 1; tn >>= 1; }
  const int m0 = tm * BM;

  const int par = wid & 1;
  const int a_ch = (lane & 7) ^ ((4 * par + (lane >> 4)) & 7);
  const unsigned a_voff = (MODE == 1) ? (unsigned)((((a_ch >> 2) * S_ + (lane >> 3)) * 32 + (a_ch & 3) * 8) * 2)
                                      : (unsigned)(((lane >> 3) * C::LDA + 8 * a_ch) * 2);
  const unsigned b_voff = (unsigned)(((lane >> 3) * C::LDB + 8 * ((lane & 7) ^ ((((lane >> 4) & 1) | (par << 1)) << 1))) * 2);
  const char* Abase = (MODE == 1) ? (const char*)(p.A + (size_t)m0 * 32) : (const char*)(p.A + (size_t)m0 * C::LDA + kh * C::K);
  const unsigned lds0 = (unsigned)(size_t)(LAS char*)smem;

  auto issue1 = [&](auto gi, int t, int bufoff) {
    constexpr int g = decltype(gi)::value;
    if constexpr (g < GA) {
      const int i = wid + NW * g;
      const char* gp = (MODE == 1) ? Abase + ((size_t)(8 * i) * 32 + (size_t)t * 2 * S_ * 32) * 2
                                   : Abase + ((size_t)(8 * i) * C::LDA + (size_t)t * 64) * 2;
      glds16(gp, a_voff, lds0 + bufoff + i * 1024);
    } else {
      const int i = wid + NW * (g - GA);
      const int pn = i >> 3, kr = 8 * (i & 7);
      const u16* bp;
      if constexpr (MODE == 2) bp = (pn < 2 ? p.B0 : p.B1) + tn * 128 + (pn & 1) * 64;
      else bp = p.B0 + (size_t)kh * C::K * C::LDB + tn * BN + pn * 64;
      const char* gp = (const char*)(bp + ((size_t)t * 64 + kr) * C::LDB);
      glds16(gp, b_voff, lds0 + bufoff + A_BYTES + i * 1024);
    }
  };

  if constexpr (C::LW == 1) {
    if (wid_all >= NW) {
      sfor<0, NS>([&](auto sc_) { sfor<0, G>([&](auto gc) { issue1(gc, decltype(sc_)::value, decltype(sc_)::value * ST_BYTES); }); });
      WAIT_V((NS - 1) * G);
      BAR();
      int lb = 0;
      for (int t = 0; t < NT - NS; ++t) {
        WAIT_V((NS - 2) * G);
        BAR();
        sfor<0, G>([&](auto gc) { issue1(gc, t + NS, lb); });
        lb = (lb + ST_BYTES == NS * ST_BYTES) ? 0 : lb + ST_BYTES;
      }
      sfor<0, NS - 1>([&](auto rc) { WAIT_V((NS - 2 - decltype(rc)::value) * G); BAR(); });
      return;
    }
  }
  if constexpr (C::LW == 2) {
    if (wid_all >= NW) {
      static_assert(NS == 3 && (NT & 1) == 0, "reg loader");
      u32x4 R0[G], R1[G];
      auto ld = [&](u32x4* R, int t) {
        sfor<0, G>([&](auto gi) {
          constexpr int g = decltype(gi)::value;
          if constexpr (g < GA) {
            const int i = wid + NW * g;
            R[g] = *(const u32x4*)(Abase + ((size_t)(8 * i) * C::LDA + (size_t)t * 64) * 2 + a_voff);
          } else {
            const int i = wid + NW * (g - GA);
            const int pn = i >> 3, kr = 8 * (i & 7);
            const u16* bp = p.B0 + (size_t)kh * C::K * C::LDB + tn * BN + pn * 64;
            R[g] = *(const u32x4*)((const char*)(bp + ((size_t)t * 64 + kr) * C::LDB) + b_voff);
          }
        });
      };
      auto st = [&](const u32x4* R, int bufoff) {
        sfor<0, G>([&](auto gi) {
          constexpr int g = decltype(gi)::value;
          const int off = g < GA ? (wid + NW * g) * 1024 : A_BYTES + (wid + NW * (g - GA)) * 1024;
          *(LAS u32x4*)(smem + bufoff + off + lane * 16) = R[g];
        });
      };
      ld(R0, 0); ld(R1, 1);
      st(R0, 0); ld(R0, 2);
      WAIT_L0(); BAR();
      st(R1, ST_BYTES); ld(R1, 3);
      st(R0, 2 * ST_BYTES); ld(R0, 4);
      int lb = 0;
      for (int t = 0; t < NT - 4; t += 2) {
        WAIT_L0(); BAR();
        st(R1, lb); if (t + 5 < NT) ld(R1, t + 5);
        lb = (lb + ST_BYTES == NS * ST_BYTES) ? 0 : lb + ST_BYTES;
        WAIT_L0(); BAR();
        st(R0, lb); if (t + 6 < NT) ld(R0, t + 6);
        lb = (lb + ST_BYTES == NS * ST_BYTES) ? 0 : lb + ST_BYTES;
      }
      WAIT_L0(); BAR();
      st(R1, lb);
      WAIT_L0(); BAR();
      BAR();
      return;
    }
  }

  const int fr = lane & 15, fq = lane >> 4;
  u32x4 rv[4];
  f32x4 gv[NI];
  if constexpr (MODE == 1 || MODE == 3 || MODE == 4) {
#pragma unroll
    for (int i = 0; i < 4; ++i) {
      if constexpr (MODE != 3) rv[i] = *(const u32x4*)(p.res + (size_t)(m0 + i * 32 + (tid >> 3)) * D_ + tn * BN + (tid & 7) * 8);
      else rv[i] = *(const u32x4*)(p.res + (size_t)(m0 + kh * 64 + i * 16 + (tid >> 4)) * D_ + tn * BN + (tid & 15) * 8);
    }
#pragma unroll
    for (int ni = 0; ni < NI; ++ni) gv[ni] = ld_mod4(p.gate, tn * BN + wn * TN + ni * 16 + 4 * fq);
  }
  const int a_rd0 = (wm * TM + fr) * 128 + ((fq ^ (fr >> 1)) << 4);
  const int a_rd1 = a_rd0 ^ 64;
  int b_rd[(MODE == 2) ? 4 : NI];
  {
    const int q = (lane & 15) >> 2, pp = lane & 3;
    const int gl = ((q >> 1) & 1) | ((fq & 1) << 1);
#pragma unroll
    for (int ni = 0; ni < ((MODE == 2) ? 4 : NI); ++ni) {
      int pn, un;
      if constexpr (MODE == 2) { pn = wn; un = ni & 3; }
      else { const int j = wn * NI + ni; pn = j >> 2; un = j & 3; }
      b_rd[ni] = A_BYTES + pn * 8192 + (8 * fq + q) * 128 + ((un ^ gl) << 5) + pp * 8;
    }
  }

  f32x4 acc[MI][NI];
#pragma unroll
  for (int mi = 0; mi < MI; ++mi)
#pragma unroll
    for (int ni = 0; ni < NI; ++ni) acc[mi][ni] = (f32x4){0.f, 0.f, 0.f, 0.f};

  constexpr int NSP = (MODE == 2) ? 2 : 1;
  constexpr int NH = NI / NSP, NM = MI * NH, NPH = 2 * NSP;
  constexpr int GW = (NS >= 3) ? NPH : NPH / 2;
  constexpr int GPP = (G + GW - 1) / GW;
  bf16x8 a0[MI], a1[MI], b0[NH], b1[NH];
  s16x4 blo[NH];

  auto rd_b = [&](auto ri, bf16x8* BF, const char* sb, int ks, int hf) {
    constexpr int r = decltype(ri)::value, ni = r >> 1;
    const int bo = (MODE == 2) ? b_rd[ni] + hf * 16384 : b_rd[ni];
    if constexpr ((r & 1) == 0) blo[ni] = tr_read(sb + bo + ks * 4096);
    else BF[ni] = __builtin_shufflevector(blo[ni], tr_read(sb + bo + ks * 4096 + 512), 0, 1, 2, 3, 4, 5, 6, 7);
  };
  auto rd_a = [&](auto ri, bf16x8* AF, const char* sb, int ks) {
    constexpr int mi = decltype(ri)::value;
    AF[mi] = lds_read16(sb + (ks ? a_rd1 : a_rd0) + mi * 2048);
  };
  auto phase = [&](auto hfc, auto rdm, auto g0c, auto g1c, bf16x8* AF, bf16x8* BF, bf16x8* AN_, bf16x8* BN_,
                   const char* rsb, int rks, int rhf, int gt, int gbuf) {
    constexpr int hf = decltype(hfc)::value, RDM = decltype(rdm)::value;
    constexpr int g0 = decltype(g0c)::value, g1 = decltype(g1c)::value, NG = g1 - g0;
    constexpr int NR = RDM == 0 ? 0 : (RDM == 1 ? 2 * NH : 2 * NH + MI);
    sfor<0, NM>([&](auto ic) {
      constexpr int i = decltype(ic)::value, mi = i / NH, ni = i % NH;
      MFMA16(acc[mi][hf * NH + ni], BF[ni], AF[mi]);
      sfor<0, NR>([&](auto rc) {
        constexpr int r = decltype(rc)::value;
        if constexpr ((r * (NM - NM / 4)) / (NR > 0 ? NR : 1) == i) {
          if constexpr (r < 2 * NH) rd_b(rc, BN_, rsb, rks, rhf);
          else rd_a(IC<r - 2 * NH>{}, AN_, rsb, rks);
        }
      });
      sfor<0, NG>([&](auto gc) {
        constexpr int g = decltype(gc)::value;
        if constexpr ((g * NM) / (NG > 0 ? NG : 1) == i) issue1(IC<g0 + g>{}, gt, gbuf);
      });
      SBAR();
    });
  };
  constexpr auto gmin = [](int a, int b) { return a < b ? a : b; };
  int cur = 0, prv = 0;
  auto ktile = [&](auto vmc, auto restc, auto firstc, int t) {
    constexpr int VM = decltype(vmc)::value;
    constexpr bool REST = decltype(restc)::value && !LW, FIRST = decltype(firstc)::value && !LW;
    const int nxt = (cur + ST_BYTES == NS * ST_BYTES) ? 0 : cur + ST_BYTES;
    const char* sc = smem + cur;
    const char* sn = smem + nxt;
    constexpr int R1 = REST ? gmin(G, GPP) : 0, R2 = REST ? gmin(G, 2 * GPP) : 0, R3 = REST ? gmin(G, 3 * GPP) : 0, R4 = REST ? gmin(G, 4 * GPP) : 0;
    constexpr int F1 = FIRST ? gmin(G, GPP) : 0;
    if constexpr (NSP == 1) {
      phase(IC<0>{}, IC<2>{}, IC<R1>{}, IC<R2>{}, a0, b0, a1, b1, sc, 1, 0, t - 1 + NS, prv);
      if constexpr (VM >= 0) {
        if constexpr (!LW) WAIT_V(VM);
        WAIT_L0(); BAR();
        phase(IC<0>{}, IC<2>{}, IC<0>{}, IC<F1>{}, a1, b1, a0, b0, sn, 0, 0, t + NS, cur);
      } else {
        phase(IC<0>{}, IC<0>{}, IC<0>{}, IC<0>{}, a1, b1, a0, b0, sn, 0, 0, 0, 0);
      }
    } else {
      phase(IC<0>{}, IC<1>{}, IC<R1>{}, IC<R2>{}, a0, b0, a1, b1, sc, 0, 1, t - 1 + NS, prv);
      phase(IC<1>{}, IC<2>{}, IC<R2>{}, IC<R3>{}, a0, b1, a1, b0, sc, 1, 0, t - 1 + NS, prv);
      phase(IC<0>{}, IC<1>{}, IC<R3>{}, IC<R4>{}, a1, b0, a1, b1, sc, 1, 1, t - 1 + NS, prv);
      if constexpr (VM >= 0) {
        WAIT_V(VM); WAIT_L0(); BAR();
        phase(IC<1>{}, IC<2>{}, IC<0>{}, IC<F1>{}, a1, b1, a0, b0, sn, 0, 0, t + NS, cur);
      } else {
        phase(IC<1>{}, IC<0>{}, IC<0>{}, IC<0>{}, a1, b1, a0, b0, sn, 0, 0, 0, 0);
      }
    }
    prv = cur; cur = nxt;
  };

  if constexpr (!LW) {
    sfor<0, NS>([&](auto sc_) { sfor<0, G>([&](auto gc) { issue1(gc, decltype(sc_)::value, decltype(sc_)::value * ST_BYTES); }); });
    WAIT_V((NS - 1) * G);
  }
  BAR();
  STAMP_MID(MODE == 0 ? 2 : MODE == 1 ? 4 : MODE == 2 ? 6 : 7, 0);
  sfor<0, 2 * NH>([&](auto rc) { rd_b(rc, b0, smem, 0, 0); });
  sfor<0, MI>([&](auto rc) { rd_a(rc, a0, smem, 0); });
  using TT = IC<1>; using FF = IC<0>;
  ktile(IC<(NS - 2) * G>{}, FF{}, TT{}, 0);
  for (int t = 1; t < NT - NS; ++t) ktile(IC<(NS - 2) * G>{}, TT{}, TT{}, t);
  ktile(IC<(NS - 2) * G>{}, TT{}, FF{}, NT - NS);
  sfor<0, NS - 2>([&](auto rc) { ktile(IC<(NS - 3 - decltype(rc)::value) * G>{}, FF{}, FF{}, 0); });
  ktile(IC<-1>{}, FF{}, FF{}, 0);

  STAMP_MID(MODE == 0 ? 2 : MODE == 1 ? 4 : MODE == 2 ? 6 : 7, 1);
  if constexpr (MODE == 0) {
    const float qs = 0.17677669529663687f * 1.4426950408889634f;
    BAR();
    char* st = smem + wid * (64 * 208);
#pragma unroll
    for (int ni = 0; ni < NI; ++ni) {
      const int n = tn * BN + wn * TN + ni * 16;
      const float sc = n < 1024 ? qs : 1.0f;
#pragma unroll
      for (int mi = 0; mi < MI; ++mi) {
        u32x2 w;
        w[0] = pack2(acc[mi][ni][0] * sc, acc[mi][ni][1] * sc);
        w[1] = pack2(acc[mi][ni][2] * sc, acc[mi][ni][3] * sc);
        *(LAS u32x2*)(st + (mi * 16 + fr) * 208 + ni * 32 + fq * 8) = w;
      }
    }
#pragma unroll
    for (int j = 0; j < 3; ++j) {
      const int hh = (tn * BN + wn * TN + j * 32) >> 5;
      u16* ob = p.out + ((size_t)hh * S_ + m0 + wm * TM) * 32 + lane * 8;
#pragma unroll
      for (int i = 0; i < 4; ++i) {
        const u32x4 v = *(const LAS u32x4*)(st + (i * 16 + (lane >> 2)) * 208 + j * 64 + (lane & 3) * 16);
        ST16(ob + i * 512, v);
      }
    }
  } else if constexpr (MODE == 3) {
    const int tile = tm * 8 + tn;
    if (wm != kh) {
      float* dst = p.slab + ((size_t)(tile * 2 + kh) * 2 + wn) * 4096 + lane * 4;
#pragma unroll
      for (int mi = 0; mi < MI; ++mi)
#pragma unroll
        for (int ni = 0; ni < NI; ++ni) ST16(dst + (mi * NI + ni) * 256, __builtin_bit_cast(u32x4, acc[mi][ni]));
      WAIT_V(0);
      if (lane == 0) __hip_atomic_fetch_add(p.flags + (tile * 2 + kh) * 32, 1u, __ATOMIC_RELAXED, __HIP_MEMORY_SCOPE_AGENT);
      BAR();
    } else {
      const unsigned* fl = p.flags + (tile * 2 + (kh ^ 1)) * 32;
      for (int spin = 0; spin < (1 << 22); ++spin) {
        if (__builtin_amdgcn_readfirstlane(__hip_atomic_load(fl, __ATOMIC_RELAXED, __HIP_MEMORY_SCOPE_AGENT)) >= 2u) break;
        __builtin_amdgcn_s_sleep(1);
      }
      const float* src = p.slab + ((size_t)(tile * 2 + (kh ^ 1)) * 2 + wn) * 4096 + lane * 4;
      f32x4 pv[MI][NI];
#pragma unroll
      for (int mi = 0; mi < MI; ++mi)
#pragma unroll
        for (int ni = 0; ni < NI; ++ni)
          asm volatile("global_load_dwordx4 %0, %1, off sc0 sc1" : "=v"(pv[mi][ni]) : "v"(src + (mi * NI + ni) * 256) : "memory");
      BAR();
      asm volatile("s_waitcnt vmcnt(0)" ::: "memory");
      SBAR();
#pragma unroll
      for (int ni = 0; ni < NI; ++ni)
#pragma unroll
        for (int mi = 0; mi < MI; ++mi) {
          asm volatile("" : "+v"(pv[mi][ni]));
          const f32x4 a = acc[mi][ni] + pv[mi][ni];
          u32x2 w;
          w[0] = pack2(gv[ni][0] * rbf(a[0]), gv[ni][1] * rbf(a[1]));
          w[1] = pack2(gv[ni][2] * rbf(a[2]), gv[ni][3] * rbf(a[3]));
          *(LAS u32x2*)(smem + (mi * 16 + fr) * 272 + wn * 128 + ni * 32 + fq * 8) = w;
        }
    }
    WAIT_L0();
    BAR();
#pragma unroll
    for (int i = 0; i < 4; ++i) {
      const u32x4 v = *(const LAS u32x4*)(smem + (i * 16 + (tid >> 4)) * 272 + (tid & 15) * 16);
      u32x4 w;
#pragma unroll
      for (int j = 0; j < 4; ++j) w[j] = pack2(lo_f(rv[i][j]) + lo_f(v[j]), hi_f(rv[i][j]) + hi_f(v[j]));
      ST16(p.out + (size_t)(m0 + kh * 64 + i * 16 + (tid >> 4)) * D_ + tn * BN + (tid & 15) * 8, w);
    }
  } else if constexpr (MODE == 1 || MODE == 4) {
    BAR();
#pragma unroll
    for (int ni = 0; ni < NI; ++ni) {
#pragma unroll
      for (int mi = 0; mi < MI; ++mi) {
        u32x2 w;
        w[0] = pack2(gv[ni][0] * rbf(acc[mi][ni][0]), gv[ni][1] * rbf(acc[mi][ni][1]));
        w[1] = pack2(gv[ni][2] * rbf(acc[mi][ni][2]), gv[ni][3] * rbf(acc[mi][ni][3]));
        *(LAS u32x2*)(smem + (wm * TM + mi * 16 + fr) * 144 + wn * 64 + ni * 32 + fq * 8) = w;
      }
    }
    WAIT_L0();
    BAR();
#pragma unroll
    for (int i = 0; i < 4; ++i) {
      const u32x4 v = *(const LAS u32x4*)(smem + (i * 32 + (tid >> 3)) * 144 + (tid & 7) * 16);
      u32x4 w;
#pragma unroll
      for (int j = 0; j < 4; ++j) w[j] = pack2(lo_f(rv[i][j]) + lo_f(v[j]), hi_f(rv[i][j]) + hi_f(v[j]));
      ST16(p.out + (size_t)(m0 + i * 32 + (tid >> 3)) * D_ + tn * BN + (tid & 7) * 8, w);
    }
  } else {
    BAR();
    char* st = smem + wid * (64 * 144);
#pragma unroll
    for (int ni = 0; ni < 4; ++ni)
#pragma unroll
      for (int mi = 0; mi < MI; ++mi) {
        float o[4];
#pragma unroll
        for (int r = 0; r < 4; ++r) o[r] = rbf(acc[mi][ni][r]) * rbf(acc[mi][ni + 4][r]);
        u32x2 w;
        w[0] = pack2(o[0], o[1]);
        w[1] = pack2(o[2], o[3]);
        *(LAS u32x2*)(st + (mi * 16 + fr) * 144 + ni * 32 + fq * 8) = w;
      }
    u16* ob = p.out + (size_t)(m0 + wm * TM + (lane >> 3)) * F_ + tn * 128 + wn * 64 + (lane & 7) * 8;
#pragma unroll
    for (int i = 0; i < 8; ++i) {
      const u32x4 v = *(const LAS u32x4*)(st + (i * 8 + (lane >> 3)) * 144 + (lane & 7) * 16);
      ST16(ob + (size_t)i * 8 * F_, v);
    }
  }
  STAMP_END(MODE == 0 ? 2 : MODE == 1 ? 4 : MODE == 2 ? 6 : 7);
}

#define AT_NSTAGE 6
constexpr int AT_KT = 128, AT_NS = AT_NSTAGE, AT_ST = 16384;
#define MFMA32(a, b, c) __builtin_amdgcn_mfma_f32_32x32x16_bf16(a, b, c, 0, 0, 0)
#define AEXP(x) __builtin_amdgcn_exp2f(x)
#define MX3(a, b, c) __builtin_fmaxf(__builtin_fmaxf((a), (b)), (c))

__device__ __forceinline__ float tile_rowmax(const f32x16& s0, const f32x16& s1) {
  float a = MX3(s0[0], s0[1], s1[0]), b = MX3(s0[2], s0[3], s1[1]);
  a = MX3(a, s1[2], s1[3]);
#pragma unroll
  for (int r = 4; r < 16; r += 4) {
    a = MX3(a, s0[r], s0[r + 1]); b = MX3(b, s0[r + 2], s0[r + 3]);
    a = MX3(a, s1[r], s1[r + 1]); b = MX3(b, s1[r + 2], s1[r + 3]);
  }
  float mx = __builtin_fmaxf(a, b);
  auto rr = __builtin_amdgcn_permlane32_swap(__float_as_uint(mx), __float_as_uint(mx), false, false);
  return __builtin_fmaxf(__uint_as_float(rr[0]), __uint_as_float(rr[1]));
}

__global__ __launch_bounds__(512, 2) void attn_kernel(const u16* __restrict__ qkv, u16* __restrict__ o) {
  extern __shared__ __attribute__((aligned(16))) char smem[];
  STAMP_BEGIN();
  const int tid = threadIdx.x, lane = tid & 63;
  const int wid = __builtin_amdgcn_readfirstlane(tid >> 6);
  const int x = blockIdx.x & 7, idx = blockIdx.x >> 3;
  const int hd = x * 4 + (idx >> 3), qb = idx & 7;
  const int r32 = lane & 31, hi = lane >> 5;
  const u16* Q = qkv + ((size_t)(0 * 32 + hd) * S_) * 32;
  const u16* Kp = qkv + ((size_t)(1 * 32 + hd) * S_) * 32;
  const u16* Vp = qkv + ((size_t)(2 * 32 + hd) * S_) * 32;
  const int q0 = qb * 256 + wid * 32;

  const unsigned k_voff = (unsigned)(((lane >> 2) * 32 + 8 * ((lane & 3) ^ ((lane >> 4) & 3))) * 2);
  const unsigned v_voff = (unsigned)(lane * 16);
  const unsigned lds0 = (unsigned)(size_t)(LAS char*)smem;
  auto issue = [&](int t, int bufoff) {
    const char* gk = (const char*)(Kp + ((size_t)t * AT_KT + wid * 16) * 32);
    const char* gv = (const char*)(Vp + ((size_t)t * AT_KT + wid * 16) * 32);
    glds16(gk, k_voff, lds0 + bufoff + wid * 1024);
    glds16(gv, v_voff, lds0 + bufoff + 8192 + wid * 1024);
  };
#pragma unroll
  for (int s = 0; s < AT_NS - 1; ++s) issue(s, s * AT_ST);
  const bf16x8 qf0 = *(const bf16x8*)(Q + (size_t)(q0 + r32) * 32 + hi * 8);
  const bf16x8 qf1 = *(const bf16x8*)(Q + (size_t)(q0 + r32) * 32 + 16 + hi * 8);

  const int k_rd0 = r32 * 64 + ((hi ^ ((r32 >> 2) & 3)) << 4);
  const int k_rd1 = k_rd0 ^ 32;
  const int v_rd = 8192 + (4 * hi + ((lane & 15) >> 2)) * 64 + ((lane >> 4) & 1) * 32 + (lane & 3) * 8;

  f32x16 oacc = {};
  f32x16 negm;
  float m, l = 0.f;
  f32x16 sa0, sa1, sb0, sb1;
  u32x4 pp0 = {}, pp1 = {}, pp2 = {}, pp3 = {};
  bf16x8 vf0, vf1, vf2, vf3;
#define AT_BIG 1.0e30f
  constexpr float BIG = AT_BIG;
  constexpr int NT = S_ / AT_KT;

#define SCORES(D0, D1, kb, CIN)                                                         \
  do {                                                                                  \
    bf16x8 k00 = lds_read16((kb) + k_rd0), k01 = lds_read16((kb) + k_rd1);              \
    bf16x8 k10 = lds_read16((kb) + 2048 + k_rd0), k11 = lds_read16((kb) + 2048 + k_rd1);\
    D0 = MFMA32(k00, qf0, CIN); D1 = MFMA32(k10, qf0, CIN);                             \
    D0 = MFMA32(k01, qf1, D0);  D1 = MFMA32(k11, qf1, D1);                              \
  } while (0)
#define VFRAG(vb, s) __builtin_shufflevector(tr_read((vb) + (s) * 1024), tr_read((vb) + (s) * 1024 + 512), 0, 1, 2, 3, 4, 5, 6, 7)
#define READV(vb) do { vf0 = VFRAG(vb, 0); vf1 = VFRAG(vb, 1); vf2 = VFRAG(vb, 2); vf3 = VFRAG(vb, 3); } while (0)
#define PV()                                                                            \
  do {                                                                                  \
    oacc = MFMA32(vf0, __builtin_bit_cast(bf16x8, pp0), oacc);                          \
    oacc = MFMA32(vf1, __builtin_bit_cast(bf16x8, pp1), oacc);                          \
    oacc = MFMA32(vf2, __builtin_bit_cast(bf16x8, pp2), oacc);                          \
    oacc = MFMA32(vf3, __builtin_bit_cast(bf16x8, pp3), oacc);                          \
  } while (0)
#define EXPSUM(C0, C1, LS)                                                              \
  do {                                                                                  \
    f32x2 ls2_ = {0.f, 0.f}, ls3_ = {0.f, 0.f};                                         \
    _Pragma("unroll") for (int r = 0; r < 16; r += 2) {                                 \
      C0[r] = AEXP(C0[r]); C0[r + 1] = AEXP(C0[r + 1]);                                 \
      C1[r] = AEXP(C1[r]); C1[r + 1] = AEXP(C1[r + 1]);                                 \
      ls2_ += (f32x2){C0[r], C0[r + 1]};                                                \
      ls3_ += (f32x2){C1[r], C1[r + 1]};                                                \
    }                                                                                   \
    ls2_ += ls3_;                                                                       \
    LS = ls2_[0] + ls2_[1];                                                             \
  } while (0)
#define PACK(C0, C1)                                                                    \
  do {                                                                                  \
    _Pragma("unroll") for (int j = 0; j < 4; ++j) {                                     \
      pp0[j] = pack2(C0[2 * j], C0[2 * j + 1]);                                         \
      pp1[j] = pack2(C0[8 + 2 * j], C0[8 + 2 * j + 1]);                                 \
      pp2[j] = pack2(C1[2 * j], C1[2 * j + 1]);                                         \
      pp3[j] = pack2(C1[8 + 2 * j], C1[8 + 2 * j + 1]);                                 \
    }                                                                                   \
  } while (0)
#define STEP(C0, C1, N0, N1, kcur, knext, vprev, HASN)                                  \
  do {                                                                                  \
    READV(vprev);                                                                       \
    if (HASN) SCORES(N0, N1, knext, negm);                                              \
    PV();                                                                               \
    float ls;                                                                           \
    EXPSUM(C0, C1, ls);                                                                 \
    if (__builtin_expect(__any(!(ls <= BIG)), 0)) {                                     \
                  \
      f32x16 z_ = {};                                                                   \
      SCORES(C0, C1, kcur, z_);                                                         \
      const float mn = __builtin_fmaxf(m, tile_rowmax(C0, C1));                         \
      const float dm = m - mn;                                                          \
      const float f = __builtin_amdgcn_exp2f(dm);                                       \
      m = mn; l *= f;                                                                   \
      _Pragma("unroll") for (int r = 0; r < 16; ++r) {                                  \
        oacc[r] *= f; negm[r] = -mn; C0[r] -= mn; C1[r] -= mn;                          \
        if (HASN) { N0[r] += dm; N1[r] += dm; }                                         \
      }                                                                                 \
      EXPSUM(C0, C1, ls);                                                               \
    }                                                                                   \
    l += ls;                                                                            \
    PACK(C0, C1);                                                                       \
  } while (0)

  WAIT_V(2 * (AT_NS - 2));
  BAR();
  {
    f32x16 z = {};
    SCORES(sa0, sa1, smem, z);
    m = tile_rowmax(sa0, sa1);
#pragma unroll
    for (int r = 0; r < 16; ++r) { negm[r] = -m; sa0[r] -= m; sa1[r] -= m; }
  }
  int cur = 0, prv = 0;
  for (int t = 0; t < NT - 1; ++t) {
    const char* sb = smem + cur;
    int nx = cur + AT_ST; if (nx >= AT_NS * AT_ST) nx = 0;
    int prv2 = cur - AT_ST; if (prv2 < 0) prv2 = (AT_NS - 1) * AT_ST;
    STEP(sa0, sa1, sb0, sb1, sb, sb + 4096, smem + prv + 4096 + v_rd, true);
    if (t + AT_NS - 1 < NT) {
      WAIT_V(2 * (AT_NS - 3));
      WAIT_L0();
      BAR();
      issue(t + AT_NS - 1, prv2);
    } else {
      WAIT_V(0);
      WAIT_L0();
      BAR();
    }
    STEP(sb0, sb1, sa0, sa1, sb + 4096, smem + nx, sb + v_rd, true);
    prv = cur; cur = nx;
  }
  {
    const char* sb = smem + cur;
    STEP(sa0, sa1, sb0, sb1, sb, sb + 4096, smem + prv + 4096 + v_rd, true);
    STEP(sb0, sb1, sa0, sa1, sb + 4096, sb, sb + v_rd, false);
    prv = cur;
  }
  READV(smem + prv + 4096 + v_rd);
  PV();
#undef SCORES
#undef VFRAG
#undef READV
#undef PV
#undef EXPSUM
#undef PACK
#undef STEP
  {
    auto rr = __builtin_amdgcn_permlane32_swap(__float_as_uint(l), __float_as_uint(l), false, false);
    l = __uint_as_float(rr[0]) + __uint_as_float(rr[1]);
  }
  const float inv = 1.0f / l;
  u16* op = o + (size_t)(q0 + r32) * D_ + hd * 32 + 4 * hi;
#pragma unroll
  for (int g = 0; g < 4; ++g) {
    u32x2 w;
    w[0] = pack2(oacc[4 * g] * inv, oacc[4 * g + 1] * inv);
    w[1] = pack2(oacc[4 * g + 2] * inv, oacc[4 * g + 3] * inv);
    *(u32x2*)(op + 8 * g) = w;
  }
  STAMP_END(3);
}

constexpr int A2_KT = 256, A2_NS = 3, A2_ST = 32768, A2_MOD = A2_NS * A2_ST + 1024, A2_RED = A2_MOD + 32768, A2_FLG = A2_RED + 4096, A2_LDS = A2_FLG + 64;
template <bool SAFE>
__device__ __forceinline__ void attn_pass(char* smem, const u16* Kp, const u16* Vp, bf16x8 qf0, bf16x8 qf1, int wid, int lane,
                                          f32x16& oacc, float& m, float& l,
                                          const u16* pw0, const u16* pw1, const u16* pw2, const u16* pw3, const u16* wm, unsigned cc) {
  const int wk = wid >> 3, r32 = lane & 31, hi = lane >> 5;
  const unsigned k_voff = (unsigned)(((lane >> 2) * 32 + 8 * ((lane & 3) ^ ((lane >> 4) & 3))) * 2);
  const unsigned v_voff = (unsigned)(lane * 16);
  const unsigned lds0 = (unsigned)(size_t)(LAS char*)smem;
  auto issue = [&](int t, int bufoff) {
    const char* gk = (const char*)(Kp + ((size_t)t * A2_KT + wid * 16) * 32);
    const char* gv = (const char*)(Vp + ((size_t)t * A2_KT + wid * 16) * 32);
    glds16(gk, k_voff, lds0 + bufoff + wid * 1024);
    glds16(gv, v_voff, lds0 + bufoff + 16384 + wid * 1024);
  };
  issue(0, 0);
  issue(1, A2_ST);
  const int k_rd0 = wk * 8192 + r32 * 64 + ((hi ^ ((r32 >> 2) & 3)) << 4);
  const int k_rd1 = k_rd0 ^ 32;
  const int v_rd = 16384 + wk * 8192 + (4 * hi + ((lane & 15) >> 2)) * 64 + ((lane >> 4) & 1) * 32 + (lane & 3) * 8;
  f32x16 negm = {};
  f32x2 lsa = {0.f, 0.f}, lsb = {0.f, 0.f};
  oacc = (f32x16){};
  m = 0.f; l = 0.f;
  constexpr int NT = S_ / A2_KT;
  int cur = 0;
  for (int t = 0; t < NT; ++t) {
    if (t == 0) WAIT_V(2); else if (!SAFE && t == 4) WAIT_V(5); else if (t + 1 < NT) WAIT_V(3); else WAIT_V(0);
    BAR();
    if (!SAFE && t == 6) {
      const int rg = wid * 8 + (lane >> 3), cl = lane & 7;
      const float c0 = lo_f(cc), c1 = hi_f(cc);
      const float s0 = rbf(c0 / (1.0f + __expf(-c0))), s1 = rbf(c1 / (1.0f + __expf(-c1)));
      const u32x4 w0 = *(const LAS u32x4*)(smem + A2_MOD + (2 * rg) * 128 + cl * 16);
      const u32x4 w1 = *(const LAS u32x4*)(smem + A2_MOD + (2 * rg + 1) * 128 + cl * 16);
      float a[8];
#pragma unroll
      for (int j = 0; j < 4; ++j) {
        a[2 * j] = s0 * lo_f(w0[j]) + s1 * lo_f(w1[j]);
        a[2 * j + 1] = s0 * hi_f(w0[j]) + s1 * hi_f(w1[j]);
      }
#pragma unroll
      for (int j = 0; j < 8; ++j) {
        a[j] += __shfl_xor(a[j], 8); a[j] += __shfl_xor(a[j], 16); a[j] += __shfl_xor(a[j], 32);
      }
      if (lane < 8) {
        float* mred = (float*)(smem + A2_RED);
#pragma unroll
        for (int j = 0; j < 8; ++j) mred[wid * 64 + lane * 8 + j] = a[j];
      }
    }
    if (!SAFE && t == 3) {
      const unsigned m_voff = (unsigned)(((lane >> 3) * 6144 + (lane & 7) * 8) * 2);
      glds16(wm + (size_t)(wid * 16) * 6144, m_voff, lds0 + A2_MOD + wid * 2048);
      glds16(wm + (size_t)(wid * 16 + 8) * 6144, m_voff, lds0 + A2_MOD + wid * 2048 + 1024);
    }
    {
      int ck = (blockIdx.x * NT + t) * 16 + wid;
      if (ck >= 2048 + 3 * 7168) ck -= 16384;
      const u16* pb = pw0;
      if (ck >= 2048) { pb = pw1; ck -= 2048; if (ck >= 7168) { pb = pw2; ck -= 7168; if (ck >= 7168) { pb = pw3; ck -= 7168; } } }
      glds16((const char*)pb + (size_t)ck * 1024, v_voff, lds0 + A2_NS * A2_ST);
      int nb = cur + 2 * A2_ST; if (nb >= A2_NS * A2_ST) nb -= A2_NS * A2_ST;
      if (t + 2 < NT) issue(t + 2, nb);
    }
#pragma unroll
    for (int hb = 0; hb < 2; ++hb) {
      const char* kb = smem + cur + hb * 4096;
      const bf16x8 k00 = lds_read16(kb + k_rd0), k01 = lds_read16(kb + k_rd1);
      const bf16x8 k10 = lds_read16(kb + 2048 + k_rd0), k11 = lds_read16(kb + 2048 + k_rd1);
      f32x16 s0, s1;
      if (SAFE || (t == 0 && hb == 0)) {
        f32x16 z_ = {};
        s0 = MFMA32(k00, qf0, z_); s1 = MFMA32(k10, qf0, z_);
        s0 = MFMA32(k01, qf1, s0); s1 = MFMA32(k11, qf1, s1);
        const float tm = tile_rowmax(s0, s1);
        const bool first = (t == 0 && hb == 0);
        const float mn = first ? tm : __builtin_fmaxf(m, tm);
        if (SAFE) {
          const float f = first ? 0.f : __builtin_amdgcn_exp2f(m - mn);
          lsa *= f; lsb *= f;
          oacc *= f;
        }
        m = mn;
#pragma unroll
        for (int r = 0; r < 16; ++r) { negm[r] = -mn; s0[r] -= mn; s1[r] -= mn; }
      } else {
        s0 = MFMA32(k00, qf0, negm); s1 = MFMA32(k10, qf0, negm);
        s0 = MFMA32(k01, qf1, s0); s1 = MFMA32(k11, qf1, s1);
      }
#pragma unroll
      for (int r = 0; r < 16; r += 2) {
        s0[r] = AEXP(s0[r]); s0[r + 1] = AEXP(s0[r + 1]);
        s1[r] = AEXP(s1[r]); s1[r + 1] = AEXP(s1[r + 1]);
        lsa += (f32x2){s0[r], s0[r + 1]};
        lsb += (f32x2){s1[r], s1[r + 1]};
      }
      u32x4 p0, p1, p2, p3;
#pragma unroll
      for (int j = 0; j < 4; ++j) {
        p0[j] = pack2(s0[2 * j], s0[2 * j + 1]);
        p1[j] = pack2(s0[8 + 2 * j], s0[8 + 2 * j + 1]);
        p2[j] = pack2(s1[2 * j], s1[2 * j + 1]);
        p3[j] = pack2(s1[8 + 2 * j], s1[8 + 2 * j + 1]);
      }
      const char* vb = smem + cur + hb * 4096 + v_rd;
#define VFRAG(s) __builtin_shufflevector(tr_read(vb + (s) * 1024), tr_read(vb + (s) * 1024 + 512), 0, 1, 2, 3, 4, 5, 6, 7)
      oacc = MFMA32(VFRAG(0), __builtin_bit_cast(bf16x8, p0), oacc);
      oacc = MFMA32(VFRAG(1), __builtin_bit_cast(bf16x8, p1), oacc);
      oacc = MFMA32(VFRAG(2), __builtin_bit_cast(bf16x8, p2), oacc);
      oacc = MFMA32(VFRAG(3), __builtin_bit_cast(bf16x8, p3), oacc);
#undef VFRAG
    }
    WAIT_L0();
    cur += A2_ST; if (cur >= A2_NS * A2_ST) cur = 0;
  }
  lsa += lsb;
  l = lsa[0] + lsa[1];
  {
    auto rr = __builtin_amdgcn_permlane32_swap(__float_as_uint(l), __float_as_uint(l), false, false);
    l = __uint_as_float(rr[0]) + __uint_as_float(rr[1]);
  }
  WAIT_V(0);
}

__global__ __launch_bounds__(1024, 4) void attn2_kernel(const u16* __restrict__ qkv, u16* __restrict__ o,
                                                        const u16* pw0, const u16* pw1, const u16* pw2, const u16* pw3,
                                                        const u16* __restrict__ cvec, const u16* wmod, float* __restrict__ modp) {
  extern __shared__ __attribute__((aligned(16))) char smem[];
  STAMP_BEGIN();
  const int tid = threadIdx.x, lane = tid & 63;
  const int wid = __builtin_amdgcn_readfirstlane(tid >> 6);
  const int wq = wid & 7, wk = wid >> 3;
  const int x = blockIdx.x & 7, idx = blockIdx.x >> 3;
  const int hd = x * 4 + (idx >> 3), qb = idx & 7;
  const int r32 = lane & 31, hi = lane >> 5;
  const u16* Q = qkv + ((size_t)(0 * 32 + hd) * S_) * 32;
  const u16* Kp = qkv + ((size_t)(1 * 32 + hd) * S_) * 32;
  const u16* Vp = qkv + ((size_t)(2 * 32 + hd) * S_) * 32;
  const int q0 = qb * 256 + wq * 32;
  const bf16x8 qf0 = *(const bf16x8*)(Q + (size_t)(q0 + r32) * 32 + hi * 8);
  const bf16x8 qf1 = *(const bf16x8*)(Q + (size_t)(q0 + r32) * 32 + 16 + hi * 8);

  f32x16 oacc;
  float m, l;
  const int mcg = 32 + (blockIdx.x >> 2), mks = blockIdx.x & 3;
  const u16* wm = wmod + (size_t)(mks * 256) * 6144 + mcg * 64;
  unsigned cc = *(const unsigned*)(cvec + mks * 256 + 2 * (tid >> 3));
  asm volatile("" : "+v"(cc));
  attn_pass<false>(smem, Kp, Vp, qf0, qf1, wid, lane, oacc, m, l, pw0, pw1, pw2, pw3, wm, cc);
  float* xb = (float*)smem + wq * (20 * 64);
  const float* mred = (const float*)(smem + A2_RED);
  volatile LAS int* badf = (volatile LAS int*)(smem + A2_FLG);
  {
    float chk = l;
#pragma unroll
    for (int r = 0; r < 16; ++r) chk += __builtin_fabsf(oacc[r]);
    const int bad = __any(!(chk < 1.0e37f));
    if (lane == 0) badf[wid] = bad;
    __syncthreads();
    const int anybad = __any(badf[lane & 15] != 0);
    if (anybad) {
      __syncthreads();
      attn_pass<true>(smem, Kp, Vp, qf0, qf1, wid, lane, oacc, m, l, pw0, pw1, pw2, pw3, wm, cc);
      __syncthreads();
    }
  }
  xb[wk * 64 + lane] = m; xb[(2 + wk) * 64 + lane] = l;
#pragma unroll
  for (int r = 0; r < 8; ++r) xb[(4 + 8 * wk + r) * 64 + lane] = wk ? oacc[r] : oacc[8 + r];
  __syncthreads();
  {
    const float m2 = xb[(wk ^ 1) * 64 + lane], l2 = xb[(2 + (wk ^ 1)) * 64 + lane];
    const float mn = __builtin_fmaxf(m, m2);
    const float f1 = __builtin_amdgcn_exp2f((m - mn) * 0.5f), f2 = __builtin_amdgcn_exp2f((m2 - mn) * 0.5f);
    const float inv = 1.0f / ((l * f1) * f1 + (l2 * f2) * f2);
    u16* op = o + ((size_t)hd * S_ + q0 + r32) * 32 + 4 * hi + 16 * wk;
#pragma unroll
    for (int g = 0; g < 2; ++g) {
      float v[4];
#pragma unroll
      for (int e = 0; e < 4; ++e) {
        const float own = wk ? oacc[8 + 4 * g + e] : oacc[4 * g + e];
        v[e] = ((own * f1) * f1 + (xb[(4 + 8 * (wk ^ 1) + 4 * g + e) * 64 + lane] * f2) * f2) * inv;
      }
      u32x2 w;
      w[0] = pack2(v[0], v[1]);
      w[1] = pack2(v[2], v[3]);
      *(u32x2*)(op + 8 * g) = w;
    }
  }
  if (wid == 15) {
    float sacc = 0.f;
#pragma unroll
    for (int w = 0; w < 16; ++w) sacc += mred[w * 64 + lane];
    modp[mks * 6144 + mcg * 64 + lane] = sacc;
  }
  STAMP_END(3);
}

template <int MODE> static void launch_gemm(const GemmP& p, hipStream_t stream) {
  using C = GCfg<MODE>;
  constexpr int lds = C::NS * (C::BM + C::BN) * 128;
  hipFuncSetAttribute((const void*)gemm_kernel<MODE>, hipFuncAttributeMaxDynamicSharedMemorySize, lds);
  gemm_kernel<MODE><<<C::NTM * C::NTN, C::WM * C::WN * 64 * (C::LW ? 2 : 1), lds, stream>>>(p);
}

extern "C" void kernel_launch(void* const* d_in, const int* in_sizes, int n_in, void* d_out, int out_size,
                              void* d_ws, size_t ws_size, hipStream_t stream) {
  const u16* x = (const u16*)d_in[0];
  const u16* wqkv = (const u16*)d_in[1];
  const u16* wo = (const u16*)d_in[2];
  const u16* wg = (const u16*)d_in[4];
  const u16* wu = (const u16*)d_in[5];
  const u16* wd = (const u16*)d_in[6];
  const u16* c = (const u16*)d_in[7];
  const u16* wmod = (const u16*)d_in[8];
  u16* out = (u16*)d_out;

  char* ws = (char*)d_ws;
  float* mod = (float*)ws;
  u16* h = (u16*)(ws + (1 << 20));
  u16* qkv = (u16*)(ws + (8 << 20));
  u16* ao = (u16*)(ws + (24 << 20));
  u16* x1 = (u16*)(ws + (32 << 20));
  u16* hh = (u16*)(ws + (40 << 20));
  float* slab = (float*)(ws + (64 << 20));
  unsigned* flags = (unsigned*)(ws + (512 << 10));

  mod_kernel<<<256, 256, 0, stream>>>(c, wmod, mod, x, wqkv, flags);
  ln_kernel<<<512 + 192, 256, 0, stream>>>(x, mod, 0, h, wqkv, 1);
  { GemmP p{h, wqkv, nullptr, nullptr, nullptr, qkv, nullptr, nullptr}; launch_gemm<0>(p, stream); }
  hipFuncSetAttribute((const void*)attn2_kernel, hipFuncAttributeMaxDynamicSharedMemorySize, A2_LDS);
  attn2_kernel<<<256, 1024, A2_LDS, stream>>>(qkv, ao, wo, wg, wu, wd, c, wmod, mod);
  { GemmP p{ao, wo, nullptr, x, mod + 2 * 1024, x1, nullptr, nullptr}; launch_gemm<1>(p, stream);
  }
  ln_kernel<<<512, 256, 0, stream>>>(x1, mod, 3 * 1024, h, wqkv, 5);
  { GemmP p{h, wg, wu, nullptr, nullptr, hh, nullptr, nullptr}; launch_gemm<2>(p, stream); }
  { GemmP p{hh, wd, nullptr, x1, mod + 5 * 1024, out, slab, flags}; launch_gemm<4>(p, stream); }
}
```

```cpp
#define NS1 3
#define NS3 3
#include <hip/hip_runtime.h>
#include <hip/hip_bf16.h>
#include <stdint.h>

typedef unsigned short u16;
typedef short bf16x8 __attribute__((ext_vector_type(8)));
typedef short s16x4 __attribute__((ext_vector_type(4)));
typedef float f32x2 __attribute__((ext_vector_type(2)));
typedef float f32x4 __attribute__((ext_vector_type(4)));
typedef float f32x16 __attribute__((ext_vector_type(16)));
typedef unsigned u32x2 __attribute__((ext_vector_type(2)));
typedef unsigned u32x4 __attribute__((ext_vector_type(4)));
typedef __bf16 bf16x2_t __attribute__((ext_vector_type(2)));
#define LAS __attribute__((address_space(3)))
#define GAS __attribute__((address_space(1)))

constexpr int S_ = 2048, D_ = 1024, H_ = 32, DH_ = 32, F_ = 3584;

__device__ __forceinline__ float bf2f(u16 u) { return __uint_as_float((unsigned)u << 16); }
__device__ __forceinline__ unsigned pack2(float lo, float hi) {
  f32x2 v = {lo, hi};
  bf16x2_t b = __builtin_convertvector(v, bf16x2_t);
  return __builtin_bit_cast(unsigned, b);
}
__device__ __forceinline__ float rbf(float f) {
  __bf16 b = (__bf16)f;
  return __uint_as_float((unsigned)__builtin_bit_cast(u16, b) << 16);
}
__device__ __forceinline__ float lo_f(unsigned u) { return __uint_as_float(u << 16); }
__device__ __forceinline__ float hi_f(unsigned u) { return __uint_as_float(u & 0xffff0000u); }

#define STAMP_BEGIN()
#define STAMP_END(kid)
#define STAMP_MID(kid, i)
#define WAIT_V(n) asm volatile("s_waitcnt vmcnt(%0)" ::"n"(n) : "memory")
#define WAIT_L0() asm volatile("s_waitcnt lgkmcnt(0)" ::: "memory")
#define BAR() __builtin_amdgcn_s_barrier()

__device__ __forceinline__ void glds16(const void* sbase, unsigned voff, unsigned ldsaddr) {
  asm volatile("s_mov_b32 m0, %2\n\ts_nop 0\n\tglobal_load_lds_dwordx4 %0, %1" ::"v"(voff), "s"(sbase), "s"(ldsaddr) : "memory");
}
#define MFMA16(c, a, b) c = __builtin_amdgcn_mfma_f32_16x16x32_bf16(a, b, c, 0, 0, 0)
#define ST16(p, v) asm volatile("global_store_dwordx4 %0, %1, off sc0 sc1\n\ts_nop 1" ::"v"(p), "v"(v) : "memory")
#define SBAR() __builtin_amdgcn_sched_barrier(0)
#define SGB(mask, n) __builtin_amdgcn_sched_group_barrier(mask, n, 0)
template <int I> struct IC { static constexpr int value = I; };
template <int I, int N, class F> __device__ __forceinline__ void sfor(F&& f) {
  if constexpr (I < N) { f(IC<I>{}); sfor<I + 1, N>(f); }
}
__device__ __forceinline__ s16x4 tr_read(const char* p) {
  return __builtin_amdgcn_ds_read_tr16_b64_v4i16((LAS s16x4*)p);
}
__device__ __forceinline__ bf16x8 lds_read16(const char* p) { return *(const LAS bf16x8*)p; }

__device__ __forceinline__ void mod_block(const u16* __restrict__ c, const u16* __restrict__ wmod, float* __restrict__ modp,
                                          int cg, int ks, float* sh) {
  const int tid = threadIdx.x;
  const int cl = tid & 7, rg = tid >> 3;
  const int col0 = cg * 64 + cl * 8;
  u32x4 w[8];
#pragma unroll
  for (int i = 0; i < 8; ++i)
    w[i] = __builtin_nontemporal_load((const u32x4*)(wmod + (size_t)(ks * 256 + rg + i * 32) * 6144 + col0));
  {
    const float v = bf2f(c[ks * 256 + tid]);
    sh[tid] = rbf(v / (1.0f + __expf(-v)));
  }
  __syncthreads();
  float acc[8];
#pragma unroll
  for (int j = 0; j < 8; ++j) acc[j] = 0.f;
#pragma unroll
  for (int i = 0; i < 8; ++i) {
    const float s = sh[rg + i * 32];
#pragma unroll
    for (int j = 0; j < 4; ++j) {
      acc[2 * j] += s * lo_f(w[i][j]);
      acc[2 * j + 1] += s * hi_f(w[i][j]);
    }
  }
  float* red = sh + 256;
#pragma unroll
  for (int j = 0; j < 8; ++j) red[rg * 65 + cl * 8 + j] = acc[j];
  __syncthreads();
  if (tid < 64) {
    float s = 0.f;
#pragma unroll 8
    for (int r = 0; r < 32; ++r) s += red[r * 65 + tid];
    modp[ks * 6144 + cg * 64 + tid] = s;
  }
}
__device__ __forceinline__ void touch_block(const char* src) {
  u32x4 v[8];
#pragma unroll
  for (int i = 0; i < 8; ++i) v[i] = *(const u32x4*)(src + i * 4096 + threadIdx.x * 16);
#pragma unroll
  for (int i = 0; i < 8; ++i) asm volatile("" ::"v"(v[i]));
}
__device__ __forceinline__ f32x4 ld_mod4(const float* modp, int idx) {
  f32x4 a = *(const f32x4*)(modp + idx), b = *(const f32x4*)(modp + 6144 + idx);
  f32x4 c = *(const f32x4*)(modp + 2 * 6144 + idx), d = *(const f32x4*)(modp + 3 * 6144 + idx);
  f32x4 s = (a + b) + (c + d);
  return (f32x4){rbf(s[0]), rbf(s[1]), rbf(s[2]), rbf(s[3])};
}

__global__ __launch_bounds__(256) void mod_kernel(const u16* __restrict__ c, const u16* __restrict__ wmod,
                                                  float* __restrict__ modp, const u16* __restrict__ pf0,
                                                  const u16* __restrict__ pf1, unsigned* flags) {
  __shared__ float sh[256 + 32 * 65];
  STAMP_BEGIN();
  const int b = blockIdx.x;
  if (threadIdx.x == 0 && b < 256) flags[b * 32] = 0u;
  if (b < 128) mod_block(c, wmod, modp, b >> 2, b & 3, sh);
  else if (b < 256) touch_block((const char*)pf0 + (size_t)(b - 128) * 32768);
  else touch_block((const char*)pf1 + (size_t)(b - 256) * 32768);
  STAMP_END(0);
}

__global__ __launch_bounds__(256) void ln_kernel(const u16* __restrict__ x, const float* modp, int moff, u16* __restrict__ h,
                                                 const u16* __restrict__ pf, int kid) {
  __shared__ float sh[2048];
  STAMP_BEGIN();
  const int tid = threadIdx.x;
  if (blockIdx.x >= 512) { touch_block((const char*)pf + (size_t)(blockIdx.x - 512) * 32768); return; }
  const int lane = tid & 63, wid = tid >> 6;
  const int row = blockIdx.x * 4 + wid;
  const u16* xr = x + (size_t)row * D_;
  const u32x4 v0 = *(const u32x4*)(xr + lane * 8);
  const u32x4 v1 = *(const u32x4*)(xr + 512 + lane * 8);
  {
    const f32x4 s0 = ld_mod4(modp, moff + tid * 4);
    const f32x4 s1 = ld_mod4(modp, moff + 1024 + tid * 4);
    *(f32x4*)(sh + tid * 4) = s0;
    *(f32x4*)(sh + 1024 + tid * 4) = (f32x4){rbf(1.0f + s1[0]), rbf(1.0f + s1[1]), rbf(1.0f + s1[2]), rbf(1.0f + s1[3])};
  }
  float f[16];
#pragma unroll
  for (int j = 0; j < 4; ++j) {
    f[2 * j] = lo_f(v0[j]); f[2 * j + 1] = hi_f(v0[j]);
    f[8 + 2 * j] = lo_f(v1[j]); f[8 + 2 * j + 1] = hi_f(v1[j]);
  }
  float s = 0.f;
#pragma unroll
  for (int j = 0; j < 16; ++j) s += f[j];
#pragma unroll
  for (int o = 32; o >= 1; o >>= 1) s += __shfl_xor(s, o);
  const float mean = s * (1.0f / D_);
  float q = 0.f;
#pragma unroll
  for (int j = 0; j < 16; ++j) { float d = f[j] - mean; q += d * d; }
#pragma unroll
  for (int o = 32; o >= 1; o >>= 1) q += __shfl_xor(q, o);
  const float mb = rbf(mean);
  const float vb = rbf(q * (1.0f / D_));
  const float sd = rbf(sqrtf(rbf(vb + 1e-5f)));
  const float inv = 1.0f / sd;
  __syncthreads();
  float o[16];
#pragma unroll
  for (int half = 0; half < 2; ++half) {
    const int c0 = half * 512 + lane * 8;
    const f32x4 sa0 = *(const f32x4*)(sh + c0), sa1 = *(const f32x4*)(sh + c0 + 4);
    const f32x4 ca0 = *(const f32x4*)(sh + 1024 + c0), ca1 = *(const f32x4*)(sh + 1024 + c0 + 4);
#pragma unroll
    for (int j = 0; j < 8; ++j) {
      const float shf = j < 4 ? sa0[j & 3] : sa1[j & 3];
      const float sc = j < 4 ? ca0[j & 3] : ca1[j & 3];
      float t = rbf(f[half * 8 + j] - mb);
      t = rbf(t * inv);
      t = rbf(t * sc);
      o[half * 8 + j] = t + shf;
    }
  }
  u16* hr = h + (size_t)row * D_;
  u32x4 w0, w1;
#pragma unroll
  for (int j = 0; j < 4; ++j) {
    w0[j] = pack2(o[2 * j], o[2 * j + 1]);
    w1[j] = pack2(o[8 + 2 * j], o[8 + 2 * j + 1]);
  }
  ST16(hr + lane * 8, w0);
  ST16(hr + 512 + lane * 8, w1);
  STAMP_END(kid);
}

struct GemmP {
  const u16* A; const u16* B0; const u16* B1; const u16* res; const float* gate; u16* out; float* slab; unsigned* flags;
};

#define NS0 3
#define LW0 1
#define LW1 1
#define LW3 1
#define AD1 0
#define AD4 0
template <int MODE> struct GCfg;
template <> struct GCfg<0> { static constexpr int BM = 128, BN = 192, WM = 2, WN = 2, NS = NS0, K = 1024, LDA = 1024, LDB = 3072, NTM = 16, NTN = 16, RM = 8, RN = 4, LW = LW0, AD = 0; };
template <> struct GCfg<1> { static constexpr int BM = 128, BN = 64, WM = AD1 ? 4 : 2, WN = AD1 ? 1 : 2, NS = NS1, K = 1024, LDA = 1024, LDB = 1024, NTM = 16, NTN = 16, RM = 4, RN = 8, LW = LW1, AD = AD1; };
template <> struct GCfg<2> { static constexpr int BM = 256, BN = 256, WM = 4, WN = 2, NS = 2, K = 1024, LDA = 1024, LDB = 3584, NTM = 8, NTN = 28, RM = 4, RN = 7, LW = 0, AD = 0; };
template <> struct GCfg<3> { static constexpr int BM = 128, BN = 128, WM = 2, WN = 2, NS = NS3, K = 1792, LDA = 3584, LDB = 1024, NTM = 16, NTN = 16, RM = 4, RN = 8, LW = LW3, AD = 0; };

template <> struct GCfg<4> { static constexpr int BM = 128, BN = 64, WM = AD4 ? 4 : 2, WN = AD4 ? 1 : 2, NS = NS3, K = 3584, LDA = 3584, LDB = 1024, NTM = 16, NTN = 16, RM = 4, RN = 8, LW = LW3, AD = AD4; };

template <int MODE>
__global__ __launch_bounds__(GCfg<MODE>::WM* GCfg<MODE>::WN * 64 * (GCfg<MODE>::LW ? 2 : 1), 2) void gemm_kernel(GemmP p) {
  using C = GCfg<MODE>;
  constexpr int BM = C::BM, BN = C::BN, WM = C::WM, WN = C::WN, NS = C::NS, NW = WM * WN;
  constexpr int TM = BM / WM, TN = BN / WN, MI = TM / 16, NI = TN / 16;
  constexpr int AD = C::AD;
  constexpr int A_BYTES = AD ? 0 : BM * 128, B_BYTES = BN * 128, ST_BYTES = A_BYTES + B_BYTES;
  constexpr int GA = AD ? 0 : BM / 8 / NW, GB = BN / 8 / NW, G = GA + GB;
  constexpr int NT = C::K / 64;
  static_assert(BM % (8 * NW) == 0 && BN % (8 * NW) == 0, "stage split");
  static_assert(NT >= NS + 1, "K too small");
  extern __shared__ __attribute__((aligned(16))) char smem[];
  STAMP_BEGIN();

  const int tid = threadIdx.x, lane = tid & 63;
  constexpr bool LW = C::LW != 0;
  const int wid_all = __builtin_amdgcn_readfirstlane(tid >> 6);
  const int wid = wid_all % NW;
  const int wm = wid / WN, wn = wid % WN;
  int tm, tn;
  {
    const int x = blockIdx.x & 7, idx = blockIdx.x >> 3;
    constexpr int NRM = C::NTM / C::RM;
    const int rm = x % NRM, rn = x / NRM;
    tm = rm * C::RM + idx % C::RM;
    tn = rn * C::RN + idx / C::RM;
  }
  int kh = 0;
  if constexpr (MODE == 3) { kh = tn & 1; tn >>= 1; }
  const int m0 = tm * BM;

  const int par = wid & 1;
  const int a_ch = (lane & 7) ^ ((4 * par + (lane >> 4)) & 7);
  const unsigned a_voff = (MODE == 1) ? (unsigned)((((a_ch >> 2) * S_ + (lane >> 3)) * 32 + (a_ch & 3) * 8) * 2)
                                      : (unsigned)(((lane >> 3) * C::LDA + 8 * a_ch) * 2);
  const unsigned b_voff = (unsigned)(((lane >> 3) * C::LDB + 8 * ((lane & 7) ^ ((((lane >> 4) & 1) | (par << 1)) << 1))) * 2);
  const char* Abase = (MODE == 1) ? (const char*)(p.A + (size_t)m0 * 32) : (const char*)(p.A + (size_t)m0 * C::LDA + kh * C::K);
  const unsigned lds0 = (unsigned)(size_t)(LAS char*)smem;

  auto issue1 = [&](auto gi, int t, int bufoff) {
    constexpr int g = decltype(gi)::value;
    if constexpr (g < GA) {
      const int i = wid + NW * g;
      const char* gp = (MODE == 1) ? Abase + ((size_t)(8 * i) * 32 + (size_t)t * 2 * S_ * 32) * 2
                                   : Abase + ((size_t)(8 * i) * C::LDA + (size_t)t * 64) * 2;
      glds16(gp, a_voff, lds0 + bufoff + i * 1024);
    } else {
      const int i = wid + NW * (g - GA);
      const int pn = i >> 3, kr = 8 * (i & 7);
      const u16* bp;
      if constexpr (MODE == 2) bp = (pn < 2 ? p.B0 : p.B1) + tn * 128 + (pn & 1) * 64;
      else bp = p.B0 + (size_t)kh * C::K * C::LDB + tn * BN + pn * 64;
      const char* gp = (const char*)(bp + ((size_t)t * 64 + kr) * C::LDB);
      glds16(gp, b_voff, lds0 + bufoff + A_BYTES + i * 1024);
    }
  };

  if constexpr (C::LW == 1) {
    if (wid_all >= NW) {
      sfor<0, NS>([&](auto sc_) { sfor<0, G>([&](auto gc) { issue1(gc, decltype(sc_)::value, decltype(sc_)::value * ST_BYTES); }); });
      WAIT_V((NS - 1) * G);
      BAR();
      int lb = 0;
      for (int t = 0; t < NT - NS; ++t) {
        WAIT_V((NS - 2) * G);
        BAR();
        sfor<0, G>([&](auto gc) { issue1(gc, t + NS, lb); });
        lb = (lb + ST_BYTES == NS * ST_BYTES) ? 0 : lb + ST_BYTES;
      }
      sfor<0, NS - 1>([&](auto rc) { WAIT_V((NS - 2 - decltype(rc)::value) * G); BAR(); });
      return;
    }
  }
  if constexpr (C::LW == 2) {
    if (wid_all >= NW) {
      static_assert(NS == 3 && (NT & 1) == 0, "reg loader");
      u32x4 R0[G], R1[G];
      auto ld = [&](u32x4* R, int t) {
        sfor<0, G>([&](auto gi) {
          constexpr int g = decltype(gi)::value;
          if constexpr (g < GA) {
            const int i = wid + NW * g;
            R[g] = *(const u32x4*)(Abase + ((size_t)(8 * i) * C::LDA + (size_t)t * 64) * 2 + a_voff);
          } else {
            const int i = wid + NW * (g - GA);
            const int pn = i >> 3, kr = 8 * (i & 7);
            const u16* bp = p.B0 + (size_t)kh * C::K * C::LDB + tn * BN + pn * 64;
            R[g] = *(const u32x4*)((const char*)(bp + ((size_t)t * 64 + kr) * C::LDB) + b_voff);
          }
        });
      };
      auto st = [&](const u32x4* R, int bufoff) {
        sfor<0, G>([&](auto gi) {
          constexpr int g = decltype(gi)::value;
          const int off = g < GA ? (wid + NW * g) * 1024 : A_BYTES + (wid + NW * (g - GA)) * 1024;
          *(LAS u32x4*)(smem + bufoff + off + lane * 16) = R[g];
        });
      };
      ld(R0, 0); ld(R1, 1);
      st(R0, 0); ld(R0, 2);
      WAIT_L0(); BAR();
      st(R1, ST_BYTES); ld(R1, 3);
      st(R0, 2 * ST_BYTES); ld(R0, 4);
      int lb = 0;
      for (int t = 0; t < NT - 4; t += 2) {
        WAIT_L0(); BAR();
        st(R1, lb); if (t + 5 < NT) ld(R1, t + 5);
        lb = (lb + ST_BYTES == NS * ST_BYTES) ? 0 : lb + ST_BYTES;
        WAIT_L0(); BAR();
        st(R0, lb); if (t + 6 < NT) ld(R0, t + 6);
        lb = (lb + ST_BYTES == NS * ST_BYTES) ? 0 : lb + ST_BYTES;
      }
      WAIT_L0(); BAR();
      st(R1, lb);
      WAIT_L0(); BAR();
      BAR();
      return;
    }
  }

  const int fr = lane & 15, fq = lane >> 4;
  u32x4 rv[4];
  f32x4 gv[NI];
  if constexpr (MODE == 1 || MODE == 3 || MODE == 4) {
#pragma unroll
    for (int i = 0; i < 4; ++i) {
      if constexpr (MODE != 3) rv[i] = *(const u32x4*)(p.res + (size_t)(m0 + i * 32 + (tid >> 3)) * D_ + tn * BN + (tid & 7) * 8);
      else rv[i] = *(const u32x4*)(p.res + (size_t)(m0 + kh * 64 + i * 16 + (tid >> 4)) * D_ + tn * BN + (tid & 15) * 8);
    }
#pragma unroll
    for (int ni = 0; ni < NI; ++ni) gv[ni] = ld_mod4(p.gate, tn * BN + wn * TN + ni * 16 + 4 * fq);
  }
  const int a_rd0 = (wm * TM + fr) * 128 + ((fq ^ (fr >> 1)) << 4);
  const int a_rd1 = a_rd0 ^ 64;
  int b_rd[(MODE == 2) ? 4 : NI];
  {
    const int q = (lane & 15) >> 2, pp = lane & 3;
    const int gl = ((q >> 1) & 1) | ((fq & 1) << 1);
#pragma unroll
    for (int ni = 0; ni < ((MODE == 2) ? 4 : NI); ++ni) {
      int pn, un;
      if constexpr (MODE == 2) { pn = wn; un = ni & 3; }
      else { const int j = wn * NI + ni; pn = j >> 2; un = j & 3; }
      b_rd[ni] = A_BYTES + pn * 8192 + (8 * fq + q) * 128 + ((un ^ gl) << 5) + pp * 8;
    }
  }

  f32x4 acc[MI][NI];
#pragma unroll
  for (int mi = 0; mi < MI; ++mi)
#pragma unroll
    for (int ni = 0; ni < NI; ++ni) acc[mi][ni] = (f32x4){0.f, 0.f, 0.f, 0.f};

  constexpr int NSP = (MODE == 2) ? 2 : 1;
  constexpr int NH = NI / NSP, NM = MI * NH, NPH = 2 * NSP;
  constexpr int GW = (NS >= 3) ? NPH : NPH / 2;
  constexpr int GPP = (G + GW - 1) / GW;
  bf16x8 a0[MI], a1[MI], b0[NH], b1[NH];
  s16x4 blo[NH];

  auto rd_b = [&](auto ri, bf16x8* BF, const char* sb, int ks, int hf) {
    constexpr int r = decltype(ri)::value, ni = r >> 1;
    const int bo = (MODE == 2) ? b_rd[ni] + hf * 16384 : b_rd[ni];
    if constexpr ((r & 1) == 0) blo[ni] = tr_read(sb + bo + ks * 4096);
    else BF[ni] = __builtin_shufflevector(blo[ni], tr_read(sb + bo + ks * 4096 + 512), 0, 1, 2, 3, 4, 5, 6, 7);
  };
  auto rd_a = [&](auto ri, bf16x8* AF, const char* sb, int ks) {
    constexpr int mi = decltype(ri)::value;
    AF[mi] = lds_read16(sb + (ks ? a_rd1 : a_rd0) + mi * 2048);
  };
  auto phase = [&](auto hfc, auto rdm, auto g0c, auto g1c, bf16x8* AF, bf16x8* BF, bf16x8* AN_, bf16x8* BN_,
                   const char* rsb, int rks, int rhf, int gt, int gbuf) {
    constexpr int hf = decltype(hfc)::value, RDM = decltype(rdm)::value;
    constexpr int g0 = decltype(g0c)::value, g1 = decltype(g1c)::value, NG = g1 - g0;
    constexpr int NR = RDM == 0 ? 0 : (RDM == 1 ? 2 * NH : 2 * NH + MI);
    sfor<0, NM>([&](auto ic) {
      constexpr int i = decltype(ic)::value, mi = i / NH, ni = i % NH;
      MFMA16(acc[mi][hf * NH + ni], BF[ni], AF[mi]);
      sfor<0, NR>([&](auto rc) {
        constexpr int r = decltype(rc)::value;
        if constexpr ((r * (NM - NM / 4)) / (NR > 0 ? NR : 1) == i) {
          if constexpr (r < 2 * NH) rd_b(rc, BN_, rsb, rks, rhf);
          else rd_a(IC<r - 2 * NH>{}, AN_, rsb, rks);
        }
      });
      sfor<0, NG>([&](auto gc) {
        constexpr int g = decltype(gc)::value;
        if constexpr ((g * NM) / (NG > 0 ? NG : 1) == i) issue1(IC<g0 + g>{}, gt, gbuf);
      });
      SBAR();
    });
  };
  constexpr auto gmin = [](int a, int b) { return a < b ? a : b; };
  int cur = 0, prv = 0;
  auto ktile = [&](auto vmc, auto restc, auto firstc, int t) {
    constexpr int VM = decltype(vmc)::value;
    constexpr bool REST = decltype(restc)::value && !LW, FIRST = decltype(firstc)::value && !LW;
    const int nxt = (cur + ST_BYTES == NS * ST_BYTES) ? 0 : cur + ST_BYTES;
    const char* sc = smem + cur;
    const char* sn = smem + nxt;
    constexpr int R1 = REST ? gmin(G, GPP) : 0, R2 = REST ? gmin(G, 2 * GPP) : 0, R3 = REST ? gmin(G, 3 * GPP) : 0, R4 = REST ? gmin(G, 4 * GPP) : 0;
    constexpr int F1 = FIRST ? gmin(G, GPP) : 0;
    if constexpr (NSP == 1) {
      phase(IC<0>{}, IC<2>{}, IC<R1>{}, IC<R2>{}, a0, b0, a1, b1, sc, 1, 0, t - 1 + NS, prv);
      if constexpr (VM >= 0) {
        if constexpr (!LW) WAIT_V(VM);
        WAIT_L0(); BAR();
        phase(IC<0>{}, IC<2>{}, IC<0>{}, IC<F1>{}, a1, b1, a0, b0, sn, 0, 0, t + NS, cur);
      } else {
        phase(IC<0>{}, IC<0>{}, IC<0>{}, IC<0>{}, a1, b1, a0, b0, sn, 0, 0, 0, 0);
      }
    } else {
      phase(IC<0>{}, IC<1>{}, IC<R1>{}, IC<R2>{}, a0, b0, a1, b1, sc, 0, 1, t - 1 + NS, prv);
      phase(IC<1>{}, IC<2>{}, IC<R2>{}, IC<R3>{}, a0, b1, a1, b0, sc, 1, 0, t - 1 + NS, prv);
      phase(IC<0>{}, IC<1>{}, IC<R3>{}, IC<R4>{}, a1, b0, a1, b1, sc, 1, 1, t - 1 + NS, prv);
      if constexpr (VM >= 0) {
        WAIT_V(VM); WAIT_L0(); BAR();
        phase(IC<1>{}, IC<2>{}, IC<0>{}, IC<F1>{}, a1, b1, a0, b0, sn, 0, 0, t + NS, cur);
      } else {
        phase(IC<1>{}, IC<0>{}, IC<0>{}, IC<0>{}, a1, b1, a0, b0, sn, 0, 0, 0, 0);
      }
    }
    prv = cur; cur = nxt;
  };

  if constexpr (AD > 0) {
    static_assert(LW && NSP == 1 && NT % AD == 0, "A-direct path");
    bf16x8 ar[AD][2][MI];
    const char* Aw = (MODE == 1) ? Abase + ((size_t)(wm * TM + fr) * 32 + fq * 8) * 2
                                 : Abase + ((size_t)(wm * TM + fr) * C::LDA + fq * 8) * 2;
    auto lda = [&](auto dc, int t) {
      constexpr int d = decltype(dc)::value;
#pragma unroll
      for (int ks = 0; ks < 2; ++ks)
#pragma unroll
        for (int mi = 0; mi < MI; ++mi) {
          if constexpr (MODE == 1) ar[d][ks][mi] = *(const bf16x8*)(Aw + ((size_t)(2 * t + ks) * S_ + mi * 16) * 64);
          else ar[d][ks][mi] = *(const bf16x8*)(Aw + (size_t)mi * 16 * C::LDA * 2 + t * 128 + ks * 64);
        }
    };
    sfor<0, AD>([&](auto dc) { lda(dc, decltype(dc)::value); });
    BAR();
    STAMP_MID(MODE == 0 ? 2 : MODE == 1 ? 4 : MODE == 2 ? 6 : 7, 0);
    sfor<0, 2 * NH>([&](auto rc) { rd_b(rc, b0, smem, 0, 0); });
    auto ktile_ad = [&](auto dc, auto ldc, auto lastc, int t) {
      constexpr int d = decltype(dc)::value;
      const int nxt = (cur + ST_BYTES == NS * ST_BYTES) ? 0 : cur + ST_BYTES;
      phase(IC<0>{}, IC<1>{}, IC<0>{}, IC<0>{}, ar[d][0], b0, a1, b1, smem + cur, 1, 0, 0, 0);
      if constexpr (!decltype(lastc)::value) {
        WAIT_L0(); BAR();
        phase(IC<0>{}, IC<1>{}, IC<0>{}, IC<0>{}, ar[d][1], b1, a0, b0, smem + nxt, 0, 0, 0, 0);
      } else {
        phase(IC<0>{}, IC<0>{}, IC<0>{}, IC<0>{}, ar[d][1], b1, a0, b0, smem + nxt, 0, 0, 0, 0);
      }
      if constexpr (decltype(ldc)::value) { lda(dc, t + AD); SBAR(); }
      cur = nxt;
    };
    for (int t = 0; t < NT - AD; t += AD) sfor<0, AD>([&](auto dc) { ktile_ad(dc, IC<1>{}, IC<0>{}, t + decltype(dc)::value); });
    sfor<0, AD>([&](auto dc) { ktile_ad(dc, IC<0>{}, IC<(decltype(dc)::value == AD - 1)>{}, 0); });
  } else {
  if constexpr (!LW) {
    sfor<0, NS>([&](auto sc_) { sfor<0, G>([&](auto gc) { issue1(gc, decltype(sc_)::value, decltype(sc_)::value * ST_BYTES); }); });
    WAIT_V((NS - 1) * G);
  }
  BAR();
  STAMP_MID(MODE == 0 ? 2 : MODE == 1 ? 4 : MODE == 2 ? 6 : 7, 0);
  sfor<0, 2 * NH>([&](auto rc) { rd_b(rc, b0, smem, 0, 0); });
  sfor<0, MI>([&](auto rc) { rd_a(rc, a0, smem, 0); });
  using TT = IC<1>; using FF = IC<0>;
  ktile(IC<(NS - 2) * G>{}, FF{}, TT{}, 0);
  for (int t = 1; t < NT - NS; ++t) ktile(IC<(NS - 2) * G>{}, TT{}, TT{}, t);
  ktile(IC<(NS - 2) * G>{}, TT{}, FF{}, NT - NS);
  sfor<0, NS - 2>([&](auto rc) { ktile(IC<(NS - 3 - decltype(rc)::value) * G>{}, FF{}, FF{}, 0); });
  ktile(IC<-1>{}, FF{}, FF{}, 0);
  }

  STAMP_MID(MODE == 0 ? 2 : MODE == 1 ? 4 : MODE == 2 ? 6 : 7, 1);
  if constexpr (MODE == 0) {
    const float qs = 0.17677669529663687f * 1.4426950408889634f;
    BAR();
    char* st = smem + wid * (64 * 208);
#pragma unroll
    for (int ni = 0; ni < NI; ++ni) {
      const int n = tn * BN + wn * TN + ni * 16;
      const float sc = n < 1024 ? qs : 1.0f;
#pragma unroll
      for (int mi = 0; mi < MI; ++mi) {
        u32x2 w;
        w[0] = pack2(acc[mi][ni][0] * sc, acc[mi][ni][1] * sc);
        w[1] = pack2(acc[mi][ni][2] * sc, acc[mi][ni][3] * sc);
        *(LAS u32x2*)(st + (mi * 16 + fr) * 208 + ni * 32 + fq * 8) = w;
      }
    }
#pragma unroll
    for (int j = 0; j < 3; ++j) {
      const int hh = (tn * BN + wn * TN + j * 32) >> 5;
      u16* ob = p.out + ((size_t)hh * S_ + m0 + wm * TM) * 32 + lane * 8;
#pragma unroll
      for (int i = 0; i < 4; ++i) {
        const u32x4 v = *(const LAS u32x4*)(st + (i * 16 + (lane >> 2)) * 208 + j * 64 + (lane & 3) * 16);
        ST16(ob + i * 512, v);
      }
    }
  } else if constexpr (MODE == 3) {
    const int tile = tm * 8 + tn;
    if (wm != kh) {
      float* dst = p.slab + ((size_t)(tile * 2 + kh) * 2 + wn) * 4096 + lane * 4;
#pragma unroll
      for (int mi = 0; mi < MI; ++mi)
#pragma unroll
        for (int ni = 0; ni < NI; ++ni) ST16(dst + (mi * NI + ni) * 256, __builtin_bit_cast(u32x4, acc[mi][ni]));
      WAIT_V(0);
      if (lane == 0) __hip_atomic_fetch_add(p.flags + (tile * 2 + kh) * 32, 1u, __ATOMIC_RELAXED, __HIP_MEMORY_SCOPE_AGENT);
      BAR();
    } else {
      const unsigned* fl = p.flags + (tile * 2 + (kh ^ 1)) * 32;
      for (int spin = 0; spin < (1 << 22); ++spin) {
        if (__builtin_amdgcn_readfirstlane(__hip_atomic_load(fl, __ATOMIC_RELAXED, __HIP_MEMORY_SCOPE_AGENT)) >= 2u) break;
        __builtin_amdgcn_s_sleep(1);
      }
      const float* src = p.slab + ((size_t)(tile * 2 + (kh ^ 1)) * 2 + wn) * 4096 + lane * 4;
      f32x4 pv[MI][NI];
#pragma unroll
      for (int mi = 0; mi < MI; ++mi)
#pragma unroll
        for (int ni = 0; ni < NI; ++ni)
          asm volatile("global_load_dwordx4 %0, %1, off sc0 sc1" : "=v"(pv[mi][ni]) : "v"(src + (mi * NI + ni) * 256) : "memory");
      BAR();
      asm volatile("s_waitcnt vmcnt(0)" ::: "memory");
      SBAR();
#pragma unroll
      for (int ni = 0; ni < NI; ++ni)
#pragma unroll
        for (int mi = 0; mi < MI; ++mi) {
          asm volatile("" : "+v"(pv[mi][ni]));
          const f32x4 a = acc[mi][ni] + pv[mi][ni];
          u32x2 w;
          w[0] = pack2(gv[ni][0] * rbf(a[0]), gv[ni][1] * rbf(a[1]));
          w[1] = pack2(gv[ni][2] * rbf(a[2]), gv[ni][3] * rbf(a[3]));
          *(LAS u32x2*)(smem + (mi * 16 + fr) * 272 + wn * 128 + ni * 32 + fq * 8) = w;
        }
    }
    WAIT_L0();
    BAR();
#pragma unroll
    for (int i = 0; i < 4; ++i) {
      const u32x4 v = *(const LAS u32x4*)(smem + (i * 16 + (tid >> 4)) * 272 + (tid & 15) * 16);
      u32x4 w;
#pragma unroll
      for (int j = 0; j < 4; ++j) w[j] = pack2(lo_f(rv[i][j]) + lo_f(v[j]), hi_f(rv[i][j]) + hi_f(v[j]));
      ST16(p.out + (size_t)(m0 + kh * 64 + i * 16 + (tid >> 4)) * D_ + tn * BN + (tid & 15) * 8, w);
    }
  } else if constexpr (MODE == 1 || MODE == 4) {
    BAR();
#pragma unroll
    for (int ni = 0; ni < NI; ++ni) {
#pragma unroll
      for (int mi = 0; mi < MI; ++mi) {
        u32x2 w;
        w[0] = pack2(gv[ni][0] * rbf(acc[mi][ni][0]), gv[ni][1] * rbf(acc[mi][ni][1]));
        w[1] = pack2(gv[ni][2] * rbf(acc[mi][ni][2]), gv[ni][3] * rbf(acc[mi][ni][3]));
        *(LAS u32x2*)(smem + (wm * TM + mi * 16 + fr) * 144 + wn * 64 + ni * 32 + fq * 8) = w;
      }
    }
    WAIT_L0();
    BAR();
#pragma unroll
    for (int i = 0; i < 4; ++i) {
      const u32x4 v = *(const LAS u32x4*)(smem + (i * 32 + (tid >> 3)) * 144 + (tid & 7) * 16);
      u32x4 w;
#pragma unroll
      for (int j = 0; j < 4; ++j) w[j] = pack2(lo_f(rv[i][j]) + lo_f(v[j]), hi_f(rv[i][j]) + hi_f(v[j]));
      ST16(p.out + (size_t)(m0 + i * 32 + (tid >> 3)) * D_ + tn * BN + (tid & 7) * 8, w);
    }
  } else {
    BAR();
    char* st = smem + wid * (64 * 144);
#pragma unroll
    for (int ni = 0; ni < 4; ++ni)
#pragma unroll
      for (int mi = 0; mi < MI; ++mi) {
        float o[4];
#pragma unroll
        for (int r = 0; r < 4; ++r) o[r] = rbf(acc[mi][ni][r]) * rbf(acc[mi][ni + 4][r]);
        u32x2 w;
        w[0] = pack2(o[0], o[1]);
        w[1] = pack2(o[2], o[3]);
        *(LAS u32x2*)(st + (mi * 16 + fr) * 144 + ni * 32 + fq * 8) = w;
      }
    u16* ob = p.out + (size_t)(m0 + wm * TM + (lane >> 3)) * F_ + tn * 128 + wn * 64 + (lane & 7) * 8;
#pragma unroll
    for (int i = 0; i < 8; ++i) {
      const u32x4 v = *(const LAS u32x4*)(st + (i * 8 + (lane >> 3)) * 144 + (lane & 7) * 16);
      ST16(ob + (size_t)i * 8 * F_, v);
    }
  }
  STAMP_END(MODE == 0 ? 2 : MODE == 1 ? 4 : MODE == 2 ? 6 : 7);
}

#define AT_NSTAGE 6
constexpr int AT_KT = 128, AT_NS = AT_NSTAGE, AT_ST = 16384;
#define MFMA32(a, b, c) __builtin_amdgcn_mfma_f32_32x32x16_bf16(a, b, c, 0, 0, 0)
#define AEXP(x) __builtin_amdgcn_exp2f(x)
#define MX3(a, b, c) __builtin_fmaxf(__builtin_fmaxf((a), (b)), (c))

__device__ __forceinline__ float tile_rowmax(const f32x16& s0, const f32x16& s1) {
  float a = MX3(s0[0], s0[1], s1[0]), b = MX3(s0[2], s0[3], s1[1]);
  a = MX3(a, s1[2], s1[3]);
#pragma unroll
  for (int r = 4; r < 16; r += 4) {
    a = MX3(a, s0[r], s0[r + 1]); b = MX3(b, s0[r + 2], s0[r + 3]);
    a = MX3(a, s1[r], s1[r + 1]); b = MX3(b, s1[r + 2], s1[r + 3]);
  }
  float mx = __builtin_fmaxf(a, b);
  auto rr = __builtin_amdgcn_permlane32_swap(__float_as_uint(mx), __float_as_uint(mx), false, false);
  return __builtin_fmaxf(__uint_as_float(rr[0]), __uint_as_float(rr[1]));
}

__global__ __launch_bounds__(512, 2) void attn_kernel(const u16* __restrict__ qkv, u16* __restrict__ o) {
  extern __shared__ __attribute__((aligned(16))) char smem[];
  STAMP_BEGIN();
  const int tid = threadIdx.x, lane = tid & 63;
  const int wid = __builtin_amdgcn_readfirstlane(tid >> 6);
  const int x = blockIdx.x & 7, idx = blockIdx.x >> 3;
  const int hd = x * 4 + (idx >> 3), qb = idx & 7;
  const int r32 = lane & 31, hi = lane >> 5;
  const u16* Q = qkv + ((size_t)(0 * 32 + hd) * S_) * 32;
  const u16* Kp = qkv + ((size_t)(1 * 32 + hd) * S_) * 32;
  const u16* Vp = qkv + ((size_t)(2 * 32 + hd) * S_) * 32;
  const int q0 = qb * 256 + wid * 32;

  const unsigned k_voff = (unsigned)(((lane >> 2) * 32 + 8 * ((lane & 3) ^ ((lane >> 4) & 3))) * 2);
  const unsigned v_voff = (unsigned)(lane * 16);
  const unsigned lds0 = (unsigned)(size_t)(LAS char*)smem;
  auto issue = [&](int t, int bufoff) {
    const char* gk = (const char*)(Kp + ((size_t)t * AT_KT + wid * 16) * 32);
    const char* gv = (const char*)(Vp + ((size_t)t * AT_KT + wid * 16) * 32);
    glds16(gk, k_voff, lds0 + bufoff + wid * 1024);
    glds16(gv, v_voff, lds0 + bufoff + 8192 + wid * 1024);
  };
#pragma unroll
  for (int s = 0; s < AT_NS - 1; ++s) issue(s, s * AT_ST);
  const bf16x8 qf0 = *(const bf16x8*)(Q + (size_t)(q0 + r32) * 32 + hi * 8);
  const bf16x8 qf1 = *(const bf16x8*)(Q + (size_t)(q0 + r32) * 32 + 16 + hi * 8);

  const int k_rd0 = r32 * 64 + ((hi ^ ((r32 >> 2) & 3)) << 4);
  const int k_rd1 = k_rd0 ^ 32;
  const int v_rd = 8192 + (4 * hi + ((lane & 15) >> 2)) * 64 + ((lane >> 4) & 1) * 32 + (lane & 3) * 8;

  f32x16 oacc = {};
  f32x16 negm;
  float m, l = 0.f;
  f32x16 sa0, sa1, sb0, sb1;
  u32x4 pp0 = {}, pp1 = {}, pp2 = {}, pp3 = {};
  bf16x8 vf0, vf1, vf2, vf3;
#define AT_BIG 1.0e30f
  constexpr float BIG = AT_BIG;
  constexpr int NT = S_ / AT_KT;

#define SCORES(D0, D1, kb, CIN)                                                         \
  do {                                                                                  \
    bf16x8 k00 = lds_read16((kb) + k_rd0), k01 = lds_read16((kb) + k_rd1);              \
    bf16x8 k10 = lds_read16((kb) + 2048 + k_rd0), k11 = lds_read16((kb) + 2048 + k_rd1);\
    D0 = MFMA32(k00, qf0, CIN); D1 = MFMA32(k10, qf0, CIN);                             \
    D0 = MFMA32(k01, qf1, D0);  D1 = MFMA32(k11, qf1, D1);                              \
  } while (0)
#define VFRAG(vb, s) __builtin_shufflevector(tr_read((vb) + (s) * 1024), tr_read((vb) + (s) * 1024 + 512), 0, 1, 2, 3, 4, 5, 6, 7)
#define READV(vb) do { vf0 = VFRAG(vb, 0); vf1 = VFRAG(vb, 1); vf2 = VFRAG(vb, 2); vf3 = VFRAG(vb, 3); } while (0)
#define PV()                                                                            \
  do {                                                                                  \
    oacc = MFMA32(vf0, __builtin_bit_cast(bf16x8, pp0), oacc);                          \
    oacc = MFMA32(vf1, __builtin_bit_cast(bf16x8, pp1), oacc);                          \
    oacc = MFMA32(vf2, __builtin_bit_cast(bf16x8, pp2), oacc);                          \
    oacc = MFMA32(vf3, __builtin_bit_cast(bf16x8, pp3), oacc);                          \
  } while (0)
#define EXPSUM(C0, C1, LS)                                                              \
  do {                                                                                  \
    f32x2 ls2_ = {0.f, 0.f}, ls3_ = {0.f, 0.f};                                         \
    _Pragma("unroll") for (int r = 0; r < 16; r += 2) {                                 \
      C0[r] = AEXP(C0[r]); C0[r + 1] = AEXP(C0[r + 1]);                                 \
      C1[r] = AEXP(C1[r]); C1[r + 1] = AEXP(C1[r + 1]);                                 \
      ls2_ += (f32x2){C0[r], C0[r + 1]};                                                \
      ls3_ += (f32x2){C1[r], C1[r + 1]};                                                \
    }                                                                                   \
    ls2_ += ls3_;                                                                       \
    LS = ls2_[0] + ls2_[1];                                                             \
  } while (0)
#define PACK(C0, C1)                                                                    \
  do {                                                                                  \
    _Pragma("unroll") for (int j = 0; j < 4; ++j) {                                     \
      pp0[j] = pack2(C0[2 * j], C0[2 * j + 1]);                                         \
      pp1[j] = pack2(C0[8 + 2 * j], C0[8 + 2 * j + 1]);                                 \
      pp2[j] = pack2(C1[2 * j], C1[2 * j + 1]);                                         \
      pp3[j] = pack2(C1[8 + 2 * j], C1[8 + 2 * j + 1]);                                 \
    }                                                                                   \
  } while (0)
#define STEP(C0, C1, N0, N1, kcur, knext, vprev, HASN)                                  \
  do {                                                                                  \
    READV(vprev);                                                                       \
    if (HASN) SCORES(N0, N1, knext, negm);                                              \
    PV();                                                                               \
    float ls;                                                                           \
    EXPSUM(C0, C1, ls);                                                                 \
    if (__builtin_expect(__any(!(ls <= BIG)), 0)) {                                     \
                  \
      f32x16 z_ = {};                                                                   \
      SCORES(C0, C1, kcur, z_);                                                         \
      const float mn = __builtin_fmaxf(m, tile_rowmax(C0, C1));                         \
      const float dm = m - mn;                                                          \
      const float f = __builtin_amdgcn_exp2f(dm);                                       \
      m = mn; l *= f;                                                                   \
      _Pragma("unroll") for (int r = 0; r < 16; ++r) {                                  \
        oacc[r] *= f; negm[r] = -mn; C0[r] -= mn; C1[r] -= mn;                          \
        if (HASN) { N0[r] += dm; N1[r] += dm; }                                         \
      }                                                                                 \
      EXPSUM(C0, C1, ls);                                                               \
    }                                                                                   \
    l += ls;                                                                            \
    PACK(C0, C1);                                                                       \
  } while (0)

  WAIT_V(2 * (AT_NS - 2));
  BAR();
  {
    f32x16 z = {};
    SCORES(sa0, sa1, smem, z);
    m = tile_rowmax(sa0, sa1);
#pragma unroll
    for (int r = 0; r < 16; ++r) { negm[r] = -m; sa0[r] -= m; sa1[r] -= m; }
  }
  int cur = 0, prv = 0;
  for (int t = 0; t < NT - 1; ++t) {
    const char* sb = smem + cur;
    int nx = cur + AT_ST; if (nx >= AT_NS * AT_ST) nx = 0;
    int prv2 = cur - AT_ST; if (prv2 < 0) prv2 = (AT_NS - 1) * AT_ST;
    STEP(sa0, sa1, sb0, sb1, sb, sb + 4096, smem + prv + 4096 + v_rd, true);
    if (t + AT_NS - 1 < NT) {
      WAIT_V(2 * (AT_NS - 3));
      WAIT_L0();
      BAR();
      issue(t + AT_NS - 1, prv2);
    } else {
      WAIT_V(0);
      WAIT_L0();
      BAR();
    }
    STEP(sb0, sb1, sa0, sa1, sb + 4096, smem + nx, sb + v_rd, true);
    prv = cur; cur = nx;
  }
  {
    const char* sb = smem + cur;
    STEP(sa0, sa1, sb0, sb1, sb, sb + 4096, smem + prv + 4096 + v_rd, true);
    STEP(sb0, sb1, sa0, sa1, sb + 4096, sb, sb + v_rd, false);
    prv = cur;
  }
  READV(smem + prv + 4096 + v_rd);
  PV();
#undef SCORES
#undef VFRAG
#undef READV
#undef PV
#undef EXPSUM
#undef PACK
#undef STEP
  {
    auto rr = __builtin_amdgcn_permlane32_swap(__float_as_uint(l), __float_as_uint(l), false, false);
    l = __uint_as_float(rr[0]) + __uint_as_float(rr[1]);
  }
  const float inv = 1.0f / l;
  u16* op = o + (size_t)(q0 + r32) * D_ + hd * 32 + 4 * hi;
#pragma unroll
  for (int g = 0; g < 4; ++g) {
    u32x2 w;
    w[0] = pack2(oacc[4 * g] * inv, oacc[4 * g + 1] * inv);
    w[1] = pack2(oacc[4 * g + 2] * inv, oacc[4 * g + 3] * inv);
    *(u32x2*)(op + 8 * g) = w;
  }
  STAMP_END(3);
}

constexpr int A2_KT = 256, A2_NS = 3, A2_ST = 32768, A2_MOD = A2_NS * A2_ST + 1024, A2_RED = A2_MOD + 32768, A2_FLG = A2_RED + 4096, A2_LDS = A2_FLG + 64;
template <bool SAFE>
__device__ __forceinline__ void attn_pass(char* smem, const u16* Kp, const u16* Vp, bf16x8 qf0, bf16x8 qf1, int wid, int lane,
                                          f32x16& oacc, float& m, float& l,
                                          const u16* pw0, const u16* pw1, const u16* pw2, const u16* pw3, const u16* wm, unsigned cc) {
  const int wk = wid >> 3, r32 = lane & 31, hi = lane >> 5;
  const unsigned k_voff = (unsigned)(((lane >> 2) * 32 + 8 * ((lane & 3) ^ ((lane >> 4) & 3))) * 2);
  const unsigned v_voff = (unsigned)(lane * 16);
  const unsigned lds0 = (unsigned)(size_t)(LAS char*)smem;
  auto issue = [&](int t, int bufoff) {
    const char* gk = (const char*)(Kp + ((size_t)t * A2_KT + wid * 16) * 32);
    const char* gv = (const char*)(Vp + ((size_t)t * A2_KT + wid * 16) * 32);
    glds16(gk, k_voff, lds0 + bufoff + wid * 1024);
    glds16(gv, v_voff, lds0 + bufoff + 16384 + wid * 1024);
  };
  issue(0, 0);
  issue(1, A2_ST);
  const int k_rd0 = wk * 8192 + r32 * 64 + ((hi ^ ((r32 >> 2) & 3)) << 4);
  const int k_rd1 = k_rd0 ^ 32;
  const int v_rd = 16384 + wk * 8192 + (4 * hi + ((lane & 15) >> 2)) * 64 + ((lane >> 4) & 1) * 32 + (lane & 3) * 8;
  f32x16 negm = {};
  f32x2 lsa = {0.f, 0.f}, lsb = {0.f, 0.f};
  oacc = (f32x16){};
  m = 0.f; l = 0.f;
  constexpr int NT = S_ / A2_KT;
  int cur = 0;
  for (int t = 0; t < NT; ++t) {
    if (t == 0) WAIT_V(2); else if (!SAFE && t == 4) WAIT_V(5); else if (t + 1 < NT) WAIT_V(3); else WAIT_V(0);
    BAR();
    if (!SAFE && t == 6) {
      const int rg = wid * 8 + (lane >> 3), cl = lane & 7;
      const float c0 = lo_f(cc), c1 = hi_f(cc);
      const float s0 = rbf(c0 / (1.0f + __expf(-c0))), s1 = rbf(c1 / (1.0f + __expf(-c1)));
      const u32x4 w0 = *(const LAS u32x4*)(smem + A2_MOD + (2 * rg) * 128 + cl * 16);
      const u32x4 w1 = *(const LAS u32x4*)(smem + A2_MOD + (2 * rg + 1) * 128 + cl * 16);
      float a[8];
#pragma unroll
      for (int j = 0; j < 4; ++j) {
        a[2 * j] = s0 * lo_f(w0[j]) + s1 * lo_f(w1[j]);
        a[2 * j + 1] = s0 * hi_f(w0[j]) + s1 * hi_f(w1[j]);
      }
#pragma unroll
      for (int j = 0; j < 8; ++j) {
        a[j] += __shfl_xor(a[j], 8); a[j] += __shfl_xor(a[j], 16); a[j] += __shfl_xor(a[j], 32);
      }
      if (lane < 8) {
        float* mred = (float*)(smem + A2_RED);
#pragma unroll
        for (int j = 0; j < 8; ++j) mred[wid * 64 + lane * 8 + j] = a[j];
      }
    }
    if (!SAFE && t == 3) {
      const unsigned m_voff = (unsigned)(((lane >> 3) * 6144 + (lane & 7) * 8) * 2);
      glds16(wm + (size_t)(wid * 16) * 6144, m_voff, lds0 + A2_MOD + wid * 2048);
      glds16(wm + (size_t)(wid * 16 + 8) * 6144, m_voff, lds0 + A2_MOD + wid * 2048 + 1024);
    }
    {
      int ck = (blockIdx.x * NT + t) * 16 + wid;
      if (ck >= 2048 + 3 * 7168) ck -= 16384;
      const u16* pb = pw0;
      if (ck >= 2048) { pb = pw1; ck -= 2048; if (ck >= 7168) { pb = pw2; ck -= 7168; if (ck >= 7168) { pb = pw3; ck -= 7168; } } }
      glds16((const char*)pb + (size_t)ck * 1024, v_voff, lds0 + A2_NS * A2_ST);
      int nb = cur + 2 * A2_ST; if (nb >= A2_NS * A2_ST) nb -= A2_NS * A2_ST;
      if (t + 2 < NT) issue(t + 2, nb);
    }
#pragma unroll
    for (int hb = 0; hb < 2; ++hb) {
      const char* kb = smem + cur + hb * 4096;
      const bf16x8 k00 = lds_read16(kb + k_rd0), k01 = lds_read16(kb + k_rd1);
      const bf16x8 k10 = lds_read16(kb + 2048 + k_rd0), k11 = lds_read16(kb + 2048 + k_rd1);
      f32x16 s0, s1;
      if (SAFE || (t == 0 && hb == 0)) {
        f32x16 z_ = {};
        s0 = MFMA32(k00, qf0, z_); s1 = MFMA32(k10, qf0, z_);
        s0 = MFMA32(k01, qf1, s0); s1 = MFMA32(k11, qf1, s1);
        const float tm = tile_rowmax(s0, s1);
        const bool first = (t == 0 && hb == 0);
        const float mn = first ? tm : __builtin_fmaxf(m, tm);
        if (SAFE) {
          const float f = first ? 0.f : __builtin_amdgcn_exp2f(m - mn);
          lsa *= f; lsb *= f;
          oacc *= f;
        }
        m = mn;
#pragma unroll
        for (int r = 0; r < 16; ++r) { negm[r] = -mn; s0[r] -= mn; s1[r] -= mn; }
      } else {
        s0 = MFMA32(k00, qf0, negm); s1 = MFMA32(k10, qf0, negm);
        s0 = MFMA32(k01, qf1, s0); s1 = MFMA32(k11, qf1, s1);
      }
#pragma unroll
      for (int r = 0; r < 16; r += 2) {
        s0[r] = AEXP(s0[r]); s0[r + 1] = AEXP(s0[r + 1]);
        s1[r] = AEXP(s1[r]); s1[r + 1] = AEXP(s1[r + 1]);
        lsa += (f32x2){s0[r], s0[r + 1]};
        lsb += (f32x2){s1[r], s1[r + 1]};
      }
      u32x4 p0, p1, p2, p3;
#pragma unroll
      for (int j = 0; j < 4; ++j) {
        p0[j] = pack2(s0[2 * j], s0[2 * j + 1]);
        p1[j] = pack2(s0[8 + 2 * j], s0[8 + 2 * j + 1]);
        p2[j] = pack2(s1[2 * j], s1[2 * j + 1]);
        p3[j] = pack2(s1[8 + 2 * j], s1[8 + 2 * j + 1]);
      }
      const char* vb = smem + cur + hb * 4096 + v_rd;
#define VFRAG(s) __builtin_shufflevector(tr_read(vb + (s) * 1024), tr_read(vb + (s) * 1024 + 512), 0, 1, 2, 3, 4, 5, 6, 7)
      oacc = MFMA32(VFRAG(0), __builtin_bit_cast(bf16x8, p0), oacc);
      oacc = MFMA32(VFRAG(1), __builtin_bit_cast(bf16x8, p1), oacc);
      oacc = MFMA32(VFRAG(2), __builtin_bit_cast(bf16x8, p2), oacc);
      oacc = MFMA32(VFRAG(3), __builtin_bit_cast(bf16x8, p3), oacc);
#undef VFRAG
    }
    WAIT_L0();
    cur += A2_ST; if (cur >= A2_NS * A2_ST) cur = 0;
  }
  lsa += lsb;
  l = lsa[0] + lsa[1];
  {
    auto rr = __builtin_amdgcn_permlane32_swap(__float_as_uint(l), __float_as_uint(l), false, false);
    l = __uint_as_float(rr[0]) + __uint_as_float(rr[1]);
  }
  WAIT_V(0);
}

__global__ __launch_bounds__(1024, 4) void attn2_kernel(const u16* __restrict__ qkv, u16* __restrict__ o,
                                                        const u16* pw0, const u16* pw1, const u16* pw2, const u16* pw3,
                                                        const u16* __restrict__ cvec, const u16* wmod, float* __restrict__ modp) {
  extern __shared__ __attribute__((aligned(16))) char smem[];
  STAMP_BEGIN();
  const int tid = threadIdx.x, lane = tid & 63;
  const int wid = __builtin_amdgcn_readfirstlane(tid >> 6);
  const int wq = wid & 7, wk = wid >> 3;
  const int x = blockIdx.x & 7, idx = blockIdx.x >> 3;
  const int hd = x * 4 + (idx >> 3), qb = idx & 7;
  const int r32 = lane & 31, hi = lane >> 5;
  const u16* Q = qkv + ((size_t)(0 * 32 + hd) * S_) * 32;
  const u16* Kp = qkv + ((size_t)(1 * 32 + hd) * S_) * 32;
  const u16* Vp = qkv + ((size_t)(2 * 32 + hd) * S_) * 32;
  const int q0 = qb * 256 + wq * 32;
  const bf16x8 qf0 = *(const bf16x8*)(Q + (size_t)(q0 + r32) * 32 + hi * 8);
  const bf16x8 qf1 = *(const bf16x8*)(Q + (size_t)(q0 + r32) * 32 + 16 + hi * 8);

  f32x16 oacc;
  float m, l;
  const int mcg = 32 + (blockIdx.x >> 2), mks = blockIdx.x & 3;
  const u16* wm = wmod + (size_t)(mks * 256) * 6144 + mcg * 64;
  unsigned cc = *(const unsigned*)(cvec + mks * 256 + 2 * (tid >> 3));
  asm volatile("" : "+v"(cc));
  attn_pass<false>(smem, Kp, Vp, qf0, qf1, wid, lane, oacc, m, l, pw0, pw1, pw2, pw3, wm, cc);
  float* xb = (float*)smem + wq * (20 * 64);
  const float* mred = (const float*)(smem + A2_RED);
  volatile LAS int* badf = (volatile LAS int*)(smem + A2_FLG);
  {
    float chk = l;
#pragma unroll
    for (int r = 0; r < 16; ++r) chk += __builtin_fabsf(oacc[r]);
    const int bad = __any(!(chk < 1.0e37f));
    if (lane == 0) badf[wid] = bad;
    __syncthreads();
    const int anybad = __any(badf[lane & 15] != 0);
    if (anybad) {
      __syncthreads();
      attn_pass<true>(smem, Kp, Vp, qf0, qf1, wid, lane, oacc, m, l, pw0, pw1, pw2, pw3, wm, cc);
      __syncthreads();
    }
  }
  xb[wk * 64 + lane] = m; xb[(2 + wk) * 64 + lane] = l;
#pragma unroll
  for (int r = 0; r < 8; ++r) xb[(4 + 8 * wk + r) * 64 + lane] = wk ? oacc[r] : oacc[8 + r];
  __syncthreads();
  {
    const float m2 = xb[(wk ^ 1) * 64 + lane], l2 = xb[(2 + (wk ^ 1)) * 64 + lane];
    const float mn = __builtin_fmaxf(m, m2);
    const float f1 = __builtin_amdgcn_exp2f((m - mn) * 0.5f), f2 = __builtin_amdgcn_exp2f((m2 - mn) * 0.5f);
    const float inv = 1.0f / ((l * f1) * f1 + (l2 * f2) * f2);
    u16* op = o + ((size_t)hd * S_ + q0 + r32) * 32 + 4 * hi + 16 * wk;
#pragma unroll
    for (int g = 0; g < 2; ++g) {
      float v[4];
#pragma unroll
      for (int e = 0; e < 4; ++e) {
        const float own = wk ? oacc[8 + 4 * g + e] : oacc[4 * g + e];
        v[e] = ((own * f1) * f1 + (xb[(4 + 8 * (wk ^ 1) + 4 * g + e) * 64 + lane] * f2) * f2) * inv;
      }
      u32x2 w;
      w[0] = pack2(v[0], v[1]);
      w[1] = pack2(v[2], v[3]);
      *(u32x2*)(op + 8 * g) = w;
    }
  }
  if (wid == 15) {
    float sacc = 0.f;
#pragma unroll
    for (int w = 0; w < 16; ++w) sacc += mred[w * 64 + lane];
    modp[mks * 6144 + mcg * 64 + lane] = sacc;
  }
  STAMP_END(3);
}

template <int MODE> static void launch_gemm(const GemmP& p, hipStream_t stream) {
  using C = GCfg<MODE>;
  constexpr int lds0_ = C::NS * ((C::AD ? 0 : C::BM) + C::BN) * 128, lds = lds0_ < 20480 ? 20480 : lds0_;
  hipFuncSetAttribute((const void*)gemm_kernel<MODE>, hipFuncAttributeMaxDynamicSharedMemorySize, lds);
  gemm_kernel<MODE><<<C::NTM * C::NTN, C::WM * C::WN * 64 * (C::LW ? 2 : 1), lds, stream>>>(p);
}

extern "C" void kernel_launch(void* const* d_in, const int* in_sizes, int n_in, void* d_out, int out_size,
                              void* d_ws, size_t ws_size, hipStream_t stream) {
  const u16* x = (const u16*)d_in[0];
  const u16* wqkv = (const u16*)d_in[1];
  const u16* wo = (const u16*)d_in[2];
  const u16* wg = (const u16*)d_in[4];
  const u16* wu = (const u16*)d_in[5];
  const u16* wd = (const u16*)d_in[6];
  const u16* c = (const u16*)d_in[7];
  const u16* wmod = (const u16*)d_in[8];
  u16* out = (u16*)d_out;

  char* ws = (char*)d_ws;
  float* mod = (float*)ws;
  u16* h = (u16*)(ws + (1 << 20));
  u16* qkv = (u16*)(ws + (8 << 20));
  u16* ao = (u16*)(ws + (24 << 20));
  u16* x1 = (u16*)(ws + (32 << 20));
  u16* hh = (u16*)(ws + (40 << 20));
  float* slab = (float*)(ws + (64 << 20));
  unsigned* flags = (unsigned*)(ws + (512 << 10));

  mod_kernel<<<256, 256, 0, stream>>>(c, wmod, mod, x, wqkv, flags);
  ln_kernel<<<512 + 192, 256, 0, stream>>>(x, mod, 0, h, wqkv, 1);
  { GemmP p{h, wqkv, nullptr, nullptr, nullptr, qkv, nullptr, nullptr}; launch_gemm<0>(p, stream); }
  hipFuncSetAttribute((const void*)attn2_kernel, hipFuncAttributeMaxDynamicSharedMemorySize, A2_LDS);
  attn2_kernel<<<256, 1024, A2_LDS, stream>>>(qkv, ao, wo, wg, wu, wd, c, wmod, mod);
  { GemmP p{ao, wo, nullptr, x, mod + 2 * 1024, x1, nullptr, nullptr}; launch_gemm<1>(p, stream);
  }
  ln_kernel<<<512, 256, 0, stream>>>(x1, mod, 3 * 1024, h, wqkv, 5);
  { GemmP p{h, wg, wu, nullptr, nullptr, hh, nullptr, nullptr}; launch_gemm<2>(p, stream); }
  { GemmP p{hh, wd, nullptr, x1, mod + 5 * 1024, out, slab, flags}; launch_gemm<4>(p, stream); }
}
```

```cpp
#include <hip/hip_runtime.h>
#include <hip/hip_bf16.h>
#include <stdint.h>

typedef unsigned short u16;
typedef short bf16x8 __attribute__((ext_vector_type(8)));
typedef short s16x4 __attribute__((ext_vector_type(4)));
typedef float f32x2 __attribute__((ext_vector_type(2)));
typedef float f32x4 __attribute__((ext_vector_type(4)));
typedef float f32x16 __attribute__((ext_vector_type(16)));
typedef unsigned u32x2 __attribute__((ext_vector_type(2)));
typedef unsigned u32x4 __attribute__((ext_vector_type(4)));
typedef __bf16 bf16x2_t __attribute__((ext_vector_type(2)));
#define LAS __attribute__((address_space(3)))
#define GAS __attribute__((address_space(1)))

constexpr int S_ = 2048, D_ = 1024, H_ = 32, DH_ = 32, F_ = 3584;

__device__ __forceinline__ float bf2f(u16 u) { return __uint_as_float((unsigned)u << 16); }
__device__ __forceinline__ unsigned pack2(float lo, float hi) {
  f32x2 v = {lo, hi};
  bf16x2_t b = __builtin_convertvector(v, bf16x2_t);
  return __builtin_bit_cast(unsigned, b);
}
__device__ __forceinline__ float rbf(float f) {
  __bf16 b = (__bf16)f;
  return __uint_as_float((unsigned)__builtin_bit_cast(u16, b) << 16);
}
__device__ __forceinline__ float lo_f(unsigned u) { return __uint_as_float(u << 16); }
__device__ __forceinline__ float hi_f(unsigned u) { return __uint_as_float(u & 0xffff0000u); }

#define STAMP_BEGIN()
#define STAMP_END(kid)
#define STAMP_MID(kid, i)
#define WAIT_V(n) asm volatile("s_waitcnt vmcnt(%0)" ::"n"(n) : "memory")
#define WAIT_L0() asm volatile("s_waitcnt lgkmcnt(0)" ::: "memory")
#define BAR() __builtin_amdgcn_s_barrier()

__device__ __forceinline__ void glds16(const void* sbase, unsigned voff, unsigned ldsaddr) {
  asm volatile("s_mov_b32 m0, %2\n\ts_nop 0\n\tglobal_load_lds_dwordx4 %0, %1" ::"v"(voff), "s"(sbase), "s"(ldsaddr) : "memory");
}
#define MFMA16(c, a, b) c = __builtin_amdgcn_mfma_f32_16x16x32_bf16(a, b, c, 0, 0, 0)
#define ST16(p, v) asm volatile("global_store_dwordx4 %0, %1, off sc0 sc1\n\ts_nop 1" ::"v"(p), "v"(v) : "memory")
#define SBAR() __builtin_amdgcn_sched_barrier(0)
#define SGB(mask, n) __builtin_amdgcn_sched_group_barrier(mask, n, 0)
template <int I> struct IC { static constexpr int value = I; };
template <int I, int N, class F> __device__ __forceinline__ void sfor(F&& f) {
  if constexpr (I < N) { f(IC<I>{}); sfor<I + 1, N>(f); }
}
__device__ __forceinline__ s16x4 tr_read(const char* p) {
  return __builtin_amdgcn_ds_read_tr16_b64_v4i16((LAS s16x4*)p);
}
__device__ __forceinline__ bf16x8 lds_read16(const char* p) { return *(const LAS bf16x8*)p; }

__device__ __forceinline__ void mod_block(const u16* __restrict__ c, const u16* __restrict__ wmod, float* __restrict__ modp,
                                          int cg, int ks, float* sh) {
  const int tid = threadIdx.x;
  const int cl = tid & 7, rg = tid >> 3;
  const int col0 = cg * 64 + cl * 8;
  u32x4 w[8];
#pragma unroll
  for (int i = 0; i < 8; ++i)
    w[i] = __builtin_nontemporal_load((const u32x4*)(wmod + (size_t)(ks * 256 + rg + i * 32) * 6144 + col0));
  {
    const float v = bf2f(c[ks * 256 + tid]);
    sh[tid] = rbf(v / (1.0f + __expf(-v)));
  }
  __syncthreads();
  float acc[8];
#pragma unroll
  for (int j = 0; j < 8; ++j) acc[j] = 0.f;
#pragma unroll
  for (int i = 0; i < 8; ++i) {
    const float s = sh[rg + i * 32];
#pragma unroll
    for (int j = 0; j < 4; ++j) {
      acc[2 * j] += s * lo_f(w[i][j]);
      acc[2 * j + 1] += s * hi_f(w[i][j]);
    }
  }
  float* red = sh + 256;
#pragma unroll
  for (int j = 0; j < 8; ++j) red[rg * 65 + cl * 8 + j] = acc[j];
  __syncthreads();
  if (tid < 64) {
    float s = 0.f;
#pragma unroll 8
    for (int r = 0; r < 32; ++r) s += red[r * 65 + tid];
    modp[ks * 6144 + cg * 64 + tid] = s;
  }
}
__device__ __forceinline__ void touch_block(const char* src) {
  u32x4 v[8];
#pragma unroll
  for (int i = 0; i < 8; ++i) v[i] = *(const u32x4*)(src + i * 4096 + threadIdx.x * 16);
#pragma unroll
  for (int i = 0; i < 8; ++i) asm volatile("" ::"v"(v[i]));
}
__device__ __forceinline__ f32x4 ld_mod4(const float* modp, int idx) {
  f32x4 a = *(const f32x4*)(modp + idx), b = *(const f32x4*)(modp + 6144 + idx);
  f32x4 c = *(const f32x4*)(modp + 2 * 6144 + idx), d = *(const f32x4*)(modp + 3 * 6144 + idx);
  f32x4 s = (a + b) + (c + d);
  return (f32x4){rbf(s[0]), rbf(s[1]), rbf(s[2]), rbf(s[3])};
}

__global__ __launch_bounds__(256) void mod_kernel(const u16* __restrict__ c, const u16* __restrict__ wmod,
                                                  float* __restrict__ modp, const u16* __restrict__ pf0,
                                                  const u16* __restrict__ pf1, unsigned* flags) {
  __shared__ float sh[256 + 32 * 65];
  STAMP_BEGIN();
  const int b = blockIdx.x;
  if (threadIdx.x == 0 && b < 256) flags[b * 32] = 0u;
  if (b < 128) mod_block(c, wmod, modp, b >> 2, b & 3, sh);
  else if (b < 256) touch_block((const char*)pf0 + (size_t)(b - 128) * 32768);
  else touch_block((const char*)pf1 + (size_t)(b - 256) * 32768);
  STAMP_END(0);
}

__global__ __launch_bounds__(256) void ln_kernel(const u16* __restrict__ x, const float* modp, int moff, u16* __restrict__ h,
                                                 const u16* __restrict__ pf, int kid) {
  __shared__ float sh[2048];
  STAMP_BEGIN();
  const int tid = threadIdx.x;
  if (blockIdx.x >= 512) { touch_block((const char*)pf + (size_t)(blockIdx.x - 512) * 32768); return; }
  const int lane = tid & 63, wid = tid >> 6;
  const int row = blockIdx.x * 4 + wid;
  const u16* xr = x + (size_t)row * D_;
  const u32x4 v0 = *(const u32x4*)(xr + lane * 8);
  const u32x4 v1 = *(const u32x4*)(xr + 512 + lane * 8);
  {
    const f32x4 s0 = ld_mod4(modp, moff + tid * 4);
    const f32x4 s1 = ld_mod4(modp, moff + 1024 + tid * 4);
    *(f32x4*)(sh + tid * 4) = s0;
    *(f32x4*)(sh + 1024 + tid * 4) = (f32x4){rbf(1.0f + s1[0]), rbf(1.0f + s1[1]), rbf(1.0f + s1[2]), rbf(1.0f + s1[3])};
  }
  float f[16];
#pragma unroll
  for (int j = 0; j < 4; ++j) {
    f[2 * j] = lo_f(v0[j]); f[2 * j + 1] = hi_f(v0[j]);
    f[8 + 2 * j] = lo_f(v1[j]); f[8 + 2 * j + 1] = hi_f(v1[j]);
  }
  float s = 0.f;
#pragma unroll
  for (int j = 0; j < 16; ++j) s += f[j];
#pragma unroll
  for (int o = 32; o >= 1; o >>= 1) s += __shfl_xor(s, o);
  const float mean = s * (1.0f / D_);
  float q = 0.f;
#pragma unroll
  for (int j = 0; j < 16; ++j) { float d = f[j] - mean; q += d * d; }
#pragma unroll
  for (int o = 32; o >= 1; o >>= 1) q += __shfl_xor(q, o);
  const float mb = rbf(mean);
  const float vb = rbf(q * (1.0f / D_));
  const float sd = rbf(sqrtf(rbf(vb + 1e-5f)));
  const float inv = 1.0f / sd;
  __syncthreads();
  float o[16];
#pragma unroll
  for (int half = 0; half < 2; ++half) {
    const int c0 = half * 512 + lane * 8;
    const f32x4 sa0 = *(const f32x4*)(sh + c0), sa1 = *(const f32x4*)(sh + c0 + 4);
    const f32x4 ca0 = *(const f32x4*)(sh + 1024 + c0), ca1 = *(const f32x4*)(sh + 1024 + c0 + 4);
#pragma unroll
    for (int j = 0; j < 8; ++j) {
      const float shf = j < 4 ? sa0[j & 3] : sa1[j & 3];
      const float sc = j < 4 ? ca0[j & 3] : ca1[j & 3];
      float t = rbf(f[half * 8 + j] - mb);
      t = rbf(t * inv);
      t = rbf(t * sc);
      o[half * 8 + j] = t + shf;
    }
  }
  u16* hr = h + (size_t)row * D_;
  u32x4 w0, w1;
#pragma unroll
  for (int j = 0; j < 4; ++j) {
    w0[j] = pack2(o[2 * j], o[2 * j + 1]);
    w1[j] = pack2(o[8 + 2 * j], o[8 + 2 * j + 1]);
  }
  ST16(hr + lane * 8, w0);
  ST16(hr + 512 + lane * 8, w1);
  STAMP_END(kid);
}

struct GemmP {
  const u16* A; const u16* B0; const u16* B1; const u16* res; const float* gate; u16* out; float* slab; unsigned* flags; u16* out2;
};

#define NS0 3
#define NS1 3
#define NS3 3
#define LW0 1
#define LW1 1
#define LW3 1
#define AD1 0
#define AD4 0
template <int MODE> struct GCfg;
template <> struct GCfg<0> { static constexpr int BM = 128, BN = 192, WM = 2, WN = 2, NS = NS0, K = 1024, LDA = 1024, LDB = 3072, NTM = 16, NTN = 16, RM = 8, RN = 4, LW = LW0, AD = 0; };
template <> struct GCfg<1> { static constexpr int BM = 128, BN = 64, WM = AD1 ? 4 : 2, WN = AD1 ? 1 : 2, NS = NS1, K = 1024, LDA = 1024, LDB = 1024, NTM = 16, NTN = 16, RM = 4, RN = 8, LW = LW1, AD = AD1; };
template <> struct GCfg<2> { static constexpr int BM = 256, BN = 256, WM = 4, WN = 2, NS = 2, K = 1024, LDA = 1024, LDB = 3584, NTM = 8, NTN = 28, RM = 4, RN = 7, LW = 0, AD = 0; };
template <> struct GCfg<3> { static constexpr int BM = 128, BN = 128, WM = 2, WN = 2, NS = NS3, K = 1792, LDA = 3584, LDB = 1024, NTM = 16, NTN = 16, RM = 4, RN = 8, LW = LW3, AD = 0; };

template <> struct GCfg<4> { static constexpr int BM = 128, BN = 64, WM = AD4 ? 4 : 2, WN = AD4 ? 1 : 2, NS = NS3, K = 3584, LDA = 3584, LDB = 1024, NTM = 16, NTN = 16, RM = 4, RN = 8, LW = LW3, AD = AD4; };

template <int MODE>
__global__ __launch_bounds__(GCfg<MODE>::WM* GCfg<MODE>::WN * 64 * (GCfg<MODE>::LW ? 2 : 1), 2) void gemm_kernel(GemmP p) {
  using C = GCfg<MODE>;
  constexpr int BM = C::BM, BN = C::BN, WM = C::WM, WN = C::WN, NS = C::NS, NW = WM * WN;
  constexpr int TM = BM / WM, TN = BN / WN, MI = TM / 16, NI = TN / 16;
  constexpr int AD = C::AD;
  constexpr int A_BYTES = AD ? 0 : BM * 128, B_BYTES = BN * 128, ST_BYTES = A_BYTES + B_BYTES;
  constexpr int GA = AD ? 0 : BM / 8 / NW, GB = BN / 8 / NW, G = GA + GB;
  constexpr int NT = C::K / 64;
  static_assert(BM % (8 * NW) == 0 && BN % (8 * NW) == 0, "stage split");
  static_assert(NT >= NS + 1, "K too small");
  extern __shared__ __attribute__((aligned(16))) char smem[];
  STAMP_BEGIN();

  const int tid = threadIdx.x, lane = tid & 63;
  constexpr bool LW = C::LW != 0;
  const int wid_all = __builtin_amdgcn_readfirstlane(tid >> 6);
  const int wid = wid_all % NW;
  const int wm = wid / WN, wn = wid % WN;
  int tm, tn;
  {
    const int x = blockIdx.x & 7, idx = blockIdx.x >> 3;
    constexpr int NRM = C::NTM / C::RM;
    const int rm = x % NRM, rn = x / NRM;
    tm = rm * C::RM + idx % C::RM;
    tn = rn * C::RN + idx / C::RM;
  }
  int kh = 0;
  if constexpr (MODE == 3) { kh = tn & 1; tn >>= 1; }
  const int m0 = tm * BM;

  const int par = wid & 1;
  const int a_ch = (lane & 7) ^ ((4 * par + (lane >> 4)) & 7);
  const unsigned a_voff = (MODE == 1) ? (unsigned)((((a_ch >> 2) * S_ + (lane >> 3)) * 32 + (a_ch & 3) * 8) * 2)
                                      : (unsigned)(((lane >> 3) * C::LDA + 8 * a_ch) * 2);
  const unsigned b_voff = (unsigned)(((lane >> 3) * C::LDB + 8 * ((lane & 7) ^ ((((lane >> 4) & 1) | (par << 1)) << 1))) * 2);
  const char* Abase = (MODE == 1) ? (const char*)(p.A + (size_t)m0 * 32) : (const char*)(p.A + (size_t)m0 * C::LDA + kh * C::K);
  const unsigned lds0 = (unsigned)(size_t)(LAS char*)smem;

  auto issue1 = [&](auto gi, int t, int bufoff) {
    constexpr int g = decltype(gi)::value;
    if constexpr (g < GA) {
      const int i = wid + NW * g;
      const char* gp = (MODE == 1) ? Abase + ((size_t)(8 * i) * 32 + (size_t)t * 2 * S_ * 32) * 2
                                   : Abase + ((size_t)(8 * i) * C::LDA + (size_t)t * 64) * 2;
      glds16(gp, a_voff, lds0 + bufoff + i * 1024);
    } else {
      const int i = wid + NW * (g - GA);
      const int pn = i >> 3, kr = 8 * (i & 7);
      const u16* bp;
      if constexpr (MODE == 2) bp = (pn < 2 ? p.B0 : p.B1) + tn * 128 + (pn & 1) * 64;
      else bp = p.B0 + (size_t)kh * C::K * C::LDB + tn * BN + pn * 64;
      const char* gp = (const char*)(bp + ((size_t)t * 64 + kr) * C::LDB);
      glds16(gp, b_voff, lds0 + bufoff + A_BYTES + i * 1024);
    }
  };

  if constexpr (C::LW == 1) {
    if (wid_all >= NW) {
      sfor<0, NS>([&](auto sc_) { sfor<0, G>([&](auto gc) { issue1(gc, decltype(sc_)::value, decltype(sc_)::value * ST_BYTES); }); });
      WAIT_V((NS - 1) * G);
      BAR();
      int lb = 0;
      for (int t = 0; t < NT - NS; ++t) {
        WAIT_V((NS - 2) * G);
        BAR();
        sfor<0, G>([&](auto gc) { issue1(gc, t + NS, lb); });
        lb = (lb + ST_BYTES == NS * ST_BYTES) ? 0 : lb + ST_BYTES;
      }
      sfor<0, NS - 1>([&](auto rc) { WAIT_V((NS - 2 - decltype(rc)::value) * G); BAR(); });
      return;
    }
  }
  if constexpr (C::LW == 2) {
    if (wid_all >= NW) {
      static_assert(NS == 3 && (NT & 1) == 0, "reg loader");
      u32x4 R0[G], R1[G];
      auto ld = [&](u32x4* R, int t) {
        sfor<0, G>([&](auto gi) {
          constexpr int g = decltype(gi)::value;
          if constexpr (g < GA) {
            const int i = wid + NW * g;
            R[g] = *(const u32x4*)(Abase + ((size_t)(8 * i) * C::LDA + (size_t)t * 64) * 2 + a_voff);
          } else {
            const int i = wid + NW * (g - GA);
            const int pn = i >> 3, kr = 8 * (i & 7);
            const u16* bp = p.B0 + (size_t)kh * C::K * C::LDB + tn * BN + pn * 64;
            R[g] = *(const u32x4*)((const char*)(bp + ((size_t)t * 64 + kr) * C::LDB) + b_voff);
          }
        });
      };
      auto st = [&](const u32x4* R, int bufoff) {
        sfor<0, G>([&](auto gi) {
          constexpr int g = decltype(gi)::value;
          const int off = g < GA ? (wid + NW * g) * 1024 : A_BYTES + (wid + NW * (g - GA)) * 1024;
          *(LAS u32x4*)(smem + bufoff + off + lane * 16) = R[g];
        });
      };
      ld(R0, 0); ld(R1, 1);
      st(R0, 0); ld(R0, 2);
      WAIT_L0(); BAR();
      st(R1, ST_BYTES); ld(R1, 3);
      st(R0, 2 * ST_BYTES); ld(R0, 4);
      int lb = 0;
      for (int t = 0; t < NT - 4; t += 2) {
        WAIT_L0(); BAR();
        st(R1, lb); if (t + 5 < NT) ld(R1, t + 5);
        lb = (lb + ST_BYTES == NS * ST_BYTES) ? 0 : lb + ST_BYTES;
        WAIT_L0(); BAR();
        st(R0, lb); if (t + 6 < NT) ld(R0, t + 6);
        lb = (lb + ST_BYTES == NS * ST_BYTES) ? 0 : lb + ST_BYTES;
      }
      WAIT_L0(); BAR();
      st(R1, lb);
      WAIT_L0(); BAR();
      BAR();
      return;
    }
  }

  const int fr = lane & 15, fq = lane >> 4;
  u32x4 rv[4];
  f32x4 gv[NI];
  if constexpr (MODE == 1 || MODE == 3 || MODE == 4) {
#pragma unroll
    for (int i = 0; i < 4; ++i) {
      if constexpr (MODE != 3) rv[i] = *(const u32x4*)(p.res + (size_t)(m0 + i * 32 + (tid >> 3)) * D_ + tn * BN + (tid & 7) * 8);
      else rv[i] = *(const u32x4*)(p.res + (size_t)(m0 + kh * 64 + i * 16 + (tid >> 4)) * D_ + tn * BN + (tid & 15) * 8);
    }
#pragma unroll
    for (int ni = 0; ni < NI; ++ni) { gv[ni] = ld_mod4(p.gate, tn * BN + wn * TN + ni * 16 + 4 * fq); asm volatile("" : "+v"(gv[ni])); }
  }
  f32x4 lsh[2], lsc[2];
  if constexpr (MODE == 1) {
#pragma unroll
    for (int j = 0; j < 2; ++j) {
      lsh[j] = ld_mod4(p.gate, 1024 + tn * BN + (tid & 7) * 8 + 4 * j);
      const f32x4 t = ld_mod4(p.gate, 2048 + tn * BN + (tid & 7) * 8 + 4 * j);
      lsc[j] = (f32x4){rbf(1.0f + t[0]), rbf(1.0f + t[1]), rbf(1.0f + t[2]), rbf(1.0f + t[3])};
      asm volatile("" : "+v"(lsh[j]), "+v"(lsc[j]));
    }
  }
  const int a_rd0 = (wm * TM + fr) * 128 + ((fq ^ (fr >> 1)) << 4);
  const int a_rd1 = a_rd0 ^ 64;
  int b_rd[(MODE == 2) ? 4 : NI];
  {
    const int q = (lane & 15) >> 2, pp = lane & 3;
    const int gl = ((q >> 1) & 1) | ((fq & 1) << 1);
#pragma unroll
    for (int ni = 0; ni < ((MODE == 2) ? 4 : NI); ++ni) {
      int pn, un;
      if constexpr (MODE == 2) { pn = wn; un = ni & 3; }
      else { const int j = wn * NI + ni; pn = j >> 2; un = j & 3; }
      b_rd[ni] = A_BYTES + pn * 8192 + (8 * fq + q) * 128 + ((un ^ gl) << 5) + pp * 8;
    }
  }

  f32x4 acc[MI][NI];
#pragma unroll
  for (int mi = 0; mi < MI; ++mi)
#pragma unroll
    for (int ni = 0; ni < NI; ++ni) acc[mi][ni] = (f32x4){0.f, 0.f, 0.f, 0.f};

  constexpr int NSP = (MODE == 2) ? 2 : 1;
  constexpr int NH = NI / NSP, NM = MI * NH, NPH = 2 * NSP;
  constexpr int GW = (NS >= 3) ? NPH : NPH / 2;
  constexpr int GPP = (G + GW - 1) / GW;
  bf16x8 a0[MI], a1[MI], b0[NH], b1[NH];
  s16x4 blo[NH];

  auto rd_b = [&](auto ri, bf16x8* BF, const char* sb, int ks, int hf) {
    constexpr int r = decltype(ri)::value, ni = r >> 1;
    const int bo = (MODE == 2) ? b_rd[ni] + hf * 16384 : b_rd[ni];
    if constexpr ((r & 1) == 0) blo[ni] = tr_read(sb + bo + ks * 4096);
    else BF[ni] = __builtin_shufflevector(blo[ni], tr_read(sb + bo + ks * 4096 + 512), 0, 1, 2, 3, 4, 5, 6, 7);
  };
  auto rd_a = [&](auto ri, bf16x8* AF, const char* sb, int ks) {
    constexpr int mi = decltype(ri)::value;
    AF[mi] = lds_read16(sb + (ks ? a_rd1 : a_rd0) + mi * 2048);
  };
  auto phase = [&](auto hfc, auto rdm, auto g0c, auto g1c, bf16x8* AF, bf16x8* BF, bf16x8* AN_, bf16x8* BN_,
                   const char* rsb, int rks, int rhf, int gt, int gbuf) {
    constexpr int hf = decltype(hfc)::value, RDM = decltype(rdm)::value;
    constexpr int g0 = decltype(g0c)::value, g1 = decltype(g1c)::value, NG = g1 - g0;
    constexpr int NR = RDM == 0 ? 0 : (RDM == 1 ? 2 * NH : 2 * NH + MI);
    sfor<0, NM>([&](auto ic) {
      constexpr int i = decltype(ic)::value, mi = i / NH, ni = i % NH;
      MFMA16(acc[mi][hf * NH + ni], BF[ni], AF[mi]);
      sfor<0, NR>([&](auto rc) {
        constexpr int r = decltype(rc)::value;
        if constexpr ((r * (NM - NM / 4)) / (NR > 0 ? NR : 1) == i) {
          if constexpr (r < 2 * NH) rd_b(rc, BN_, rsb, rks, rhf);
          else rd_a(IC<r - 2 * NH>{}, AN_, rsb, rks);
        }
      });
      sfor<0, NG>([&](auto gc) {
        constexpr int g = decltype(gc)::value;
        if constexpr ((g * NM) / (NG > 0 ? NG : 1) == i) issue1(IC<g0 + g>{}, gt, gbuf);
      });
      SBAR();
    });
  };
  constexpr auto gmin = [](int a, int b) { return a < b ? a : b; };
  int cur = 0, prv = 0;
  auto ktile = [&](auto vmc, auto restc, auto firstc, int t) {
    constexpr int VM = decltype(vmc)::value;
    constexpr bool REST = decltype(restc)::value && !LW, FIRST = decltype(firstc)::value && !LW;
    const int nxt = (cur + ST_BYTES == NS * ST_BYTES) ? 0 : cur + ST_BYTES;
    const char* sc = smem + cur;
    const char* sn = smem + nxt;
    constexpr int R1 = REST ? gmin(G, GPP) : 0, R2 = REST ? gmin(G, 2 * GPP) : 0, R3 = REST ? gmin(G, 3 * GPP) : 0, R4 = REST ? gmin(G, 4 * GPP) : 0;
    constexpr int F1 = FIRST ? gmin(G, GPP) : 0;
    if constexpr (NSP == 1) {
      phase(IC<0>{}, IC<2>{}, IC<R1>{}, IC<R2>{}, a0, b0, a1, b1, sc, 1, 0, t - 1 + NS, prv);
      if constexpr (VM >= 0) {
        if constexpr (!LW) WAIT_V(VM);
        WAIT_L0(); BAR();
        phase(IC<0>{}, IC<2>{}, IC<0>{}, IC<F1>{}, a1, b1, a0, b0, sn, 0, 0, t + NS, cur);
      } else {
        phase(IC<0>{}, IC<0>{}, IC<0>{}, IC<0>{}, a1, b1, a0, b0, sn, 0, 0, 0, 0);
      }
    } else {
      phase(IC<0>{}, IC<1>{}, IC<R1>{}, IC<R2>{}, a0, b0, a1, b1, sc, 0, 1, t - 1 + NS, prv);
      phase(IC<1>{}, IC<2>{}, IC<R2>{}, IC<R3>{}, a0, b1, a1, b0, sc, 1, 0, t - 1 + NS, prv);
      phase(IC<0>{}, IC<1>{}, IC<R3>{}, IC<R4>{}, a1, b0, a1, b1, sc, 1, 1, t - 1 + NS, prv);
      if constexpr (VM >= 0) {
        WAIT_V(VM); WAIT_L0(); BAR();
        phase(IC<1>{}, IC<2>{}, IC<0>{}, IC<F1>{}, a1, b1, a0, b0, sn, 0, 0, t + NS, cur);
      } else {
        phase(IC<1>{}, IC<0>{}, IC<0>{}, IC<0>{}, a1, b1, a0, b0, sn, 0, 0, 0, 0);
      }
    }
    prv = cur; cur = nxt;
  };

  if constexpr (AD > 0) {
    static_assert(LW && NSP == 1 && NT % AD == 0, "A-direct path");
    bf16x8 ar[AD][2][MI];
    const char* Aw = (MODE == 1) ? Abase + ((size_t)(wm * TM + fr) * 32 + fq * 8) * 2
                                 : Abase + ((size_t)(wm * TM + fr) * C::LDA + fq * 8) * 2;
    auto lda = [&](auto dc, int t) {
      constexpr int d = decltype(dc)::value;
#pragma unroll
      for (int ks = 0; ks < 2; ++ks)
#pragma unroll
        for (int mi = 0; mi < MI; ++mi) {
          if constexpr (MODE == 1) ar[d][ks][mi] = *(const bf16x8*)(Aw + ((size_t)(2 * t + ks) * S_ + mi * 16) * 64);
          else ar[d][ks][mi] = *(const bf16x8*)(Aw + (size_t)mi * 16 * C::LDA * 2 + t * 128 + ks * 64);
        }
    };
    sfor<0, AD>([&](auto dc) { lda(dc, decltype(dc)::value); });
    BAR();
    STAMP_MID(MODE == 0 ? 2 : MODE == 1 ? 4 : MODE == 2 ? 6 : 7, 0);
    sfor<0, 2 * NH>([&](auto rc) { rd_b(rc, b0, smem, 0, 0); });
    auto ktile_ad = [&](auto dc, auto ldc, auto lastc, int t) {
      constexpr int d = decltype(dc)::value;
      const int nxt = (cur + ST_BYTES == NS * ST_BYTES) ? 0 : cur + ST_BYTES;
      phase(IC<0>{}, IC<1>{}, IC<0>{}, IC<0>{}, ar[d][0], b0, a1, b1, smem + cur, 1, 0, 0, 0);
      if constexpr (!decltype(lastc)::value) {
        WAIT_L0(); BAR();
        phase(IC<0>{}, IC<1>{}, IC<0>{}, IC<0>{}, ar[d][1], b1, a0, b0, smem + nxt, 0, 0, 0, 0);
      } else {
        phase(IC<0>{}, IC<0>{}, IC<0>{}, IC<0>{}, ar[d][1], b1, a0, b0, smem + nxt, 0, 0, 0, 0);
      }
      if constexpr (decltype(ldc)::value) { lda(dc, t + AD); SBAR(); }
      cur = nxt;
    };
    for (int t = 0; t < NT - AD; t += AD) sfor<0, AD>([&](auto dc) { ktile_ad(dc, IC<1>{}, IC<0>{}, t + decltype(dc)::value); });
    sfor<0, AD>([&](auto dc) { ktile_ad(dc, IC<0>{}, IC<(decltype(dc)::value == AD - 1)>{}, 0); });
  } else {
  if constexpr (!LW) {
    sfor<0, NS>([&](auto sc_) { sfor<0, G>([&](auto gc) { issue1(gc, decltype(sc_)::value, decltype(sc_)::value * ST_BYTES); }); });
    WAIT_V((NS - 1) * G);
  }
  BAR();
  STAMP_MID(MODE == 0 ? 2 : MODE == 1 ? 4 : MODE == 2 ? 6 : 7, 0);
  sfor<0, 2 * NH>([&](auto rc) { rd_b(rc, b0, smem, 0, 0); });
  sfor<0, MI>([&](auto rc) { rd_a(rc, a0, smem, 0); });
  using TT = IC<1>; using FF = IC<0>;
  ktile(IC<(NS - 2) * G>{}, FF{}, TT{}, 0);
  for (int t = 1; t < NT - NS; ++t) ktile(IC<(NS - 2) * G>{}, TT{}, TT{}, t);
  ktile(IC<(NS - 2) * G>{}, TT{}, FF{}, NT - NS);
  sfor<0, NS - 2>([&](auto rc) { ktile(IC<(NS - 3 - decltype(rc)::value) * G>{}, FF{}, FF{}, 0); });
  ktile(IC<-1>{}, FF{}, FF{}, 0);
  }

  STAMP_MID(MODE == 0 ? 2 : MODE == 1 ? 4 : MODE == 2 ? 6 : 7, 1);
  if constexpr (MODE == 0) {
    const float qs = 0.17677669529663687f * 1.4426950408889634f;
    BAR();
    char* st = smem + wid * (64 * 208);
#pragma unroll
    for (int ni = 0; ni < NI; ++ni) {
      const int n = tn * BN + wn * TN + ni * 16;
      const float sc = n < 1024 ? qs : 1.0f;
#pragma unroll
      for (int mi = 0; mi < MI; ++mi) {
        u32x2 w;
        w[0] = pack2(acc[mi][ni][0] * sc, acc[mi][ni][1] * sc);
        w[1] = pack2(acc[mi][ni][2] * sc, acc[mi][ni][3] * sc);
        *(LAS u32x2*)(st + (mi * 16 + fr) * 208 + ni * 32 + fq * 8) = w;
      }
    }
#pragma unroll
    for (int j = 0; j < 3; ++j) {
      const int hh = (tn * BN + wn * TN + j * 32) >> 5;
      u16* ob = p.out + ((size_t)hh * S_ + m0 + wm * TM) * 32 + lane * 8;
#pragma unroll
      for (int i = 0; i < 4; ++i) {
        const u32x4 v = *(const LAS u32x4*)(st + (i * 16 + (lane >> 2)) * 208 + j * 64 + (lane & 3) * 16);
        ST16(ob + i * 512, v);
      }
    }
  } else if constexpr (MODE == 3) {
    const int tile = tm * 8 + tn;
    if (wm != kh) {
      float* dst = p.slab + ((size_t)(tile * 2 + kh) * 2 + wn) * 4096 + lane * 4;
#pragma unroll
      for (int mi = 0; mi < MI; ++mi)
#pragma unroll
        for (int ni = 0; ni < NI; ++ni) ST16(dst + (mi * NI + ni) * 256, __builtin_bit_cast(u32x4, acc[mi][ni]));
      WAIT_V(0);
      if (lane == 0) __hip_atomic_fetch_add(p.flags + (tile * 2 + kh) * 32, 1u, __ATOMIC_RELAXED, __HIP_MEMORY_SCOPE_AGENT);
      BAR();
    } else {
      const unsigned* fl = p.flags + (tile * 2 + (kh ^ 1)) * 32;
      for (int spin = 0; spin < (1 << 22); ++spin) {
        if (__builtin_amdgcn_readfirstlane(__hip_atomic_load(fl, __ATOMIC_RELAXED, __HIP_MEMORY_SCOPE_AGENT)) >= 2u) break;
        __builtin_amdgcn_s_sleep(1);
      }
      const float* src = p.slab + ((size_t)(tile * 2 + (kh ^ 1)) * 2 + wn) * 4096 + lane * 4;
      f32x4 pv[MI][NI];
#pragma unroll
      for (int mi = 0; mi < MI; ++mi)
#pragma unroll
        for (int ni = 0; ni < NI; ++ni)
          asm volatile("global_load_dwordx4 %0, %1, off sc0 sc1" : "=v"(pv[mi][ni]) : "v"(src + (mi * NI + ni) * 256) : "memory");
      BAR();
      asm volatile("s_waitcnt vmcnt(0)" ::: "memory");
      SBAR();
#pragma unroll
      for (int ni = 0; ni < NI; ++ni)
#pragma unroll
        for (int mi = 0; mi < MI; ++mi) {
          asm volatile("" : "+v"(pv[mi][ni]));
          const f32x4 a = acc[mi][ni] + pv[mi][ni];
          u32x2 w;
          w[0] = pack2(gv[ni][0] * rbf(a[0]), gv[ni][1] * rbf(a[1]));
          w[1] = pack2(gv[ni][2] * rbf(a[2]), gv[ni][3] * rbf(a[3]));
          *(LAS u32x2*)(smem + (mi * 16 + fr) * 272 + wn * 128 + ni * 32 + fq * 8) = w;
        }
    }
    WAIT_L0();
    BAR();
#pragma unroll
    for (int i = 0; i < 4; ++i) {
      const u32x4 v = *(const LAS u32x4*)(smem + (i * 16 + (tid >> 4)) * 272 + (tid & 15) * 16);
      u32x4 w;
#pragma unroll
      for (int j = 0; j < 4; ++j) w[j] = pack2(lo_f(rv[i][j]) + lo_f(v[j]), hi_f(rv[i][j]) + hi_f(v[j]));
      ST16(p.out + (size_t)(m0 + kh * 64 + i * 16 + (tid >> 4)) * D_ + tn * BN + (tid & 15) * 8, w);
    }
  } else if constexpr (MODE == 1 || MODE == 4) {
    BAR();
#pragma unroll
    for (int ni = 0; ni < NI; ++ni) {
#pragma unroll
      for (int mi = 0; mi < MI; ++mi) {
        u32x2 w;
        w[0] = pack2(gv[ni][0] * rbf(acc[mi][ni][0]), gv[ni][1] * rbf(acc[mi][ni][1]));
        w[1] = pack2(gv[ni][2] * rbf(acc[mi][ni][2]), gv[ni][3] * rbf(acc[mi][ni][3]));
        *(LAS u32x2*)(smem + (wm * TM + mi * 16 + fr) * 144 + wn * 64 + ni * 32 + fq * 8) = w;
      }
    }
    WAIT_L0();
    BAR();
    u32x4 xw[4];
#pragma unroll
    for (int i = 0; i < 4; ++i) {
      const u32x4 v = *(const LAS u32x4*)(smem + (i * 32 + (tid >> 3)) * 144 + (tid & 7) * 16);
#pragma unroll
      for (int j = 0; j < 4; ++j) xw[i][j] = pack2(lo_f(rv[i][j]) + lo_f(v[j]), hi_f(rv[i][j]) + hi_f(v[j]));
      ST16(p.out + (size_t)(m0 + i * 32 + (tid >> 3)) * D_ + tn * BN + (tid & 7) * 8, xw[i]);
    }
    if constexpr (MODE == 1) {
      unsigned long long* stats = (unsigned long long*)p.slab + (size_t)tm * 16 * 128;
#pragma unroll
      for (int i = 0; i < 4; ++i) {
        float sm = 0.f;
#pragma unroll
        for (int j = 0; j < 4; ++j) sm += lo_f(xw[i][j]) + hi_f(xw[i][j]);
        sm += __shfl_xor(sm, 1); sm += __shfl_xor(sm, 2); sm += __shfl_xor(sm, 4);
        const float mu = sm * (1.0f / 64.0f);
        float q = 0.f;
#pragma unroll
        for (int j = 0; j < 4; ++j) { const float a = lo_f(xw[i][j]) - mu, b = hi_f(xw[i][j]) - mu; q += a * a + b * b; }
        q += __shfl_xor(q, 1); q += __shfl_xor(q, 2); q += __shfl_xor(q, 4);
        if ((tid & 7) == 0)
          __hip_atomic_store(stats + tn * 128 + i * 32 + (tid >> 3), ((unsigned long long)__float_as_uint(q) << 32) | __float_as_uint(mu),
                             __ATOMIC_RELAXED, __HIP_MEMORY_SCOPE_AGENT);
      }
      WAIT_V(0);
      BAR();
      if (wid == 0) {
        unsigned* cnt = p.flags + tm * 32;
        if (lane == 0) __hip_atomic_fetch_add(cnt, 1u, __ATOMIC_RELAXED, __HIP_MEMORY_SCOPE_AGENT);
        for (int spin = 0; spin < (1 << 22); ++spin) {
          if (__builtin_amdgcn_readfirstlane(__hip_atomic_load(cnt, __ATOMIC_RELAXED, __HIP_MEMORY_SCOPE_AGENT)) >= 16u) break;
          __builtin_amdgcn_s_sleep(1);
        }
      }
      asm volatile("" ::: "memory");
      BAR();
      asm volatile("" ::: "memory");
      unsigned long long sv[4][2];
#pragma unroll
      for (int i = 0; i < 4; ++i)
#pragma unroll
        for (int j = 0; j < 2; ++j)
          sv[i][j] = __hip_atomic_load(stats + ((tid & 7) + 8 * j) * 128 + i * 32 + (tid >> 3), __ATOMIC_RELAXED, __HIP_MEMORY_SCOPE_AGENT);
#pragma unroll
      for (int i = 0; i < 4; ++i) {
        const float m0_ = __uint_as_float((unsigned)sv[i][0]), m1_ = __uint_as_float((unsigned)sv[i][1]);
        float sm = m0_ + m1_;
        sm += __shfl_xor(sm, 1); sm += __shfl_xor(sm, 2); sm += __shfl_xor(sm, 4);
        const float mean = sm * (1.0f / 16.0f);
        float q = __uint_as_float((unsigned)(sv[i][0] >> 32)) + __uint_as_float((unsigned)(sv[i][1] >> 32)) +
                  64.0f * ((m0_ - mean) * (m0_ - mean) + (m1_ - mean) * (m1_ - mean));
        q += __shfl_xor(q, 1); q += __shfl_xor(q, 2); q += __shfl_xor(q, 4);
        const float mb = rbf(mean);
        const float vb = rbf(q * (1.0f / D_));
        const float inv = 1.0f / rbf(sqrtf(rbf(vb + 1e-5f)));
        u32x4 hw;
#pragma unroll
        for (int j = 0; j < 4; ++j) {
          float a = rbf(rbf(rbf(lo_f(xw[i][j]) - mb) * inv) * lsc[j >> 1][(2 * j) & 3]) + lsh[j >> 1][(2 * j) & 3];
          float b = rbf(rbf(rbf(hi_f(xw[i][j]) - mb) * inv) * lsc[j >> 1][(2 * j + 1) & 3]) + lsh[j >> 1][(2 * j + 1) & 3];
          hw[j] = pack2(a, b);
        }
        ST16(p.out2 + (size_t)(m0 + i * 32 + (tid >> 3)) * D_ + tn * BN + (tid & 7) * 8, hw);
      }
    }
  } else {
    BAR();
    char* st = smem + wid * (64 * 144);
#pragma unroll
    for (int ni = 0; ni < 4; ++ni)
#pragma unroll
      for (int mi = 0; mi < MI; ++mi) {
        float o[4];
#pragma unroll
        for (int r = 0; r < 4; ++r) o[r] = rbf(acc[mi][ni][r]) * rbf(acc[mi][ni + 4][r]);
        u32x2 w;
        w[0] = pack2(o[0], o[1]);
        w[1] = pack2(o[2], o[3]);
        *(LAS u32x2*)(st + (mi * 16 + fr) * 144 + ni * 32 + fq * 8) = w;
      }
    u16* ob = p.out + (size_t)(m0 + wm * TM + (lane >> 3)) * F_ + tn * 128 + wn * 64 + (lane & 7) * 8;
#pragma unroll
    for (int i = 0; i < 8; ++i) {
      const u32x4 v = *(const LAS u32x4*)(st + (i * 8 + (lane >> 3)) * 144 + (lane & 7) * 16);
      ST16(ob + (size_t)i * 8 * F_, v);
    }
  }
  STAMP_END(MODE == 0 ? 2 : MODE == 1 ? 4 : MODE == 2 ? 6 : 7);
}

#define AT_NSTAGE 6
constexpr int AT_KT = 128, AT_NS = AT_NSTAGE, AT_ST = 16384;
#define MFMA32(a, b, c) __builtin_amdgcn_mfma_f32_32x32x16_bf16(a, b, c, 0, 0, 0)
#define AEXP(x) __builtin_amdgcn_exp2f(x)
#define MX3(a, b, c) __builtin_fmaxf(__builtin_fmaxf((a), (b)), (c))

__device__ __forceinline__ float tile_rowmax(const f32x16& s0, const f32x16& s1) {
  float a = MX3(s0[0], s0[1], s1[0]), b = MX3(s0[2], s0[3], s1[1]);
  a = MX3(a, s1[2], s1[3]);
#pragma unroll
  for (int r = 4; r < 16; r += 4) {
    a = MX3(a, s0[r], s0[r + 1]); b = MX3(b, s0[r + 2], s0[r + 3]);
    a = MX3(a, s1[r], s1[r + 1]); b = MX3(b, s1[r + 2], s1[r + 3]);
  }
  float mx = __builtin_fmaxf(a, b);
  auto rr = __builtin_amdgcn_permlane32_swap(__float_as_uint(mx), __float_as_uint(mx), false, false);
  return __builtin_fmaxf(__uint_as_float(rr[0]), __uint_as_float(rr[1]));
}

__global__ __launch_bounds__(512, 2) void attn_kernel(const u16* __restrict__ qkv, u16* __restrict__ o) {
  extern __shared__ __attribute__((aligned(16))) char smem[];
  STAMP_BEGIN();
  const int tid = threadIdx.x, lane = tid & 63;
  const int wid = __builtin_amdgcn_readfirstlane(tid >> 6);
  const int x = blockIdx.x & 7, idx = blockIdx.x >> 3;
  const int hd = x * 4 + (idx >> 3), qb = idx & 7;
  const int r32 = lane & 31, hi = lane >> 5;
  const u16* Q = qkv + ((size_t)(0 * 32 + hd) * S_) * 32;
  const u16* Kp = qkv + ((size_t)(1 * 32 + hd) * S_) * 32;
  const u16* Vp = qkv + ((size_t)(2 * 32 + hd) * S_) * 32;
  const int q0 = qb * 256 + wid * 32;

  const unsigned k_voff = (unsigned)(((lane >> 2) * 32 + 8 * ((lane & 3) ^ ((lane >> 4) & 3))) * 2);
  const unsigned v_voff = (unsigned)(lane * 16);
  const unsigned lds0 = (unsigned)(size_t)(LAS char*)smem;
  auto issue = [&](int t, int bufoff) {
    const char* gk = (const char*)(Kp + ((size_t)t * AT_KT + wid * 16) * 32);
    const char* gv = (const char*)(Vp + ((size_t)t * AT_KT + wid * 16) * 32);
    glds16(gk, k_voff, lds0 + bufoff + wid * 1024);
    glds16(gv, v_voff, lds0 + bufoff + 8192 + wid * 1024);
  };
#pragma unroll
  for (int s = 0; s < AT_NS - 1; ++s) issue(s, s * AT_ST);
  const bf16x8 qf0 = *(const bf16x8*)(Q + (size_t)(q0 + r32) * 32 + hi * 8);
  const bf16x8 qf1 = *(const bf16x8*)(Q + (size_t)(q0 + r32) * 32 + 16 + hi * 8);

  const int k_rd0 = r32 * 64 + ((hi ^ ((r32 >> 2) & 3)) << 4);
  const int k_rd1 = k_rd0 ^ 32;
  const int v_rd = 8192 + (4 * hi + ((lane & 15) >> 2)) * 64 + ((lane >> 4) & 1) * 32 + (lane & 3) * 8;

  f32x16 oacc = {};
  f32x16 negm;
  float m, l = 0.f;
  f32x16 sa0, sa1, sb0, sb1;
  u32x4 pp0 = {}, pp1 = {}, pp2 = {}, pp3 = {};
  bf16x8 vf0, vf1, vf2, vf3;
#define AT_BIG 1.0e30f
  constexpr float BIG = AT_BIG;
  constexpr int NT = S_ / AT_KT;

#define SCORES(D0, D1, kb, CIN)                                                         \
  do {                                                                                  \
    bf16x8 k00 = lds_read16((kb) + k_rd0), k01 = lds_read16((kb) + k_rd1);              \
    bf16x8 k10 = lds_read16((kb) + 2048 + k_rd0), k11 = lds_read16((kb) + 2048 + k_rd1);\
    D0 = MFMA32(k00, qf0, CIN); D1 = MFMA32(k10, qf0, CIN);                             \
    D0 = MFMA32(k01, qf1, D0);  D1 = MFMA32(k11, qf1, D1);                              \
  } while (0)
#define VFRAG(vb, s) __builtin_shufflevector(tr_read((vb) + (s) * 1024), tr_read((vb) + (s) * 1024 + 512), 0, 1, 2, 3, 4, 5, 6, 7)
#define READV(vb) do { vf0 = VFRAG(vb, 0); vf1 = VFRAG(vb, 1); vf2 = VFRAG(vb, 2); vf3 = VFRAG(vb, 3); } while (0)
#define PV()                                                                            \
  do {                                                                                  \
    oacc = MFMA32(vf0, __builtin_bit_cast(bf16x8, pp0), oacc);                          \
    oacc = MFMA32(vf1, __builtin_bit_cast(bf16x8, pp1), oacc);                          \
    oacc = MFMA32(vf2, __builtin_bit_cast(bf16x8, pp2), oacc);                          \
    oacc = MFMA32(vf3, __builtin_bit_cast(bf16x8, pp3), oacc);                          \
  } while (0)
#define EXPSUM(C0, C1, LS)                                                              \
  do {                                                                                  \
    f32x2 ls2_ = {0.f, 0.f}, ls3_ = {0.f, 0.f};                                         \
    _Pragma("unroll") for (int r = 0; r < 16; r += 2) {                                 \
      C0[r] = AEXP(C0[r]); C0[r + 1] = AEXP(C0[r + 1]);                                 \
      C1[r] = AEXP(C1[r]); C1[r + 1] = AEXP(C1[r + 1]);                                 \
      ls2_ += (f32x2){C0[r], C0[r + 1]};                                                \
      ls3_ += (f32x2){C1[r], C1[r + 1]};                                                \
    }                                                                                   \
    ls2_ += ls3_;                                                                       \
    LS = ls2_[0] + ls2_[1];                                                             \
  } while (0)
#define PACK(C0, C1)                                                                    \
  do {                                                                                  \
    _Pragma("unroll") for (int j = 0; j < 4; ++j) {                                     \
      pp0[j] = pack2(C0[2 * j], C0[2 * j + 1]);                                         \
      pp1[j] = pack2(C0[8 + 2 * j], C0[8 + 2 * j + 1]);                                 \
      pp2[j] = pack2(C1[2 * j], C1[2 * j + 1]);                                         \
      pp3[j] = pack2(C1[8 + 2 * j], C1[8 + 2 * j + 1]);                                 \
    }                                                                                   \
  } while (0)
#define STEP(C0, C1, N0, N1, kcur, knext, vprev, HASN)                                  \
  do {                                                                                  \
    READV(vprev);                                                                       \
    if (HASN) SCORES(N0, N1, knext, negm);                                              \
    PV();                                                                               \
    float ls;                                                                           \
    EXPSUM(C0, C1, ls);                                                                 \
    if (__builtin_expect(__any(!(ls <= BIG)), 0)) {                                     \
                  \
      f32x16 z_ = {};                                                                   \
      SCORES(C0, C1, kcur, z_);                                                         \
      const float mn = __builtin_fmaxf(m, tile_rowmax(C0, C1));                         \
      const float dm = m - mn;                                                          \
      const float f = __builtin_amdgcn_exp2f(dm);                                       \
      m = mn; l *= f;                                                                   \
      _Pragma("unroll") for (int r = 0; r < 16; ++r) {                                  \
        oacc[r] *= f; negm[r] = -mn; C0[r] -= mn; C1[r] -= mn;                          \
        if (HASN) { N0[r] += dm; N1[r] += dm; }                                         \
      }                                                                                 \
      EXPSUM(C0, C1, ls);                                                               \
    }                                                                                   \
    l += ls;                                                                            \
    PACK(C0, C1);                                                                       \
  } while (0)

  WAIT_V(2 * (AT_NS - 2));
  BAR();
  {
    f32x16 z = {};
    SCORES(sa0, sa1, smem, z);
    m = tile_rowmax(sa0, sa1);
#pragma unroll
    for (int r = 0; r < 16; ++r) { negm[r] = -m; sa0[r] -= m; sa1[r] -= m; }
  }
  int cur = 0, prv = 0;
  for (int t = 0; t < NT - 1; ++t) {
    const char* sb = smem + cur;
    int nx = cur + AT_ST; if (nx >= AT_NS * AT_ST) nx = 0;
    int prv2 = cur - AT_ST; if (prv2 < 0) prv2 = (AT_NS - 1) * AT_ST;
    STEP(sa0, sa1, sb0, sb1, sb, sb + 4096, smem + prv + 4096 + v_rd, true);
    if (t + AT_NS - 1 < NT) {
      WAIT_V(2 * (AT_NS - 3));
      WAIT_L0();
      BAR();
      issue(t + AT_NS - 1, prv2);
    } else {
      WAIT_V(0);
      WAIT_L0();
      BAR();
    }
    STEP(sb0, sb1, sa0, sa1, sb + 4096, smem + nx, sb + v_rd, true);
    prv = cur; cur = nx;
  }
  {
    const char* sb = smem + cur;
    STEP(sa0, sa1, sb0, sb1, sb, sb + 4096, smem + prv + 4096 + v_rd, true);
    STEP(sb0, sb1, sa0, sa1, sb + 4096, sb, sb + v_rd, false);
    prv = cur;
  }
  READV(smem + prv + 4096 + v_rd);
  PV();
#undef SCORES
#undef VFRAG
#undef READV
#undef PV
#undef EXPSUM
#undef PACK
#undef STEP
  {
    auto rr = __builtin_amdgcn_permlane32_swap(__float_as_uint(l), __float_as_uint(l), false, false);
    l = __uint_as_float(rr[0]) + __uint_as_float(rr[1]);
  }
  const float inv = 1.0f / l;
  u16* op = o + (size_t)(q0 + r32) * D_ + hd * 32 + 4 * hi;
#pragma unroll
  for (int g = 0; g < 4; ++g) {
    u32x2 w;
    w[0] = pack2(oacc[4 * g] * inv, oacc[4 * g + 1] * inv);
    w[1] = pack2(oacc[4 * g + 2] * inv, oacc[4 * g + 3] * inv);
    *(u32x2*)(op + 8 * g) = w;
  }
  STAMP_END(3);
}

constexpr int A2_KT = 256, A2_NS = 3, A2_ST = 32768, A2_MOD = A2_NS * A2_ST + 1024, A2_RED = A2_MOD + 32768, A2_FLG = A2_RED + 4096, A2_LDS = A2_FLG + 64;
template <bool SAFE>
__device__ __forceinline__ void attn_pass(char* smem, const u16* Kp, const u16* Vp, bf16x8 qf0, bf16x8 qf1, int wid, int lane,
                                          f32x16& oacc, float& m, float& l,
                                          const u16* pw0, const u16* pw1, const u16* pw2, const u16* pw3, const u16* wm, unsigned cc) {
  const int wk = wid >> 3, r32 = lane & 31, hi = lane >> 5;
  const unsigned k_voff = (unsigned)(((lane >> 2) * 32 + 8 * ((lane & 3) ^ ((lane >> 4) & 3))) * 2);
  const unsigned v_voff = (unsigned)(lane * 16);
  const unsigned lds0 = (unsigned)(size_t)(LAS char*)smem;
  auto issue = [&](int t, int bufoff) {
    const char* gk = (const char*)(Kp + ((size_t)t * A2_KT + wid * 16) * 32);
    const char* gv = (const char*)(Vp + ((size_t)t * A2_KT + wid * 16) * 32);
    glds16(gk, k_voff, lds0 + bufoff + wid * 1024);
    glds16(gv, v_voff, lds0 + bufoff + 16384 + wid * 1024);
  };
  issue(0, 0);
  issue(1, A2_ST);
  const int k_rd0 = wk * 8192 + r32 * 64 + ((hi ^ ((r32 >> 2) & 3)) << 4);
  const int k_rd1 = k_rd0 ^ 32;
  const int v_rd = 16384 + wk * 8192 + (4 * hi + ((lane & 15) >> 2)) * 64 + ((lane >> 4) & 1) * 32 + (lane & 3) * 8;
  f32x16 negm = {};
  f32x2 lsa = {0.f, 0.f}, lsb = {0.f, 0.f};
  oacc = (f32x16){};
  m = 0.f; l = 0.f;
  constexpr int NT = S_ / A2_KT;
  int cur = 0;
  for (int t = 0; t < NT; ++t) {
    if (t == 0) WAIT_V(2); else if (!SAFE && t == 4) WAIT_V(5); else if (t + 1 < NT) WAIT_V(3); else WAIT_V(0);
    BAR();
    if (!SAFE && t == 6) {
      const int rg = wid * 8 + (lane >> 3), cl = lane & 7;
      const float c0 = lo_f(cc), c1 = hi_f(cc);
      const float s0 = rbf(c0 / (1.0f + __expf(-c0))), s1 = rbf(c1 / (1.0f + __expf(-c1)));
      const u32x4 w0 = *(const LAS u32x4*)(smem + A2_MOD + (2 * rg) * 128 + cl * 16);
      const u32x4 w1 = *(const LAS u32x4*)(smem + A2_MOD + (2 * rg + 1) * 128 + cl * 16);
      float a[8];
#pragma unroll
      for (int j = 0; j < 4; ++j) {
        a[2 * j] = s0 * lo_f(w0[j]) + s1 * lo_f(w1[j]);
        a[2 * j + 1] = s0 * hi_f(w0[j]) + s1 * hi_f(w1[j]);
      }
#pragma unroll
      for (int j = 0; j < 8; ++j) {
        a[j] += __shfl_xor(a[j], 8); a[j] += __shfl_xor(a[j], 16); a[j] += __shfl_xor(a[j], 32);
      }
      if (lane < 8) {
        float* mred = (float*)(smem + A2_RED);
#pragma unroll
        for (int j = 0; j < 8; ++j) mred[wid * 64 + lane * 8 + j] = a[j];
      }
    }
    if (!SAFE && t == 3) {
      const unsigned m_voff = (unsigned)(((lane >> 3) * 6144 + (lane & 7) * 8) * 2);
      glds16(wm + (size_t)(wid * 16) * 6144, m_voff, lds0 + A2_MOD + wid * 2048);
      glds16(wm + (size_t)(wid * 16 + 8) * 6144, m_voff, lds0 + A2_MOD + wid * 2048 + 1024);
    }
    {
      int ck = (blockIdx.x * NT + t) * 16 + wid;
      if (ck >= 2048 + 3 * 7168) ck -= 16384;
      const u16* pb = pw0;
      if (ck >= 2048) { pb = pw1; ck -= 2048; if (ck >= 7168) { pb = pw2; ck -= 7168; if (ck >= 7168) { pb = pw3; ck -= 7168; } } }
      glds16((const char*)pb + (size_t)ck * 1024, v_voff, lds0 + A2_NS * A2_ST);
      int nb = cur + 2 * A2_ST; if (nb >= A2_NS * A2_ST) nb -= A2_NS * A2_ST;
      if (t + 2 < NT) issue(t + 2, nb);
    }
#pragma unroll
    for (int hb = 0; hb < 2; ++hb) {
      const char* kb = smem + cur + hb * 4096;
      const bf16x8 k00 = lds_read16(kb + k_rd0), k01 = lds_read16(kb + k_rd1);
      const bf16x8 k10 = lds_read16(kb + 2048 + k_rd0), k11 = lds_read16(kb + 2048 + k_rd1);
      f32x16 s0, s1;
      if (SAFE || (t == 0 && hb == 0)) {
        f32x16 z_ = {};
        s0 = MFMA32(k00, qf0, z_); s1 = MFMA32(k10, qf0, z_);
        s0 = MFMA32(k01, qf1, s0); s1 = MFMA32(k11, qf1, s1);
        const float tm = tile_rowmax(s0, s1);
        const bool first = (t == 0 && hb == 0);
        const float mn = first ? tm : __builtin_fmaxf(m, tm);
        if (SAFE) {
          const float f = first ? 0.f : __builtin_amdgcn_exp2f(m - mn);
          lsa *= f; lsb *= f;
          oacc *= f;
        }
        m = mn;
#pragma unroll
        for (int r = 0; r < 16; ++r) { negm[r] = -mn; s0[r] -= mn; s1[r] -= mn; }
      } else {
        s0 = MFMA32(k00, qf0, negm); s1 = MFMA32(k10, qf0, negm);
        s0 = MFMA32(k01, qf1, s0); s1 = MFMA32(k11, qf1, s1);
      }
#pragma unroll
      for (int r = 0; r < 16; r += 2) {
        s0[r] = AEXP(s0[r]); s0[r + 1] = AEXP(s0[r + 1]);
        s1[r] = AEXP(s1[r]); s1[r + 1] = AEXP(s1[r + 1]);
        lsa += (f32x2){s0[r], s0[r + 1]};
        lsb += (f32x2){s1[r], s1[r + 1]};
      }
      u32x4 p0, p1, p2, p3;
#pragma unroll
      for (int j = 0; j < 4; ++j) {
        p0[j] = pack2(s0[2 * j], s0[2 * j + 1]);
        p1[j] = pack2(s0[8 + 2 * j], s0[8 + 2 * j + 1]);
        p2[j] = pack2(s1[2 * j], s1[2 * j + 1]);
        p3[j] = pack2(s1[8 + 2 * j], s1[8 + 2 * j + 1]);
      }
      const char* vb = smem + cur + hb * 4096 + v_rd;
#define VFRAG(s) __builtin_shufflevector(tr_read(vb + (s) * 1024), tr_read(vb + (s) * 1024 + 512), 0, 1, 2, 3, 4, 5, 6, 7)
      oacc = MFMA32(VFRAG(0), __builtin_bit_cast(bf16x8, p0), oacc);
      oacc = MFMA32(VFRAG(1), __builtin_bit_cast(bf16x8, p1), oacc);
      oacc = MFMA32(VFRAG(2), __builtin_bit_cast(bf16x8, p2), oacc);
      oacc = MFMA32(VFRAG(3), __builtin_bit_cast(bf16x8, p3), oacc);
#undef VFRAG
    }
    WAIT_L0();
    cur += A2_ST; if (cur >= A2_NS * A2_ST) cur = 0;
  }
  lsa += lsb;
  l = lsa[0] + lsa[1];
  {
    auto rr = __builtin_amdgcn_permlane32_swap(__float_as_uint(l), __float_as_uint(l), false, false);
    l = __uint_as_float(rr[0]) + __uint_as_float(rr[1]);
  }
  WAIT_V(0);
}

__global__ __launch_bounds__(1024, 4) void attn2_kernel(const u16* __restrict__ qkv, u16* __restrict__ o,
                                                        const u16* pw0, const u16* pw1, const u16* pw2, const u16* pw3,
                                                        const u16* __restrict__ cvec, const u16* wmod, float* __restrict__ modp) {
  extern __shared__ __attribute__((aligned(16))) char smem[];
  STAMP_BEGIN();
  const int tid = threadIdx.x, lane = tid & 63;
  const int wid = __builtin_amdgcn_readfirstlane(tid >> 6);
  const int wq = wid & 7, wk = wid >> 3;
  const int x = blockIdx.x & 7, idx = blockIdx.x >> 3;
  const int hd = x * 4 + (idx >> 3), qb = idx & 7;
  const int r32 = lane & 31, hi = lane >> 5;
  const u16* Q = qkv + ((size_t)(0 * 32 + hd) * S_) * 32;
  const u16* Kp = qkv + ((size_t)(1 * 32 + hd) * S_) * 32;
  const u16* Vp = qkv + ((size_t)(2 * 32 + hd) * S_) * 32;
  const int q0 = qb * 256 + wq * 32;
  const bf16x8 qf0 = *(const bf16x8*)(Q + (size_t)(q0 + r32) * 32 + hi * 8);
  const bf16x8 qf1 = *(const bf16x8*)(Q + (size_t)(q0 + r32) * 32 + 16 + hi * 8);

  f32x16 oacc;
  float m, l;
  const int mcg = 32 + (blockIdx.x >> 2), mks = blockIdx.x & 3;
  const u16* wm = wmod + (size_t)(mks * 256) * 6144 + mcg * 64;
  unsigned cc = *(const unsigned*)(cvec + mks * 256 + 2 * (tid >> 3));
  asm volatile("" : "+v"(cc));
  attn_pass<false>(smem, Kp, Vp, qf0, qf1, wid, lane, oacc, m, l, pw0, pw1, pw2, pw3, wm, cc);
  float* xb = (float*)smem + wq * (20 * 64);
  const float* mred = (const float*)(smem + A2_RED);
  volatile LAS int* badf = (volatile LAS int*)(smem + A2_FLG);
  {
    float chk = l;
#pragma unroll
    for (int r = 0; r < 16; ++r) chk += __builtin_fabsf(oacc[r]);
    const int bad = __any(!(chk < 1.0e37f));
    if (lane == 0) badf[wid] = bad;
    __syncthreads();
    const int anybad = __any(badf[lane & 15] != 0);
    if (anybad) {
      __syncthreads();
      attn_pass<true>(smem, Kp, Vp, qf0, qf1, wid, lane, oacc, m, l, pw0, pw1, pw2, pw3, wm, cc);
      __syncthreads();
    }
  }
  xb[wk * 64 + lane] = m; xb[(2 + wk) * 64 + lane] = l;
#pragma unroll
  for (int r = 0; r < 8; ++r) xb[(4 + 8 * wk + r) * 64 + lane] = wk ? oacc[r] : oacc[8 + r];
  __syncthreads();
  {
    const float m2 = xb[(wk ^ 1) * 64 + lane], l2 = xb[(2 + (wk ^ 1)) * 64 + lane];
    const float mn = __builtin_fmaxf(m, m2);
    const float f1 = __builtin_amdgcn_exp2f((m - mn) * 0.5f), f2 = __builtin_amdgcn_exp2f((m2 - mn) * 0.5f);
    const float inv = 1.0f / ((l * f1) * f1 + (l2 * f2) * f2);
    u16* op = o + ((size_t)hd * S_ + q0 + r32) * 32 + 4 * hi + 16 * wk;
#pragma unroll
    for (int g = 0; g < 2; ++g) {
      float v[4];
#pragma unroll
      for (int e = 0; e < 4; ++e) {
        const float own = wk ? oacc[8 + 4 * g + e] : oacc[4 * g + e];
        v[e] = ((own * f1) * f1 + (xb[(4 + 8 * (wk ^ 1) + 4 * g + e) * 64 + lane] * f2) * f2) * inv;
      }
      u32x2 w;
      w[0] = pack2(v[0], v[1]);
      w[1] = pack2(v[2], v[3]);
      *(u32x2*)(op + 8 * g) = w;
    }
  }
  if (wid == 15) {
    float sacc = 0.f;
#pragma unroll
    for (int w = 0; w < 16; ++w) sacc += mred[w * 64 + lane];
    modp[mks * 6144 + mcg * 64 + lane] = sacc;
  }
  STAMP_END(3);
}

template <int MODE> static void launch_gemm(const GemmP& p, hipStream_t stream) {
  using C = GCfg<MODE>;
  constexpr int lds0_ = C::NS * ((C::AD ? 0 : C::BM) + C::BN) * 128, lds = lds0_ < 20480 ? 20480 : lds0_;
  hipFuncSetAttribute((const void*)gemm_kernel<MODE>, hipFuncAttributeMaxDynamicSharedMemorySize, lds);
  gemm_kernel<MODE><<<C::NTM * C::NTN, C::WM * C::WN * 64 * (C::LW ? 2 : 1), lds, stream>>>(p);
}

extern "C" void kernel_launch(void* const* d_in, const int* in_sizes, int n_in, void* d_out, int out_size,
                              void* d_ws, size_t ws_size, hipStream_t stream) {
  const u16* x = (const u16*)d_in[0];
  const u16* wqkv = (const u16*)d_in[1];
  const u16* wo = (const u16*)d_in[2];
  const u16* wg = (const u16*)d_in[4];
  const u16* wu = (const u16*)d_in[5];
  const u16* wd = (const u16*)d_in[6];
  const u16* c = (const u16*)d_in[7];
  const u16* wmod = (const u16*)d_in[8];
  u16* out = (u16*)d_out;

  char* ws = (char*)d_ws;
  float* mod = (float*)ws;
  u16* h = (u16*)(ws + (1 << 20));
  u16* qkv = (u16*)(ws + (8 << 20));
  u16* ao = (u16*)(ws + (24 << 20));
  u16* x1 = (u16*)(ws + (32 << 20));
  u16* hh = (u16*)(ws + (40 << 20));
  float* slab = (float*)(ws + (64 << 20));
  unsigned* flags = (unsigned*)(ws + (512 << 10));

  mod_kernel<<<256, 256, 0, stream>>>(c, wmod, mod, x, wqkv, flags);
  ln_kernel<<<512 + 192, 256, 0, stream>>>(x, mod, 0, h, wqkv, 1);
  { GemmP p{h, wqkv, nullptr, nullptr, nullptr, qkv, nullptr, nullptr, nullptr}; launch_gemm<0>(p, stream); }
  hipFuncSetAttribute((const void*)attn2_kernel, hipFuncAttributeMaxDynamicSharedMemorySize, A2_LDS);
  attn2_kernel<<<256, 1024, A2_LDS, stream>>>(qkv, ao, wo, wg, wu, wd, c, wmod, mod);
  { GemmP p{ao, wo, nullptr, x, mod + 2 * 1024, x1, slab, flags, h}; launch_gemm<1>(p, stream);
  }
  { GemmP p{h, wg, wu, nullptr, nullptr, hh, nullptr, nullptr, nullptr}; launch_gemm<2>(p, stream); }
  { GemmP p{hh, wd, nullptr, x1, mod + 5 * 1024, out, slab, flags, nullptr}; launch_gemm<4>(p, stream); }
}
```

```cpp
#define NO_LN2_FUSE
#include <hip/hip_runtime.h>
#include <hip/hip_bf16.h>
#include <stdint.h>

typedef unsigned short u16;
typedef short bf16x8 __attribute__((ext_vector_type(8)));
typedef short s16x4 __attribute__((ext_vector_type(4)));
typedef float f32x2 __attribute__((ext_vector_type(2)));
typedef float f32x4 __attribute__((ext_vector_type(4)));
typedef float f32x16 __attribute__((ext_vector_type(16)));
typedef unsigned u32x2 __attribute__((ext_vector_type(2)));
typedef unsigned u32x4 __attribute__((ext_vector_type(4)));
typedef __bf16 bf16x2_t __attribute__((ext_vector_type(2)));
#define LAS __attribute__((address_space(3)))
#define GAS __attribute__((address_space(1)))

constexpr int S_ = 2048, D_ = 1024, H_ = 32, DH_ = 32, F_ = 3584;

__device__ __forceinline__ float bf2f(u16 u) { return __uint_as_float((unsigned)u << 16); }
__device__ __forceinline__ unsigned pack2(float lo, float hi) {
  f32x2 v = {lo, hi};
  bf16x2_t b = __builtin_convertvector(v, bf16x2_t);
  return __builtin_bit_cast(unsigned, b);
}
__device__ __forceinline__ float rbf(float f) {
  __bf16 b = (__bf16)f;
  return __uint_as_float((unsigned)__builtin_bit_cast(u16, b) << 16);
}
__device__ __forceinline__ float lo_f(unsigned u) { return __uint_as_float(u << 16); }
__device__ __forceinline__ float hi_f(unsigned u) { return __uint_as_float(u & 0xffff0000u); }

#define STAMP_BEGIN()
#define STAMP_END(kid)
#define STAMP_MID(kid, i)
#define WAIT_V(n) asm volatile("s_waitcnt vmcnt(%0)" ::"n"(n) : "memory")
#define WAIT_L0() asm volatile("s_waitcnt lgkmcnt(0)" ::: "memory")
#define BAR() __builtin_amdgcn_s_barrier()

__device__ __forceinline__ void glds16(const void* sbase, unsigned voff, unsigned ldsaddr) {
  asm volatile("s_mov_b32 m0, %2\n\ts_nop 0\n\tglobal_load_lds_dwordx4 %0, %1" ::"v"(voff), "s"(sbase), "s"(ldsaddr) : "memory");
}
#define MFMA16(c, a, b) c = __builtin_amdgcn_mfma_f32_16x16x32_bf16(a, b, c, 0, 0, 0)
#define ST16(p, v) asm volatile("global_store_dwordx4 %0, %1, off sc0 sc1\n\ts_nop 1" ::"v"(p), "v"(v) : "memory")
#define SBAR() __builtin_amdgcn_sched_barrier(0)
#define SGB(mask, n) __builtin_amdgcn_sched_group_barrier(mask, n, 0)
template <int I> struct IC { static constexpr int value = I; };
template <int I, int N, class F> __device__ __forceinline__ void sfor(F&& f) {
  if constexpr (I < N) { f(IC<I>{}); sfor<I + 1, N>(f); }
}
__device__ __forceinline__ s16x4 tr_read(const char* p) {
  return __builtin_amdgcn_ds_read_tr16_b64_v4i16((LAS s16x4*)p);
}
__device__ __forceinline__ bf16x8 lds_read16(const char* p) { return *(const LAS bf16x8*)p; }

__device__ __forceinline__ void mod_block(const u16* __restrict__ c, const u16* __restrict__ wmod, float* __restrict__ modp,
                                          int cg, int ks, float* sh) {
  const int tid = threadIdx.x;
  const int cl = tid & 7, rg = tid >> 3;
  const int col0 = cg * 64 + cl * 8;
  u32x4 w[8];
#pragma unroll
  for (int i = 0; i < 8; ++i)
    w[i] = __builtin_nontemporal_load((const u32x4*)(wmod + (size_t)(ks * 256 + rg + i * 32) * 6144 + col0));
  {
    const float v = bf2f(c[ks * 256 + tid]);
    sh[tid] = rbf(v / (1.0f + __expf(-v)));
  }
  __syncthreads();
  float acc[8];
#pragma unroll
  for (int j = 0; j < 8; ++j) acc[j] = 0.f;
#pragma unroll
  for (int i = 0; i < 8; ++i) {
    const float s = sh[rg + i * 32];
#pragma unroll
    for (int j = 0; j < 4; ++j) {
      acc[2 * j] += s * lo_f(w[i][j]);
      acc[2 * j + 1] += s * hi_f(w[i][j]);
    }
  }
  float* red = sh + 256;
#pragma unroll
  for (int j = 0; j < 8; ++j) red[rg * 65 + cl * 8 + j] = acc[j];
  __syncthreads();
  if (tid < 64) {
    float s = 0.f;
#pragma unroll 8
    for (int r = 0; r < 32; ++r) s += red[r * 65 + tid];
    modp[ks * 6144 + cg * 64 + tid] = s;
  }
}
__device__ __forceinline__ void touch_block(const char* src) {
  u32x4 v[8];
#pragma unroll
  for (int i = 0; i < 8; ++i) v[i] = *(const u32x4*)(src + i * 4096 + threadIdx.x * 16);
#pragma unroll
  for (int i = 0; i < 8; ++i) asm volatile("" ::"v"(v[i]));
}
__device__ __forceinline__ f32x4 ld_mod4(const float* modp, int idx) {
  f32x4 a = *(const f32x4*)(modp + idx), b = *(const f32x4*)(modp + 6144 + idx);
  f32x4 c = *(const f32x4*)(modp + 2 * 6144 + idx), d = *(const f32x4*)(modp + 3 * 6144 + idx);
  f32x4 s = (a + b) + (c + d);
  return (f32x4){rbf(s[0]), rbf(s[1]), rbf(s[2]), rbf(s[3])};
}

__global__ __launch_bounds__(256) void mod_kernel(const u16* __restrict__ c, const u16* __restrict__ wmod,
                                                  float* __restrict__ modp, const u16* __restrict__ pf0,
                                                  const u16* __restrict__ pf1, unsigned* flags) {
  __shared__ float sh[256 + 32 * 65];
  STAMP_BEGIN();
  const int b = blockIdx.x;
  if (threadIdx.x == 0 && b < 256) flags[b * 32] = 0u;
  if (b < 128) mod_block(c, wmod, modp, b >> 2, b & 3, sh);
  else if (b < 256) touch_block((const char*)pf0 + (size_t)(b - 128) * 32768);
  else touch_block((const char*)pf1 + (size_t)(b - 256) * 32768);
  STAMP_END(0);
}

__global__ __launch_bounds__(256) void ln_kernel(const u16* __restrict__ x, const float* modp, int moff, u16* __restrict__ h,
                                                 const u16* __restrict__ pf, int kid) {
  __shared__ float sh[2048];
  STAMP_BEGIN();
  const int tid = threadIdx.x;
  if (blockIdx.x >= 512) { touch_block((const char*)pf + (size_t)(blockIdx.x - 512) * 32768); return; }
  const int lane = tid & 63, wid = tid >> 6;
  const int row = blockIdx.x * 4 + wid;
  const u16* xr = x + (size_t)row * D_;
  const u32x4 v0 = *(const u32x4*)(xr + lane * 8);
  const u32x4 v1 = *(const u32x4*)(xr + 512 + lane * 8);
  {
    const f32x4 s0 = ld_mod4(modp, moff + tid * 4);
    const f32x4 s1 = ld_mod4(modp, moff + 1024 + tid * 4);
    *(f32x4*)(sh + tid * 4) = s0;
    *(f32x4*)(sh + 1024 + tid * 4) = (f32x4){rbf(1.0f + s1[0]), rbf(1.0f + s1[1]), rbf(1.0f + s1[2]), rbf(1.0f + s1[3])};
  }
  float f[16];
#pragma unroll
  for (int j = 0; j < 4; ++j) {
    f[2 * j] = lo_f(v0[j]); f[2 * j + 1] = hi_f(v0[j]);
    f[8 + 2 * j] = lo_f(v1[j]); f[8 + 2 * j + 1] = hi_f(v1[j]);
  }
  float s = 0.f;
#pragma unroll
  for (int j = 0; j < 16; ++j) s += f[j];
#pragma unroll
  for (int o = 32; o >= 1; o >>= 1) s += __shfl_xor(s, o);
  const float mean = s * (1.0f / D_);
  float q = 0.f;
#pragma unroll
  for (int j = 0; j < 16; ++j) { float d = f[j] - mean; q += d * d; }
#pragma unroll
  for (int o = 32; o >= 1; o >>= 1) q += __shfl_xor(q, o);
  const float mb = rbf(mean);
  const float vb = rbf(q * (1.0f / D_));
  const float sd = rbf(sqrtf(rbf(vb + 1e-5f)));
  const float inv = 1.0f / sd;
  __syncthreads();
  float o[16];
#pragma unroll
  for (int half = 0; half < 2; ++half) {
    const int c0 = half * 512 + lane * 8;
    const f32x4 sa0 = *(const f32x4*)(sh + c0), sa1 = *(const f32x4*)(sh + c0 + 4);
    const f32x4 ca0 = *(const f32x4*)(sh + 1024 + c0), ca1 = *(const f32x4*)(sh + 1024 + c0 + 4);
#pragma unroll
    for (int j = 0; j < 8; ++j) {
      const float shf = j < 4 ? sa0[j & 3] : sa1[j & 3];
      const float sc = j < 4 ? ca0[j & 3] : ca1[j & 3];
      float t = rbf(f[half * 8 + j] - mb);
      t = rbf(t * inv);
      t = rbf(t * sc);
      o[half * 8 + j] = t + shf;
    }
  }
  u16* hr = h + (size_t)row * D_;
  u32x4 w0, w1;
#pragma unroll
  for (int j = 0; j < 4; ++j) {
    w0[j] = pack2(o[2 * j], o[2 * j + 1]);
    w1[j] = pack2(o[8 + 2 * j], o[8 + 2 * j + 1]);
  }
  ST16(hr + lane * 8, w0);
  ST16(hr + 512 + lane * 8, w1);
  STAMP_END(kid);
}

struct GemmP {
  const u16* A; const u16* B0; const u16* B1; const u16* res; const float* gate; u16* out; float* slab; unsigned* flags; u16* out2;
};

#define NS0 3
#define NS1 3
#define NS3 3
#define LW0 1
#define LW1 1
#define LW3 1
#define AD1 0
#define AD4 0
template <int MODE> struct GCfg;
template <> struct GCfg<0> { static constexpr int BM = 128, BN = 192, WM = 2, WN = 2, NS = NS0, K = 1024, LDA = 1024, LDB = 3072, NTM = 16, NTN = 16, RM = 8, RN = 4, LW = LW0, AD = 0; };
template <> struct GCfg<1> { static constexpr int BM = 128, BN = 64, WM = AD1 ? 4 : 2, WN = AD1 ? 1 : 2, NS = NS1, K = 1024, LDA = 1024, LDB = 1024, NTM = 16, NTN = 16, RM = 4, RN = 8, LW = LW1, AD = AD1; };
template <> struct GCfg<2> { static constexpr int BM = 256, BN = 256, WM = 4, WN = 2, NS = 2, K = 1024, LDA = 1024, LDB = 3584, NTM = 8, NTN = 28, RM = 4, RN = 7, LW = 0, AD = 0; };
template <> struct GCfg<3> { static constexpr int BM = 128, BN = 128, WM = 2, WN = 2, NS = NS3, K = 1792, LDA = 3584, LDB = 1024, NTM = 16, NTN = 16, RM = 4, RN = 8, LW = LW3, AD = 0; };

template <> struct GCfg<4> { static constexpr int BM = 128, BN = 64, WM = AD4 ? 4 : 2, WN = AD4 ? 1 : 2, NS = NS3, K = 3584, LDA = 3584, LDB = 1024, NTM = 16, NTN = 16, RM = 4, RN = 8, LW = LW3, AD = AD4; };

template <int MODE>
__global__ __launch_bounds__(GCfg<MODE>::WM* GCfg<MODE>::WN * 64 * (GCfg<MODE>::LW ? 2 : 1), 2) void gemm_kernel(GemmP p) {
  using C = GCfg<MODE>;
  constexpr int BM = C::BM, BN = C::BN, WM = C::WM, WN = C::WN, NS = C::NS, NW = WM * WN;
  constexpr int TM = BM / WM, TN = BN / WN, MI = TM / 16, NI = TN / 16;
  constexpr int AD = C::AD;
  constexpr int A_BYTES = AD ? 0 : BM * 128, B_BYTES = BN * 128, ST_BYTES = A_BYTES + B_BYTES;
  constexpr int GA = AD ? 0 : BM / 8 / NW, GB = BN / 8 / NW, G = GA + GB;
  constexpr int NT = C::K / 64;
  static_assert(BM % (8 * NW) == 0 && BN % (8 * NW) == 0, "stage split");
  static_assert(NT >= NS + 1, "K too small");
  extern __shared__ __attribute__((aligned(16))) char smem[];
  STAMP_BEGIN();

  const int tid = threadIdx.x, lane = tid & 63;
  constexpr bool LW = C::LW != 0;
  const int wid_all = __builtin_amdgcn_readfirstlane(tid >> 6);
  const int wid = wid_all % NW;
  const int wm = wid / WN, wn = wid % WN;
  int tm, tn;
  {
    const int x = blockIdx.x & 7, idx = blockIdx.x >> 3;
    constexpr int NRM = C::NTM / C::RM;
    const int rm = x % NRM, rn = x / NRM;
    tm = rm * C::RM + idx % C::RM;
    tn = rn * C::RN + idx / C::RM;
  }
  int kh = 0;
  if constexpr (MODE == 3) { kh = tn & 1; tn >>= 1; }
  const int m0 = tm * BM;

  const int par = wid & 1;
  const int a_ch = (lane & 7) ^ ((4 * par + (lane >> 4)) & 7);
  const unsigned a_voff = (MODE == 1) ? (unsigned)((((a_ch >> 2) * S_ + (lane >> 3)) * 32 + (a_ch & 3) * 8) * 2)
                                      : (unsigned)(((lane >> 3) * C::LDA + 8 * a_ch) * 2);
  const unsigned b_voff = (unsigned)(((lane >> 3) * C::LDB + 8 * ((lane & 7) ^ ((((lane >> 4) & 1) | (par << 1)) << 1))) * 2);
  const char* Abase = (MODE == 1) ? (const char*)(p.A + (size_t)m0 * 32) : (const char*)(p.A + (size_t)m0 * C::LDA + kh * C::K);
  const unsigned lds0 = (unsigned)(size_t)(LAS char*)smem;

  auto issue1 = [&](auto gi, int t, int bufoff) {
    constexpr int g = decltype(gi)::value;
    if constexpr (g < GA) {
      const int i = wid + NW * g;
      const char* gp = (MODE == 1) ? Abase + ((size_t)(8 * i) * 32 + (size_t)t * 2 * S_ * 32) * 2
                                   : Abase + ((size_t)(8 * i) * C::LDA + (size_t)t * 64) * 2;
      glds16(gp, a_voff, lds0 + bufoff + i * 1024);
    } else {
      const int i = wid + NW * (g - GA);
      const int pn = i >> 3, kr = 8 * (i & 7);
      const u16* bp;
      if constexpr (MODE == 2) bp = (pn < 2 ? p.B0 : p.B1) + tn * 128 + (pn & 1) * 64;
      else bp = p.B0 + (size_t)kh * C::K * C::LDB + tn * BN + pn * 64;
      const char* gp = (const char*)(bp + ((size_t)t * 64 + kr) * C::LDB);
      glds16(gp, b_voff, lds0 + bufoff + A_BYTES + i * 1024);
    }
  };

  if constexpr (C::LW == 1) {
    if (wid_all >= NW) {
      sfor<0, NS>([&](auto sc_) { sfor<0, G>([&](auto gc) { issue1(gc, decltype(sc_)::value, decltype(sc_)::value * ST_BYTES); }); });
      WAIT_V((NS - 1) * G);
      BAR();
      int lb = 0;
      for (int t = 0; t < NT - NS; ++t) {
        WAIT_V((NS - 2) * G);
        BAR();
        sfor<0, G>([&](auto gc) { issue1(gc, t + NS, lb); });
        lb = (lb + ST_BYTES == NS * ST_BYTES) ? 0 : lb + ST_BYTES;
      }
      sfor<0, NS - 1>([&](auto rc) { WAIT_V((NS - 2 - decltype(rc)::value) * G); BAR(); });
      return;
    }
  }
  if constexpr (C::LW == 2) {
    if (wid_all >= NW) {
      static_assert(NS == 3 && (NT & 1) == 0, "reg loader");
      u32x4 R0[G], R1[G];
      auto ld = [&](u32x4* R, int t) {
        sfor<0, G>([&](auto gi) {
          constexpr int g = decltype(gi)::value;
          if constexpr (g < GA) {
            const int i = wid + NW * g;
            R[g] = *(const u32x4*)(Abase + ((size_t)(8 * i) * C::LDA + (size_t)t * 64) * 2 + a_voff);
          } else {
            const int i = wid + NW * (g - GA);
            const int pn = i >> 3, kr = 8 * (i & 7);
            const u16* bp = p.B0 + (size_t)kh * C::K * C::LDB + tn * BN + pn * 64;
            R[g] = *(const u32x4*)((const char*)(bp + ((size_t)t * 64 + kr) * C::LDB) + b_voff);
          }
        });
      };
      auto st = [&](const u32x4* R, int bufoff) {
        sfor<0, G>([&](auto gi) {
          constexpr int g = decltype(gi)::value;
          const int off = g < GA ? (wid + NW * g) * 1024 : A_BYTES + (wid + NW * (g - GA)) * 1024;
          *(LAS u32x4*)(smem + bufoff + off + lane * 16) = R[g];
        });
      };
      ld(R0, 0); ld(R1, 1);
      st(R0, 0); ld(R0, 2);
      WAIT_L0(); BAR();
      st(R1, ST_BYTES); ld(R1, 3);
      st(R0, 2 * ST_BYTES); ld(R0, 4);
      int lb = 0;
      for (int t = 0; t < NT - 4; t += 2) {
        WAIT_L0(); BAR();
        st(R1, lb); if (t + 5 < NT) ld(R1, t + 5);
        lb = (lb + ST_BYTES == NS * ST_BYTES) ? 0 : lb + ST_BYTES;
        WAIT_L0(); BAR();
        st(R0, lb); if (t + 6 < NT) ld(R0, t + 6);
        lb = (lb + ST_BYTES == NS * ST_BYTES) ? 0 : lb + ST_BYTES;
      }
      WAIT_L0(); BAR();
      st(R1, lb);
      WAIT_L0(); BAR();
      BAR();
      return;
    }
  }

  const int fr = lane & 15, fq = lane >> 4;
  u32x4 rv[4];
  f32x4 gv[NI];
  if constexpr (MODE == 1 || MODE == 3 || MODE == 4) {
#pragma unroll
    for (int i = 0; i < 4; ++i) {
      if constexpr (MODE != 3) rv[i] = *(const u32x4*)(p.res + (size_t)(m0 + i * 32 + (tid >> 3)) * D_ + tn * BN + (tid & 7) * 8);
      else rv[i] = *(const u32x4*)(p.res + (size_t)(m0 + kh * 64 + i * 16 + (tid >> 4)) * D_ + tn * BN + (tid & 15) * 8);
    }
#pragma unroll
    for (int ni = 0; ni < NI; ++ni) { gv[ni] = ld_mod4(p.gate, tn * BN + wn * TN + ni * 16 + 4 * fq); asm volatile("" : "+v"(gv[ni])); }
  }
  const int a_rd0 = (wm * TM + fr) * 128 + ((fq ^ (fr >> 1)) << 4);
  const int a_rd1 = a_rd0 ^ 64;
  int b_rd[(MODE == 2) ? 4 : NI];
  {
    const int q = (lane & 15) >> 2, pp = lane & 3;
    const int gl = ((q >> 1) & 1) | ((fq & 1) << 1);
#pragma unroll
    for (int ni = 0; ni < ((MODE == 2) ? 4 : NI); ++ni) {
      int pn, un;
      if constexpr (MODE == 2) { pn = wn; un = ni & 3; }
      else { const int j = wn * NI + ni; pn = j >> 2; un = j & 3; }
      b_rd[ni] = A_BYTES + pn * 8192 + (8 * fq + q) * 128 + ((un ^ gl) << 5) + pp * 8;
    }
  }

  f32x4 acc[MI][NI];
#pragma unroll
  for (int mi = 0; mi < MI; ++mi)
#pragma unroll
    for (int ni = 0; ni < NI; ++ni) acc[mi][ni] = (f32x4){0.f, 0.f, 0.f, 0.f};

  constexpr int NSP = (MODE == 2) ? 2 : 1;
  constexpr int NH = NI / NSP, NM = MI * NH, NPH = 2 * NSP;
  constexpr int GW = (NS >= 3) ? NPH : NPH / 2;
  constexpr int GPP = (G + GW - 1) / GW;
  bf16x8 a0[MI], a1[MI], b0[NH], b1[NH];
  s16x4 blo[NH];

  auto rd_b = [&](auto ri, bf16x8* BF, const char* sb, int ks, int hf) {
    constexpr int r = decltype(ri)::value, ni = r >> 1;
    const int bo = (MODE == 2) ? b_rd[ni] + hf * 16384 : b_rd[ni];
    if constexpr ((r & 1) == 0) blo[ni] = tr_read(sb + bo + ks * 4096);
    else BF[ni] = __builtin_shufflevector(blo[ni], tr_read(sb + bo + ks * 4096 + 512), 0, 1, 2, 3, 4, 5, 6, 7);
  };
  auto rd_a = [&](auto ri, bf16x8* AF, const char* sb, int ks) {
    constexpr int mi = decltype(ri)::value;
    AF[mi] = lds_read16(sb + (ks ? a_rd1 : a_rd0) + mi * 2048);
  };
  auto phase = [&](auto hfc, auto rdm, auto g0c, auto g1c, bf16x8* AF, bf16x8* BF, bf16x8* AN_, bf16x8* BN_,
                   const char* rsb, int rks, int rhf, int gt, int gbuf) {
    constexpr int hf = decltype(hfc)::value, RDM = decltype(rdm)::value;
    constexpr int g0 = decltype(g0c)::value, g1 = decltype(g1c)::value, NG = g1 - g0;
    constexpr int NR = RDM == 0 ? 0 : (RDM == 1 ? 2 * NH : 2 * NH + MI);
    sfor<0, NM>([&](auto ic) {
      constexpr int i = decltype(ic)::value, mi = i / NH, ni = i % NH;
      MFMA16(acc[mi][hf * NH + ni], BF[ni], AF[mi]);
      sfor<0, NR>([&](auto rc) {
        constexpr int r = decltype(rc)::value;
        if constexpr ((r * (NM - NM / 4)) / (NR > 0 ? NR : 1) == i) {
          if constexpr (r < 2 * NH) rd_b(rc, BN_, rsb, rks, rhf);
          else rd_a(IC<r - 2 * NH>{}, AN_, rsb, rks);
        }
      });
      sfor<0, NG>([&](auto gc) {
        constexpr int g = decltype(gc)::value;
        if constexpr ((g * NM) / (NG > 0 ? NG : 1) == i) issue1(IC<g0 + g>{}, gt, gbuf);
      });
      SBAR();
    });
  };
  constexpr auto gmin = [](int a, int b) { return a < b ? a : b; };
  int cur = 0, prv = 0;
  auto ktile = [&](auto vmc, auto restc, auto firstc, int t) {
    constexpr int VM = decltype(vmc)::value;
    constexpr bool REST = decltype(restc)::value && !LW, FIRST = decltype(firstc)::value && !LW;
    const int nxt = (cur + ST_BYTES == NS * ST_BYTES) ? 0 : cur + ST_BYTES;
    const char* sc = smem + cur;
    const char* sn = smem + nxt;
    constexpr int R1 = REST ? gmin(G, GPP) : 0, R2 = REST ? gmin(G, 2 * GPP) : 0, R3 = REST ? gmin(G, 3 * GPP) : 0, R4 = REST ? gmin(G, 4 * GPP) : 0;
    constexpr int F1 = FIRST ? gmin(G, GPP) : 0;
    if constexpr (NSP == 1) {
      phase(IC<0>{}, IC<2>{}, IC<R1>{}, IC<R2>{}, a0, b0, a1, b1, sc, 1, 0, t - 1 + NS, prv);
      if constexpr (VM >= 0) {
        if constexpr (!LW) WAIT_V(VM);
        WAIT_L0(); BAR();
        phase(IC<0>{}, IC<2>{}, IC<0>{}, IC<F1>{}, a1, b1, a0, b0, sn, 0, 0, t + NS, cur);
      } else {
        phase(IC<0>{}, IC<0>{}, IC<0>{}, IC<0>{}, a1, b1, a0, b0, sn, 0, 0, 0, 0);
      }
    } else {
      phase(IC<0>{}, IC<1>{}, IC<R1>{}, IC<R2>{}, a0, b0, a1, b1, sc, 0, 1, t - 1 + NS, prv);
      phase(IC<1>{}, IC<2>{}, IC<R2>{}, IC<R3>{}, a0, b1, a1, b0, sc, 1, 0, t - 1 + NS, prv);
      phase(IC<0>{}, IC<1>{}, IC<R3>{}, IC<R4>{}, a1, b0, a1, b1, sc, 1, 1, t - 1 + NS, prv);
      if constexpr (VM >= 0) {
        WAIT_V(VM); WAIT_L0(); BAR();
        phase(IC<1>{}, IC<2>{}, IC<0>{}, IC<F1>{}, a1, b1, a0, b0, sn, 0, 0, t + NS, cur);
      } else {
        phase(IC<1>{}, IC<0>{}, IC<0>{}, IC<0>{}, a1, b1, a0, b0, sn, 0, 0, 0, 0);
      }
    }
    prv = cur; cur = nxt;
  };

  if constexpr (AD > 0) {
    static_assert(LW && NSP == 1 && NT % AD == 0, "A-direct path");
    bf16x8 ar[AD][2][MI];
    const char* Aw = (MODE == 1) ? Abase + ((size_t)(wm * TM + fr) * 32 + fq * 8) * 2
                                 : Abase + ((size_t)(wm * TM + fr) * C::LDA + fq * 8) * 2;
    auto lda = [&](auto dc, int t) {
      constexpr int d = decltype(dc)::value;
#pragma unroll
      for (int ks = 0; ks < 2; ++ks)
#pragma unroll
        for (int mi = 0; mi < MI; ++mi) {
          if constexpr (MODE == 1) ar[d][ks][mi] = *(const bf16x8*)(Aw + ((size_t)(2 * t + ks) * S_ + mi * 16) * 64);
          else ar[d][ks][mi] = *(const bf16x8*)(Aw + (size_t)mi * 16 * C::LDA * 2 + t * 128 + ks * 64);
        }
    };
    sfor<0, AD>([&](auto dc) { lda(dc, decltype(dc)::value); });
    BAR();
    STAMP_MID(MODE == 0 ? 2 : MODE == 1 ? 4 : MODE == 2 ? 6 : 7, 0);
    sfor<0, 2 * NH>([&](auto rc) { rd_b(rc, b0, smem, 0, 0); });
    auto ktile_ad = [&](auto dc, auto ldc, auto lastc, int t) {
      constexpr int d = decltype(dc)::value;
      const int nxt = (cur + ST_BYTES == NS * ST_BYTES) ? 0 : cur + ST_BYTES;
      phase(IC<0>{}, IC<1>{}, IC<0>{}, IC<0>{}, ar[d][0], b0, a1, b1, smem + cur, 1, 0, 0, 0);
      if constexpr (!decltype(lastc)::value) {
        WAIT_L0(); BAR();
        phase(IC<0>{}, IC<1>{}, IC<0>{}, IC<0>{}, ar[d][1], b1, a0, b0, smem + nxt, 0, 0, 0, 0);
      } else {
        phase(IC<0>{}, IC<0>{}, IC<0>{}, IC<0>{}, ar[d][1], b1, a0, b0, smem + nxt, 0, 0, 0, 0);
      }
      if constexpr (decltype(ldc)::value) { lda(dc, t + AD); SBAR(); }
      cur = nxt;
    };
    for (int t = 0; t < NT - AD; t += AD) sfor<0, AD>([&](auto dc) { ktile_ad(dc, IC<1>{}, IC<0>{}, t + decltype(dc)::value); });
    sfor<0, AD>([&](auto dc) { ktile_ad(dc, IC<0>{}, IC<(decltype(dc)::value == AD - 1)>{}, 0); });
  } else {
  if constexpr (!LW) {
    sfor<0, NS>([&](auto sc_) { sfor<0, G>([&](auto gc) { issue1(gc, decltype(sc_)::value, decltype(sc_)::value * ST_BYTES); }); });
    WAIT_V((NS - 1) * G);
  }
  BAR();
  STAMP_MID(MODE == 0 ? 2 : MODE == 1 ? 4 : MODE == 2 ? 6 : 7, 0);
  sfor<0, 2 * NH>([&](auto rc) { rd_b(rc, b0, smem, 0, 0); });
  sfor<0, MI>([&](auto rc) { rd_a(rc, a0, smem, 0); });
  using TT = IC<1>; using FF = IC<0>;
  ktile(IC<(NS - 2) * G>{}, FF{}, TT{}, 0);
  for (int t = 1; t < NT - NS; ++t) ktile(IC<(NS - 2) * G>{}, TT{}, TT{}, t);
  ktile(IC<(NS - 2) * G>{}, TT{}, FF{}, NT - NS);
  sfor<0, NS - 2>([&](auto rc) { ktile(IC<(NS - 3 - decltype(rc)::value) * G>{}, FF{}, FF{}, 0); });
  ktile(IC<-1>{}, FF{}, FF{}, 0);
  }

  STAMP_MID(MODE == 0 ? 2 : MODE == 1 ? 4 : MODE == 2 ? 6 : 7, 1);
  if constexpr (MODE == 0) {
    const float qs = 0.17677669529663687f * 1.4426950408889634f;
    BAR();
    char* st = smem + wid * (64 * 208);
#pragma unroll
    for (int ni = 0; ni < NI; ++ni) {
      const int n = tn * BN + wn * TN + ni * 16;
      const float sc = n < 1024 ? qs : 1.0f;
#pragma unroll
      for (int mi = 0; mi < MI; ++mi) {
        u32x2 w;
        w[0] = pack2(acc[mi][ni][0] * sc, acc[mi][ni][1] * sc);
        w[1] = pack2(acc[mi][ni][2] * sc, acc[mi][ni][3] * sc);
        *(LAS u32x2*)(st + (mi * 16 + fr) * 208 + ni * 32 + fq * 8) = w;
      }
    }
#pragma unroll
    for (int j = 0; j < 3; ++j) {
      const int hh = (tn * BN + wn * TN + j * 32) >> 5;
      u16* ob = p.out + ((size_t)hh * S_ + m0 + wm * TM) * 32 + lane * 8;
#pragma unroll
      for (int i = 0; i < 4; ++i) {
        const u32x4 v = *(const LAS u32x4*)(st + (i * 16 + (lane >> 2)) * 208 + j * 64 + (lane & 3) * 16);
        ST16(ob + i * 512, v);
      }
    }
  } else if constexpr (MODE == 3) {
    const int tile = tm * 8 + tn;
    if (wm != kh) {
      float* dst = p.slab + ((size_t)(tile * 2 + kh) * 2 + wn) * 4096 + lane * 4;
#pragma unroll
      for (int mi = 0; mi < MI; ++mi)
#pragma unroll
        for (int ni = 0; ni < NI; ++ni) ST16(dst + (mi * NI + ni) * 256, __builtin_bit_cast(u32x4, acc[mi][ni]));
      WAIT_V(0);
      if (lane == 0) __hip_atomic_fetch_add(p.flags + (tile * 2 + kh) * 32, 1u, __ATOMIC_RELAXED, __HIP_MEMORY_SCOPE_AGENT);
      BAR();
    } else {
      const unsigned* fl = p.flags + (tile * 2 + (kh ^ 1)) * 32;
      for (int spin = 0; spin < (1 << 22); ++spin) {
        if (__builtin_amdgcn_readfirstlane(__hip_atomic_load(fl, __ATOMIC_RELAXED, __HIP_MEMORY_SCOPE_AGENT)) >= 2u) break;
        __builtin_amdgcn_s_sleep(1);
      }
      const float* src = p.slab + ((size_t)(tile * 2 + (kh ^ 1)) * 2 + wn) * 4096 + lane * 4;
      f32x4 pv[MI][NI];
#pragma unroll
      for (int mi = 0; mi < MI; ++mi)
#pragma unroll
        for (int ni = 0; ni < NI; ++ni)
          asm volatile("global_load_dwordx4 %0, %1, off sc0 sc1" : "=v"(pv[mi][ni]) : "v"(src + (mi * NI + ni) * 256) : "memory");
      BAR();
      asm volatile("s_waitcnt vmcnt(0)" ::: "memory");
      SBAR();
#pragma unroll
      for (int ni = 0; ni < NI; ++ni)
#pragma unroll
        for (int mi = 0; mi < MI; ++mi) {
          asm volatile("" : "+v"(pv[mi][ni]));
          const f32x4 a = acc[mi][ni] + pv[mi][ni];
          u32x2 w;
          w[0] = pack2(gv[ni][0] * rbf(a[0]), gv[ni][1] * rbf(a[1]));
          w[1] = pack2(gv[ni][2] * rbf(a[2]), gv[ni][3] * rbf(a[3]));
          *(LAS u32x2*)(smem + (mi * 16 + fr) * 272 + wn * 128 + ni * 32 + fq * 8) = w;
        }
    }
    WAIT_L0();
    BAR();
#pragma unroll
    for (int i = 0; i < 4; ++i) {
      const u32x4 v = *(const LAS u32x4*)(smem + (i * 16 + (tid >> 4)) * 272 + (tid & 15) * 16);
      u32x4 w;
#pragma unroll
      for (int j = 0; j < 4; ++j) w[j] = pack2(lo_f(rv[i][j]) + lo_f(v[j]), hi_f(rv[i][j]) + hi_f(v[j]));
      ST16(p.out + (size_t)(m0 + kh * 64 + i * 16 + (tid >> 4)) * D_ + tn * BN + (tid & 15) * 8, w);
    }
  } else if constexpr (MODE == 1 || MODE == 4) {
    BAR();
#pragma unroll
    for (int ni = 0; ni < NI; ++ni) {
#pragma unroll
      for (int mi = 0; mi < MI; ++mi) {
        u32x2 w;
        w[0] = pack2(gv[ni][0] * rbf(acc[mi][ni][0]), gv[ni][1] * rbf(acc[mi][ni][1]));
        w[1] = pack2(gv[ni][2] * rbf(acc[mi][ni][2]), gv[ni][3] * rbf(acc[mi][ni][3]));
        *(LAS u32x2*)(smem + (wm * TM + mi * 16 + fr) * 144 + wn * 64 + ni * 32 + fq * 8) = w;
      }
    }
    WAIT_L0();
    BAR();
    u32x4 xw[4];
#pragma unroll
    for (int i = 0; i < 4; ++i) {
      const u32x4 v = *(const LAS u32x4*)(smem + (i * 32 + (tid >> 3)) * 144 + (tid & 7) * 16);
#pragma unroll
      for (int j = 0; j < 4; ++j) xw[i][j] = pack2(lo_f(rv[i][j]) + lo_f(v[j]), hi_f(rv[i][j]) + hi_f(v[j]));
      ST16(p.out + (size_t)(m0 + i * 32 + (tid >> 3)) * D_ + tn * BN + (tid & 7) * 8, xw[i]);
    }
  } else {
    BAR();
    char* st = smem + wid * (64 * 144);
#pragma unroll
    for (int ni = 0; ni < 4; ++ni)
#pragma unroll
      for (int mi = 0; mi < MI; ++mi) {
        float o[4];
#pragma unroll
        for (int r = 0; r < 4; ++r) o[r] = rbf(acc[mi][ni][r]) * rbf(acc[mi][ni + 4][r]);
        u32x2 w;
        w[0] = pack2(o[0], o[1]);
        w[1] = pack2(o[2], o[3]);
        *(LAS u32x2*)(st + (mi * 16 + fr) * 144 + ni * 32 + fq * 8) = w;
      }
    u16* ob = p.out + (size_t)(m0 + wm * TM + (lane >> 3)) * F_ + tn * 128 + wn * 64 + (lane & 7) * 8;
#pragma unroll
    for (int i = 0; i < 8; ++i) {
      const u32x4 v = *(const LAS u32x4*)(st + (i * 8 + (lane >> 3)) * 144 + (lane & 7) * 16);
      ST16(ob + (size_t)i * 8 * F_, v);
    }
  }
  STAMP_END(MODE == 0 ? 2 : MODE == 1 ? 4 : MODE == 2 ? 6 : 7);
}

#define AT_NSTAGE 6
constexpr int AT_KT = 128, AT_NS = AT_NSTAGE, AT_ST = 16384;
#define MFMA32(a, b, c) __builtin_amdgcn_mfma_f32_32x32x16_bf16(a, b, c, 0, 0, 0)
#define AEXP(x) __builtin_amdgcn_exp2f(x)
#define MX3(a, b, c) __builtin_fmaxf(__builtin_fmaxf((a), (b)), (c))

__device__ __forceinline__ float tile_rowmax(const f32x16& s0, const f32x16& s1) {
  float a = MX3(s0[0], s0[1], s1[0]), b = MX3(s0[2], s0[3], s1[1]);
  a = MX3(a, s1[2], s1[3]);
#pragma unroll
  for (int r = 4; r < 16; r += 4) {
    a = MX3(a, s0[r], s0[r + 1]); b = MX3(b, s0[r + 2], s0[r + 3]);
    a = MX3(a, s1[r], s1[r + 1]); b = MX3(b, s1[r + 2], s1[r + 3]);
  }
  float mx = __builtin_fmaxf(a, b);
  auto rr = __builtin_amdgcn_permlane32_swap(__float_as_uint(mx), __float_as_uint(mx), false, false);
  return __builtin_fmaxf(__uint_as_float(rr[0]), __uint_as_float(rr[1]));
}

__global__ __launch_bounds__(512, 2) void attn_kernel(const u16* __restrict__ qkv, u16* __restrict__ o) {
  extern __shared__ __attribute__((aligned(16))) char smem[];
  STAMP_BEGIN();
  const int tid = threadIdx.x, lane = tid & 63;
  const int wid = __builtin_amdgcn_readfirstlane(tid >> 6);
  const int x = blockIdx.x & 7, idx = blockIdx.x >> 3;
  const int hd = x * 4 + (idx >> 3), qb = idx & 7;
  const int r32 = lane & 31, hi = lane >> 5;
  const u16* Q = qkv + ((size_t)(0 * 32 + hd) * S_) * 32;
  const u16* Kp = qkv + ((size_t)(1 * 32 + hd) * S_) * 32;
  const u16* Vp = qkv + ((size_t)(2 * 32 + hd) * S_) * 32;
  const int q0 = qb * 256 + wid * 32;

  const unsigned k_voff = (unsigned)(((lane >> 2) * 32 + 8 * ((lane & 3) ^ ((lane >> 4) & 3))) * 2);
  const unsigned v_voff = (unsigned)(lane * 16);
  const unsigned lds0 = (unsigned)(size_t)(LAS char*)smem;
  auto issue = [&](int t, int bufoff) {
    const char* gk = (const char*)(Kp + ((size_t)t * AT_KT + wid * 16) * 32);
    const char* gv = (const char*)(Vp + ((size_t)t * AT_KT + wid * 16) * 32);
    glds16(gk, k_voff, lds0 + bufoff + wid * 1024);
    glds16(gv, v_voff, lds0 + bufoff + 8192 + wid * 1024);
  };
#pragma unroll
  for (int s = 0; s < AT_NS - 1; ++s) issue(s, s * AT_ST);
  const bf16x8 qf0 = *(const bf16x8*)(Q + (size_t)(q0 + r32) * 32 + hi * 8);
  const bf16x8 qf1 = *(const bf16x8*)(Q + (size_t)(q0 + r32) * 32 + 16 + hi * 8);

  const int k_rd0 = r32 * 64 + ((hi ^ ((r32 >> 2) & 3)) << 4);
  const int k_rd1 = k_rd0 ^ 32;
  const int v_rd = 8192 + (4 * hi + ((lane & 15) >> 2)) * 64 + ((lane >> 4) & 1) * 32 + (lane & 3) * 8;

  f32x16 oacc = {};
  f32x16 negm;
  float m, l = 0.f;
  f32x16 sa0, sa1, sb0, sb1;
  u32x4 pp0 = {}, pp1 = {}, pp2 = {}, pp3 = {};
  bf16x8 vf0, vf1, vf2, vf3;
#define AT_BIG 1.0e30f
  constexpr float BIG = AT_BIG;
  constexpr int NT = S_ / AT_KT;

#define SCORES(D0, D1, kb, CIN)                                                         \
  do {                                                                                  \
    bf16x8 k00 = lds_read16((kb) + k_rd0), k01 = lds_read16((kb) + k_rd1);              \
    bf16x8 k10 = lds_read16((kb) + 2048 + k_rd0), k11 = lds_read16((kb) + 2048 + k_rd1);\
    D0 = MFMA32(k00, qf0, CIN); D1 = MFMA32(k10, qf0, CIN);                             \
    D0 = MFMA32(k01, qf1, D0);  D1 = MFMA32(k11, qf1, D1);                              \
  } while (0)
#define VFRAG(vb, s) __builtin_shufflevector(tr_read((vb) + (s) * 1024), tr_read((vb) + (s) * 1024 + 512), 0, 1, 2, 3, 4, 5, 6, 7)
#define READV(vb) do { vf0 = VFRAG(vb, 0); vf1 = VFRAG(vb, 1); vf2 = VFRAG(vb, 2); vf3 = VFRAG(vb, 3); } while (0)
#define PV()                                                                            \
  do {                                                                                  \
    oacc = MFMA32(vf0, __builtin_bit_cast(bf16x8, pp0), oacc);                          \
    oacc = MFMA32(vf1, __builtin_bit_cast(bf16x8, pp1), oacc);                          \
    oacc = MFMA32(vf2, __builtin_bit_cast(bf16x8, pp2), oacc);                          \
    oacc = MFMA32(vf3, __builtin_bit_cast(bf16x8, pp3), oacc);                          \
  } while (0)
#define EXPSUM(C0, C1, LS)                                                              \
  do {                                                                                  \
    f32x2 ls2_ = {0.f, 0.f}, ls3_ = {0.f, 0.f};                                         \
    _Pragma("unroll") for (int r = 0; r < 16; r += 2) {                                 \
      C0[r] = AEXP(C0[r]); C0[r + 1] = AEXP(C0[r + 1]);                                 \
      C1[r] = AEXP(C1[r]); C1[r + 1] = AEXP(C1[r + 1]);                                 \
      ls2_ += (f32x2){C0[r], C0[r + 1]};                                                \
      ls3_ += (f32x2){C1[r], C1[r + 1]};                                                \
    }                                                                                   \
    ls2_ += ls3_;                                                                       \
    LS = ls2_[0] + ls2_[1];                                                             \
  } while (0)
#define PACK(C0, C1)                                                                    \
  do {                                                                                  \
    _Pragma("unroll") for (int j = 0; j < 4; ++j) {                                     \
      pp0[j] = pack2(C0[2 * j], C0[2 * j + 1]);                                         \
      pp1[j] = pack2(C0[8 + 2 * j], C0[8 + 2 * j + 1]);                                 \
      pp2[j] = pack2(C1[2 * j], C1[2 * j + 1]);                                         \
      pp3[j] = pack2(C1[8 + 2 * j], C1[8 + 2 * j + 1]);                                 \
    }                                                                                   \
  } while (0)
#define STEP(C0, C1, N0, N1, kcur, knext, vprev, HASN)                                  \
  do {                                                                                  \
    READV(vprev);                                                                       \
    if (HASN) SCORES(N0, N1, knext, negm);                                              \
    PV();                                                                               \
    float ls;                                                                           \
    EXPSUM(C0, C1, ls);                                                                 \
    if (__builtin_expect(__any(!(ls <= BIG)), 0)) {                                     \
                  \
      f32x16 z_ = {};                                                                   \
      SCORES(C0, C1, kcur, z_);                                                         \
      const float mn = __builtin_fmaxf(m, tile_rowmax(C0, C1));                         \
      const float dm = m - mn;                                                          \
      const float f = __builtin_amdgcn_exp2f(dm);                                       \
      m = mn; l *= f;                                                                   \
      _Pragma("unroll") for (int r = 0; r < 16; ++r) {                                  \
        oacc[r] *= f; negm[r] = -mn; C0[r] -= mn; C1[r] -= mn;                          \
        if (HASN) { N0[r] += dm; N1[r] += dm; }                                         \
      }                                                                                 \
      EXPSUM(C0, C1, ls);                                                               \
    }                                                                                   \
    l += ls;                                                                            \
    PACK(C0, C1);                                                                       \
  } while (0)

  WAIT_V(2 * (AT_NS - 2));
  BAR();
  {
    f32x16 z = {};
    SCORES(sa0, sa1, smem, z);
    m = tile_rowmax(sa0, sa1);
#pragma unroll
    for (int r = 0; r < 16; ++r) { negm[r] = -m; sa0[r] -= m; sa1[r] -= m; }
  }
  int cur = 0, prv = 0;
  for (int t = 0; t < NT - 1; ++t) {
    const char* sb = smem + cur;
    int nx = cur + AT_ST; if (nx >= AT_NS * AT_ST) nx = 0;
    int prv2 = cur - AT_ST; if (prv2 < 0) prv2 = (AT_NS - 1) * AT_ST;
    STEP(sa0, sa1, sb0, sb1, sb, sb + 4096, smem + prv + 4096 + v_rd, true);
    if (t + AT_NS - 1 < NT) {
      WAIT_V(2 * (AT_NS - 3));
      WAIT_L0();
      BAR();
      issue(t + AT_NS - 1, prv2);
    } else {
      WAIT_V(0);
      WAIT_L0();
      BAR();
    }
    STEP(sb0, sb1, sa0, sa1, sb + 4096, smem + nx, sb + v_rd, true);
    prv = cur; cur = nx;
  }
  {
    const char* sb = smem + cur;
    STEP(sa0, sa1, sb0, sb1, sb, sb + 4096, smem + prv + 4096 + v_rd, true);
    STEP(sb0, sb1, sa0, sa1, sb + 4096, sb, sb + v_rd, false);
    prv = cur;
  }
  READV(smem + prv + 4096 + v_rd);
  PV();
#undef SCORES
#undef VFRAG
#undef READV
#undef PV
#undef EXPSUM
#undef PACK
#undef STEP
  {
    auto rr = __builtin_amdgcn_permlane32_swap(__float_as_uint(l), __float_as_uint(l), false, false);
    l = __uint_as_float(rr[0]) + __uint_as_float(rr[1]);
  }
  const float inv = 1.0f / l;
  u16* op = o + (size_t)(q0 + r32) * D_ + hd * 32 + 4 * hi;
#pragma unroll
  for (int g = 0; g < 4; ++g) {
    u32x2 w;
    w[0] = pack2(oacc[4 * g] * inv, oacc[4 * g + 1] * inv);
    w[1] = pack2(oacc[4 * g + 2] * inv, oacc[4 * g + 3] * inv);
    *(u32x2*)(op + 8 * g) = w;
  }
  STAMP_END(3);
}

constexpr int A2_KT = 256, A2_NS = 3, A2_ST = 32768, A2_MOD = A2_NS * A2_ST + 1024, A2_RED = A2_MOD + 32768, A2_FLG = A2_RED + 4096, A2_LDS = A2_FLG + 64;
template <bool SAFE>
__device__ __forceinline__ void attn_pass(char* smem, const u16* Kp, const u16* Vp, bf16x8 qf0, bf16x8 qf1, int wid, int lane,
                                          f32x16& oacc, float& m, float& l,
                                          const u16* pw0, const u16* pw1, const u16* pw2, const u16* pw3, const u16* wm, unsigned cc) {
  const int wk = wid >> 3, r32 = lane & 31, hi = lane >> 5;
  const unsigned k_voff = (unsigned)(((lane >> 2) * 32 + 8 * ((lane & 3) ^ ((lane >> 4) & 3))) * 2);
  const unsigned v_voff = (unsigned)(lane * 16);
  const unsigned lds0 = (unsigned)(size_t)(LAS char*)smem;
  auto issue = [&](int t, int bufoff) {
    const char* gk = (const char*)(Kp + ((size_t)t * A2_KT + wid * 16) * 32);
    const char* gv = (const char*)(Vp + ((size_t)t * A2_KT + wid * 16) * 32);
    glds16(gk, k_voff, lds0 + bufoff + wid * 1024);
    glds16(gv, v_voff, lds0 + bufoff + 16384 + wid * 1024);
  };
  issue(0, 0);
  issue(1, A2_ST);
  const int k_rd0 = wk * 8192 + r32 * 64 + ((hi ^ ((r32 >> 2) & 3)) << 4);
  const int k_rd1 = k_rd0 ^ 32;
  const int v_rd = 16384 + wk * 8192 + (4 * hi + ((lane & 15) >> 2)) * 64 + ((lane >> 4) & 1) * 32 + (lane & 3) * 8;
  f32x16 negm = {};
  f32x2 lsa = {0.f, 0.f}, lsb = {0.f, 0.f};
  oacc = (f32x16){};
  m = 0.f; l = 0.f;
  constexpr int NT = S_ / A2_KT;
  int cur = 0;
  for (int t = 0; t < NT; ++t) {
    if (t == 0) WAIT_V(2); else if (!SAFE && t == 4) WAIT_V(5); else if (t + 1 < NT) WAIT_V(3); else WAIT_V(0);
    BAR();
    if (!SAFE && t == 6) {
      const int rg = wid * 8 + (lane >> 3), cl = lane & 7;
      const float c0 = lo_f(cc), c1 = hi_f(cc);
      const float s0 = rbf(c0 / (1.0f + __expf(-c0))), s1 = rbf(c1 / (1.0f + __expf(-c1)));
      const u32x4 w0 = *(const LAS u32x4*)(smem + A2_MOD + (2 * rg) * 128 + cl * 16);
      const u32x4 w1 = *(const LAS u32x4*)(smem + A2_MOD + (2 * rg + 1) * 128 + cl * 16);
      float a[8];
#pragma unroll
      for (int j = 0; j < 4; ++j) {
        a[2 * j] = s0 * lo_f(w0[j]) + s1 * lo_f(w1[j]);
        a[2 * j + 1] = s0 * hi_f(w0[j]) + s1 * hi_f(w1[j]);
      }
#pragma unroll
      for (int j = 0; j < 8; ++j) {
        a[j] += __shfl_xor(a[j], 8); a[j] += __shfl_xor(a[j], 16); a[j] += __shfl_xor(a[j], 32);
      }
      if (lane < 8) {
        float* mred = (float*)(smem + A2_RED);
#pragma unroll
        for (int j = 0; j < 8; ++j) mred[wid * 64 + lane * 8 + j] = a[j];
      }
    }
    if (!SAFE && t == 3) {
      const unsigned m_voff = (unsigned)(((lane >> 3) * 6144 + (lane & 7) * 8) * 2);
      glds16(wm + (size_t)(wid * 16) * 6144, m_voff, lds0 + A2_MOD + wid * 2048);
      glds16(wm + (size_t)(wid * 16 + 8) * 6144, m_voff, lds0 + A2_MOD + wid * 2048 + 1024);
    }
    {
      int ck = (blockIdx.x * NT + t) * 16 + wid;
      if (ck >= 2048 + 3 * 7168) ck -= 16384;
      const u16* pb = pw0;
      if (ck >= 2048) { pb = pw1; ck -= 2048; if (ck >= 7168) { pb = pw2; ck -= 7168; if (ck >= 7168) { pb = pw3; ck -= 7168; } } }
      glds16((const char*)pb + (size_t)ck * 1024, v_voff, lds0 + A2_NS * A2_ST);
      int nb = cur + 2 * A2_ST; if (nb >= A2_NS * A2_ST) nb -= A2_NS * A2_ST;
      if (t + 2 < NT) issue(t + 2, nb);
    }
#pragma unroll
    for (int hb = 0; hb < 2; ++hb) {
      const char* kb = smem + cur + hb * 4096;
      const bf16x8 k00 = lds_read16(kb + k_rd0), k01 = lds_read16(kb + k_rd1);
      const bf16x8 k10 = lds_read16(kb + 2048 + k_rd0), k11 = lds_read16(kb + 2048 + k_rd1);
      f32x16 s0, s1;
      if (SAFE || (t == 0 && hb == 0)) {
        f32x16 z_ = {};
        s0 = MFMA32(k00, qf0, z_); s1 = MFMA32(k10, qf0, z_);
        s0 = MFMA32(k01, qf1, s0); s1 = MFMA32(k11, qf1, s1);
        const float tm = tile_rowmax(s0, s1);
        const bool first = (t == 0 && hb == 0);
        const float mn = first ? tm : __builtin_fmaxf(m, tm);
        if (SAFE) {
          const float f = first ? 0.f : __builtin_amdgcn_exp2f(m - mn);
          lsa *= f; lsb *= f;
          oacc *= f;
        }
        m = mn;
#pragma unroll
        for (int r = 0; r < 16; ++r) { negm[r] = -mn; s0[r] -= mn; s1[r] -= mn; }
      } else {
        s0 = MFMA32(k00, qf0, negm); s1 = MFMA32(k10, qf0, negm);
        s0 = MFMA32(k01, qf1, s0); s1 = MFMA32(k11, qf1, s1);
      }
#pragma unroll
      for (int r = 0; r < 16; r += 2) {
        s0[r] = AEXP(s0[r]); s0[r + 1] = AEXP(s0[r + 1]);
        s1[r] = AEXP(s1[r]); s1[r + 1] = AEXP(s1[r + 1]);
        lsa += (f32x2){s0[r], s0[r + 1]};
        lsb += (f32x2){s1[r], s1[r + 1]};
      }
      u32x4 p0, p1, p2, p3;
#pragma unroll
      for (int j = 0; j < 4; ++j) {
        p0[j] = pack2(s0[2 * j], s0[2 * j + 1]);
        p1[j] = pack2(s0[8 + 2 * j], s0[8 + 2 * j + 1]);
        p2[j] = pack2(s1[2 * j], s1[2 * j + 1]);
        p3[j] = pack2(s1[8 + 2 * j], s1[8 + 2 * j + 1]);
      }
      const char* vb = smem + cur + hb * 4096 + v_rd;
#define VFRAG(s) __builtin_shufflevector(tr_read(vb + (s) * 1024), tr_read(vb + (s) * 1024 + 512), 0, 1, 2, 3, 4, 5, 6, 7)
      oacc = MFMA32(VFRAG(0), __builtin_bit_cast(bf16x8, p0), oacc);
      oacc = MFMA32(VFRAG(1), __builtin_bit_cast(bf16x8, p1), oacc);
      oacc = MFMA32(VFRAG(2), __builtin_bit_cast(bf16x8, p2), oacc);
      oacc = MFMA32(VFRAG(3), __builtin_bit_cast(bf16x8, p3), oacc);
#undef VFRAG
    }
    WAIT_L0();
    cur += A2_ST; if (cur >= A2_NS * A2_ST) cur = 0;
  }
  lsa += lsb;
  l = lsa[0] + lsa[1];
  {
    auto rr = __builtin_amdgcn_permlane32_swap(__float_as_uint(l), __float_as_uint(l), false, false);
    l = __uint_as_float(rr[0]) + __uint_as_float(rr[1]);
  }
  WAIT_V(0);
}

__global__ __launch_bounds__(1024, 4) void attn2_kernel(const u16* __restrict__ qkv, u16* __restrict__ o,
                                                        const u16* pw0, const u16* pw1, const u16* pw2, const u16* pw3,
                                                        const u16* __restrict__ cvec, const u16* wmod, float* __restrict__ modp) {
  extern __shared__ __attribute__((aligned(16))) char smem[];
  STAMP_BEGIN();
  const int tid = threadIdx.x, lane = tid & 63;
  const int wid = __builtin_amdgcn_readfirstlane(tid >> 6);
  const int wq = wid & 7, wk = wid >> 3;
  const int x = blockIdx.x & 7, idx = blockIdx.x >> 3;
  const int hd = x * 4 + (idx >> 3), qb = idx & 7;
  const int r32 = lane & 31, hi = lane >> 5;
  const u16* Q = qkv + ((size_t)(0 * 32 + hd) * S_) * 32;
  const u16* Kp = qkv + ((size_t)(1 * 32 + hd) * S_) * 32;
  const u16* Vp = qkv + ((size_t)(2 * 32 + hd) * S_) * 32;
  const int q0 = qb * 256 + wq * 32;
  const bf16x8 qf0 = *(const bf16x8*)(Q + (size_t)(q0 + r32) * 32 + hi * 8);
  const bf16x8 qf1 = *(const bf16x8*)(Q + (size_t)(q0 + r32) * 32 + 16 + hi * 8);

  f32x16 oacc;
  float m, l;
  const int mcg = 32 + (blockIdx.x >> 2), mks = blockIdx.x & 3;
  const u16* wm = wmod + (size_t)(mks * 256) * 6144 + mcg * 64;
  unsigned cc = *(const unsigned*)(cvec + mks * 256 + 2 * (tid >> 3));
  asm volatile("" : "+v"(cc));
  attn_pass<false>(smem, Kp, Vp, qf0, qf1, wid, lane, oacc, m, l, pw0, pw1, pw2, pw3, wm, cc);
  float* xb = (float*)smem + wq * (20 * 64);
  const float* mred = (const float*)(smem + A2_RED);
  volatile LAS int* badf = (volatile LAS int*)(smem + A2_FLG);
  {
    float chk = l;
#pragma unroll
    for (int r = 0; r < 16; ++r) chk += __builtin_fabsf(oacc[r]);
    const int bad = __any(!(chk < 1.0e37f));
    if (lane == 0) badf[wid] = bad;
    __syncthreads();
    const int anybad = __any(badf[lane & 15] != 0);
    if (anybad) {
      __syncthreads();
      attn_pass<true>(smem, Kp, Vp, qf0, qf1, wid, lane, oacc, m, l, pw0, pw1, pw2, pw3, wm, cc);
      __syncthreads();
    }
  }
  xb[wk * 64 + lane] = m; xb[(2 + wk) * 64 + lane] = l;
#pragma unroll
  for (int r = 0; r < 8; ++r) xb[(4 + 8 * wk + r) * 64 + lane] = wk ? oacc[r] : oacc[8 + r];
  __syncthreads();
  {
    const float m2 = xb[(wk ^ 1) * 64 + lane], l2 = xb[(2 + (wk ^ 1)) * 64 + lane];
    const float mn = __builtin_fmaxf(m, m2);
    const float f1 = __builtin_amdgcn_exp2f((m - mn) * 0.5f), f2 = __builtin_amdgcn_exp2f((m2 - mn) * 0.5f);
    const float inv = 1.0f / ((l * f1) * f1 + (l2 * f2) * f2);
    u16* op = o + ((size_t)hd * S_ + q0 + r32) * 32 + 4 * hi + 16 * wk;
#pragma unroll
    for (int g = 0; g < 2; ++g) {
      float v[4];
#pragma unroll
      for (int e = 0; e < 4; ++e) {
        const float own = wk ? oacc[8 + 4 * g + e] : oacc[4 * g + e];
        v[e] = ((own * f1) * f1 + (xb[(4 + 8 * (wk ^ 1) + 4 * g + e) * 64 + lane] * f2) * f2) * inv;
      }
      u32x2 w;
      w[0] = pack2(v[0], v[1]);
      w[1] = pack2(v[2], v[3]);
      *(u32x2*)(op + 8 * g) = w;
    }
  }
  if (wid == 15) {
    float sacc = 0.f;
#pragma unroll
    for (int w = 0; w < 16; ++w) sacc += mred[w * 64 + lane];
    modp[mks * 6144 + mcg * 64 + lane] = sacc;
  }
  STAMP_END(3);
}

template <int MODE> static void launch_gemm(const GemmP& p, hipStream_t stream) {
  using C = GCfg<MODE>;
  constexpr int lds0_ = C::NS * ((C::AD ? 0 : C::BM) + C::BN) * 128, lds = lds0_ < 20480 ? 20480 : lds0_;
  hipFuncSetAttribute((const void*)gemm_kernel<MODE>, hipFuncAttributeMaxDynamicSharedMemorySize, lds);
  gemm_kernel<MODE><<<C::NTM * C::NTN, C::WM * C::WN * 64 * (C::LW ? 2 : 1), lds, stream>>>(p);
}

extern "C" void kernel_launch(void* const* d_in, const int* in_sizes, int n_in, void* d_out, int out_size,
                              void* d_ws, size_t ws_size, hipStream_t stream) {
  const u16* x = (const u16*)d_in[0];
  const u16* wqkv = (const u16*)d_in[1];
  const u16* wo = (const u16*)d_in[2];
  const u16* wg = (const u16*)d_in[4];
  const u16* wu = (const u16*)d_in[5];
  const u16* wd = (const u16*)d_in[6];
  const u16* c = (const u16*)d_in[7];
  const u16* wmod = (const u16*)d_in[8];
  u16* out = (u16*)d_out;

  char* ws = (char*)d_ws;
  float* mod = (float*)ws;
  u16* h = (u16*)(ws + (1 << 20));
  u16* qkv = (u16*)(ws + (8 << 20));
  u16* ao = (u16*)(ws + (24 << 20));
  u16* x1 = (u16*)(ws + (32 << 20));
  u16* hh = (u16*)(ws + (40 << 20));
  float* slab = (float*)(ws + (64 << 20));
  unsigned* flags = (unsigned*)(ws + (512 << 10));

  mod_kernel<<<256, 256, 0, stream>>>(c, wmod, mod, x, wqkv, flags);
  ln_kernel<<<512 + 192, 256, 0, stream>>>(x, mod, 0, h, wqkv, 1);
  { GemmP p{h, wqkv, nullptr, nullptr, nullptr, qkv, nullptr, nullptr, nullptr}; launch_gemm<0>(p, stream); }
  hipFuncSetAttribute((const void*)attn2_kernel, hipFuncAttributeMaxDynamicSharedMemorySize, A2_LDS);
  attn2_kernel<<<256, 1024, A2_LDS, stream>>>(qkv, ao, wo, wg, wu, wd, c, wmod, mod);
  { GemmP p{ao, wo, nullptr, x, mod + 2 * 1024, x1, slab, flags, h}; launch_gemm<1>(p, stream);
  }
  ln_kernel<<<512, 256, 0, stream>>>(x1, mod, 3 * 1024, h, wqkv, 5);
  { GemmP p{h, wg, wu, nullptr, nullptr, hh, nullptr, nullptr, nullptr}; launch_gemm<2>(p, stream); }
  { GemmP p{hh, wd, nullptr, x1, mod + 5 * 1024, out, slab, flags, nullptr}; launch_gemm<4>(p, stream); }
}
```

```cpp
#include <hip/hip_runtime.h>
#include <hip/hip_bf16.h>
#include <stdint.h>
#define NO_LN2_FUSE

typedef unsigned short u16;
typedef short bf16x8 __attribute__((ext_vector_type(8)));
typedef short s16x4 __attribute__((ext_vector_type(4)));
typedef float f32x2 __attribute__((ext_vector_type(2)));
typedef float f32x4 __attribute__((ext_vector_type(4)));
typedef float f32x16 __attribute__((ext_vector_type(16)));
typedef unsigned u32x2 __attribute__((ext_vector_type(2)));
typedef unsigned u32x4 __attribute__((ext_vector_type(4)));
typedef __bf16 bf16x2_t __attribute__((ext_vector_type(2)));
#define LAS __attribute__((address_space(3)))
#define GAS __attribute__((address_space(1)))

constexpr int S_ = 2048, D_ = 1024, H_ = 32, DH_ = 32, F_ = 3584;

__device__ __forceinline__ float bf2f(u16 u) { return __uint_as_float((unsigned)u << 16); }
__device__ __forceinline__ unsigned pack2(float lo, float hi) {
  f32x2 v = {lo, hi};
  bf16x2_t b = __builtin_convertvector(v, bf16x2_t);
  return __builtin_bit_cast(unsigned, b);
}
__device__ __forceinline__ float rbf(float f) {
  __bf16 b = (__bf16)f;
  return __uint_as_float((unsigned)__builtin_bit_cast(u16, b) << 16);
}
__device__ __forceinline__ float lo_f(unsigned u) { return __uint_as_float(u << 16); }
__device__ __forceinline__ float hi_f(unsigned u) { return __uint_as_float(u & 0xffff0000u); }

#define STAMP_BEGIN()
#define STAMP_END(kid)
#define STAMP_MID(kid, i)
#define WAIT_V(n) asm volatile("s_waitcnt vmcnt(%0)" ::"n"(n) : "memory")
#define WAIT_L0() asm volatile("s_waitcnt lgkmcnt(0)" ::: "memory")
#define BAR() __builtin_amdgcn_s_barrier()

__device__ __forceinline__ void glds16(const void* sbase, unsigned voff, unsigned ldsaddr) {
  asm volatile("s_mov_b32 m0, %2\n\ts_nop 0\n\tglobal_load_lds_dwordx4 %0, %1" ::"v"(voff), "s"(sbase), "s"(ldsaddr) : "memory");
}
#define MFMA16(c, a, b) c = __builtin_amdgcn_mfma_f32_16x16x32_bf16(a, b, c, 0, 0, 0)
#define ST16(p, v) asm volatile("global_store_dwordx4 %0, %1, off sc0 sc1\n\ts_nop 1" ::"v"(p), "v"(v) : "memory")
#define SBAR() __builtin_amdgcn_sched_barrier(0)
#define SGB(mask, n) __builtin_amdgcn_sched_group_barrier(mask, n, 0)
template <int I> struct IC { static constexpr int value = I; };
template <int I, int N, class F> __device__ __forceinline__ void sfor(F&& f) {
  if constexpr (I < N) { f(IC<I>{}); sfor<I + 1, N>(f); }
}
__device__ __forceinline__ s16x4 tr_read(const char* p) {
  return __builtin_amdgcn_ds_read_tr16_b64_v4i16((LAS s16x4*)p);
}
__device__ __forceinline__ bf16x8 lds_read16(const char* p) { return *(const LAS bf16x8*)p; }

__device__ __forceinline__ void mod_block(const u16* __restrict__ c, const u16* __restrict__ wmod, float* __restrict__ modp,
                                          int cg, int ks, float* sh) {
  const int tid = threadIdx.x;
  const int cl = tid & 7, rg = tid >> 3;
  const int col0 = cg * 64 + cl * 8;
  u32x4 w[8];
#pragma unroll
  for (int i = 0; i < 8; ++i)
    w[i] = __builtin_nontemporal_load((const u32x4*)(wmod + (size_t)(ks * 256 + rg + i * 32) * 6144 + col0));
  {
    const float v = bf2f(c[ks * 256 + tid]);
    sh[tid] = rbf(v / (1.0f + __expf(-v)));
  }
  __syncthreads();
  float acc[8];
#pragma unroll
  for (int j = 0; j < 8; ++j) acc[j] = 0.f;
#pragma unroll
  for (int i = 0; i < 8; ++i) {
    const float s = sh[rg + i * 32];
#pragma unroll
    for (int j = 0; j < 4; ++j) {
      acc[2 * j] += s * lo_f(w[i][j]);
      acc[2 * j + 1] += s * hi_f(w[i][j]);
    }
  }
  float* red = sh + 256;
#pragma unroll
  for (int j = 0; j < 8; ++j) red[rg * 65 + cl * 8 + j] = acc[j];
  __syncthreads();
  if (tid < 64) {
    float s = 0.f;
#pragma unroll 8
    for (int r = 0; r < 32; ++r) s += red[r * 65 + tid];
    modp[ks * 6144 + cg * 64 + tid] = s;
  }
}
__device__ __forceinline__ void touch_block(const char* src) {
  u32x4 v[8];
#pragma unroll
  for (int i = 0; i < 8; ++i) v[i] = *(const u32x4*)(src + i * 4096 + threadIdx.x * 16);
#pragma unroll
  for (int i = 0; i < 8; ++i) asm volatile("" ::"v"(v[i]));
}
__device__ __forceinline__ f32x4 ld_mod4(const float* modp, int idx) {
  f32x4 a = *(const f32x4*)(modp + idx), b = *(const f32x4*)(modp + 6144 + idx);
  f32x4 c = *(const f32x4*)(modp + 2 * 6144 + idx), d = *(const f32x4*)(modp + 3 * 6144 + idx);
  f32x4 s = (a + b) + (c + d);
  return (f32x4){rbf(s[0]), rbf(s[1]), rbf(s[2]), rbf(s[3])};
}

__global__ __launch_bounds__(256) void mod_kernel(const u16* __restrict__ c, const u16* __restrict__ wmod,
                                                  float* __restrict__ modp, const u16* __restrict__ pf0,
                                                  const u16* __restrict__ pf1, unsigned* flags) {
  __shared__ float sh[256 + 32 * 65];
  STAMP_BEGIN();
  const int b = blockIdx.x;
  if (threadIdx.x == 0 && b < 256) flags[b * 32] = 0u;
  if (b < 128) mod_block(c, wmod, modp, b >> 2, b & 3, sh);
  else if (b < 256) touch_block((const char*)pf0 + (size_t)(b - 128) * 32768);
  else touch_block((const char*)pf1 + (size_t)(b - 256) * 32768);
  STAMP_END(0);
}

__global__ __launch_bounds__(256) void ln_kernel(const u16* __restrict__ x, const float* modp, int moff, u16* __restrict__ h,
                                                 const u16* __restrict__ pf, const u16* __restrict__ c,
                                                 const u16* __restrict__ wmod, float* modp_w, int kid) {
  __shared__ float sh[256 + 32 * 65];
  STAMP_BEGIN();
  const int tid = threadIdx.x;
  if (blockIdx.x >= 512) {
    const int b = blockIdx.x - 512;
    if (b < 192) touch_block((const char*)pf + (size_t)b * 32768);
    else mod_block(c, wmod, modp_w, 32 + ((b - 192) >> 2), b & 3, sh);
    return;
  }
  const int lane = tid & 63, wid = tid >> 6;
  const int row = blockIdx.x * 4 + wid;
  const u16* xr = x + (size_t)row * D_;
  const u32x4 v0 = *(const u32x4*)(xr + lane * 8);
  const u32x4 v1 = *(const u32x4*)(xr + 512 + lane * 8);
  {
    const f32x4 s0 = ld_mod4(modp, moff + tid * 4);
    const f32x4 s1 = ld_mod4(modp, moff + 1024 + tid * 4);
    *(f32x4*)(sh + tid * 4) = s0;
    *(f32x4*)(sh + 1024 + tid * 4) = (f32x4){rbf(1.0f + s1[0]), rbf(1.0f + s1[1]), rbf(1.0f + s1[2]), rbf(1.0f + s1[3])};
  }
  float f[16];
#pragma unroll
  for (int j = 0; j < 4; ++j) {
    f[2 * j] = lo_f(v0[j]); f[2 * j + 1] = hi_f(v0[j]);
    f[8 + 2 * j] = lo_f(v1[j]); f[8 + 2 * j + 1] = hi_f(v1[j]);
  }
  float s = 0.f;
#pragma unroll
  for (int j = 0; j < 16; ++j) s += f[j];
#pragma unroll
  for (int o = 32; o >= 1; o >>= 1) s += __shfl_xor(s, o);
  const float mean = s * (1.0f / D_);
  float q = 0.f;
#pragma unroll
  for (int j = 0; j < 16; ++j) { float d = f[j] - mean; q += d * d; }
#pragma unroll
  for (int o = 32; o >= 1; o >>= 1) q += __shfl_xor(q, o);
  const float mb = rbf(mean);
  const float vb = rbf(q * (1.0f / D_));
  const float sd = rbf(sqrtf(rbf(vb + 1e-5f)));
  const float inv = 1.0f / sd;
  __syncthreads();
  float o[16];
#pragma unroll
  for (int half = 0; half < 2; ++half) {
    const int c0 = half * 512 + lane * 8;
    const f32x4 sa0 = *(const f32x4*)(sh + c0), sa1 = *(const f32x4*)(sh + c0 + 4);
    const f32x4 ca0 = *(const f32x4*)(sh + 1024 + c0), ca1 = *(const f32x4*)(sh + 1024 + c0 + 4);
#pragma unroll
    for (int j = 0; j < 8; ++j) {
      const float shf = j < 4 ? sa0[j & 3] : sa1[j & 3];
      const float sc = j < 4 ? ca0[j & 3] : ca1[j & 3];
      float t = rbf(f[half * 8 + j] - mb);
      t = rbf(t * inv);
      t = rbf(t * sc);
      o[half * 8 + j] = t + shf;
    }
  }
  u16* hr = h + (size_t)row * D_;
  u32x4 w0, w1;
#pragma unroll
  for (int j = 0; j < 4; ++j) {
    w0[j] = pack2(o[2 * j], o[2 * j + 1]);
    w1[j] = pack2(o[8 + 2 * j], o[8 + 2 * j + 1]);
  }
  ST16(hr + lane * 8, w0);
  ST16(hr + 512 + lane * 8, w1);
  STAMP_END(kid);
}

struct GemmP {
  const u16* A; const u16* B0; const u16* B1; const u16* res; const float* gate; u16* out; float* slab; unsigned* flags; u16* out2;
};

#define NS0 3
#define NS1 3
#define NS3 3
#define LW0 1
#define LW1 1
#define LW3 1
#define AD1 0
#define AD4 0
template <int MODE> struct GCfg;
template <> struct GCfg<0> { static constexpr int BM = 128, BN = 192, WM = 2, WN = 2, NS = NS0, K = 1024, LDA = 1024, LDB = 3072, NTM = 16, NTN = 16, RM = 8, RN = 4, LW = LW0, AD = 0; };
template <> struct GCfg<1> { static constexpr int BM = 128, BN = 64, WM = AD1 ? 4 : 2, WN = AD1 ? 1 : 2, NS = NS1, K = 1024, LDA = 1024, LDB = 1024, NTM = 16, NTN = 16, RM = 4, RN = 8, LW = LW1, AD = AD1; };
template <> struct GCfg<2> { static constexpr int BM = 256, BN = 256, WM = 4, WN = 2, NS = 2, K = 1024, LDA = 1024, LDB = 3584, NTM = 8, NTN = 28, RM = 4, RN = 7, LW = 0, AD = 0; };
template <> struct GCfg<3> { static constexpr int BM = 128, BN = 128, WM = 2, WN = 2, NS = NS3, K = 1792, LDA = 3584, LDB = 1024, NTM = 16, NTN = 16, RM = 4, RN = 8, LW = LW3, AD = 0; };

template <> struct GCfg<4> { static constexpr int BM = 128, BN = 64, WM = AD4 ? 4 : 2, WN = AD4 ? 1 : 2, NS = NS3, K = 3584, LDA = 3584, LDB = 1024, NTM = 16, NTN = 16, RM = 4, RN = 8, LW = LW3, AD = AD4; };

template <int MODE>
__global__ __launch_bounds__(GCfg<MODE>::WM* GCfg<MODE>::WN * 64 * (GCfg<MODE>::LW ? 2 : 1), 2) void gemm_kernel(GemmP p) {
  using C = GCfg<MODE>;
  constexpr int BM = C::BM, BN = C::BN, WM = C::WM, WN = C::WN, NS = C::NS, NW = WM * WN;
  constexpr int TM = BM / WM, TN = BN / WN, MI = TM / 16, NI = TN / 16;
  constexpr int AD = C::AD;
  constexpr int A_BYTES = AD ? 0 : BM * 128, B_BYTES = BN * 128, ST_BYTES = A_BYTES + B_BYTES;
  constexpr int GA = AD ? 0 : BM / 8 / NW, GB = BN / 8 / NW, G = GA + GB;
  constexpr int NT = C::K / 64;
  static_assert(BM % (8 * NW) == 0 && BN % (8 * NW) == 0, "stage split");
  static_assert(NT >= NS + 1, "K too small");
  extern __shared__ __attribute__((aligned(16))) char smem[];
  STAMP_BEGIN();

  const int tid = threadIdx.x, lane = tid & 63;
  constexpr bool LW = C::LW != 0;
  const int wid_all = __builtin_amdgcn_readfirstlane(tid >> 6);
  const int wid = wid_all % NW;
  const int wm = wid / WN, wn = wid % WN;
  int tm, tn;
  {
    const int x = blockIdx.x & 7, idx = blockIdx.x >> 3;
    constexpr int NRM = C::NTM / C::RM;
    const int rm = x % NRM, rn = x / NRM;
    tm = rm * C::RM + idx % C::RM;
    tn = rn * C::RN + idx / C::RM;
  }
  int kh = 0;
  if constexpr (MODE == 3) { kh = tn & 1; tn >>= 1; }
  const int m0 = tm * BM;

  const int par = wid & 1;
  const int a_ch = (lane & 7) ^ ((4 * par + (lane >> 4)) & 7);
  const unsigned a_voff = (MODE == 1) ? (unsigned)((((a_ch >> 2) * S_ + (lane >> 3)) * 32 + (a_ch & 3) * 8) * 2)
                                      : (unsigned)(((lane >> 3) * C::LDA + 8 * a_ch) * 2);
  const unsigned b_voff = (unsigned)(((lane >> 3) * C::LDB + 8 * ((lane & 7) ^ ((((lane >> 4) & 1) | (par << 1)) << 1))) * 2);
  const char* Abase = (MODE == 1) ? (const char*)(p.A + (size_t)m0 * 32) : (const char*)(p.A + (size_t)m0 * C::LDA + kh * C::K);
  const unsigned lds0 = (unsigned)(size_t)(LAS char*)smem;

  auto issue1 = [&](auto gi, int t, int bufoff) {
    constexpr int g = decltype(gi)::value;
    if constexpr (g < GA) {
      const int i = wid + NW * g;
      const char* gp = (MODE == 1) ? Abase + ((size_t)(8 * i) * 32 + (size_t)t * 2 * S_ * 32) * 2
                                   : Abase + ((size_t)(8 * i) * C::LDA + (size_t)t * 64) * 2;
      glds16(gp, a_voff, lds0 + bufoff + i * 1024);
    } else {
      const int i = wid + NW * (g - GA);
      const int pn = i >> 3, kr = 8 * (i & 7);
      const u16* bp;
      if constexpr (MODE == 2) bp = (pn < 2 ? p.B0 : p.B1) + tn * 128 + (pn & 1) * 64;
      else bp = p.B0 + (size_t)kh * C::K * C::LDB + tn * BN + pn * 64;
      const char* gp = (const char*)(bp + ((size_t)t * 64 + kr) * C::LDB);
      glds16(gp, b_voff, lds0 + bufoff + A_BYTES + i * 1024);
    }
  };

  if constexpr (C::LW == 1) {
    if (wid_all >= NW) {
      sfor<0, NS>([&](auto sc_) { sfor<0, G>([&](auto gc) { issue1(gc, decltype(sc_)::value, decltype(sc_)::value * ST_BYTES); }); });
      WAIT_V((NS - 1) * G);
      BAR();
      int lb = 0;
      for (int t = 0; t < NT - NS; ++t) {
        WAIT_V((NS - 2) * G);
        BAR();
        sfor<0, G>([&](auto gc) { issue1(gc, t + NS, lb); });
        lb = (lb + ST_BYTES == NS * ST_BYTES) ? 0 : lb + ST_BYTES;
      }
      sfor<0, NS - 1>([&](auto rc) { WAIT_V((NS - 2 - decltype(rc)::value) * G); BAR(); });
      return;
    }
  }
  if constexpr (C::LW == 2) {
    if (wid_all >= NW) {
      static_assert(NS == 3 && (NT & 1) == 0, "reg loader");
      u32x4 R0[G], R1[G];
      auto ld = [&](u32x4* R, int t) {
        sfor<0, G>([&](auto gi) {
          constexpr int g = decltype(gi)::value;
          if constexpr (g < GA) {
            const int i = wid + NW * g;
            R[g] = *(const u32x4*)(Abase + ((size_t)(8 * i) * C::LDA + (size_t)t * 64) * 2 + a_voff);
          } else {
            const int i = wid + NW * (g - GA);
            const int pn = i >> 3, kr = 8 * (i & 7);
            const u16* bp = p.B0 + (size_t)kh * C::K * C::LDB + tn * BN + pn * 64;
            R[g] = *(const u32x4*)((const char*)(bp + ((size_t)t * 64 + kr) * C::LDB) + b_voff);
          }
        });
      };
      auto st = [&](const u32x4* R, int bufoff) {
        sfor<0, G>([&](auto gi) {
          constexpr int g = decltype(gi)::value;
          const int off = g < GA ? (wid + NW * g) * 1024 : A_BYTES + (wid + NW * (g - GA)) * 1024;
          *(LAS u32x4*)(smem + bufoff + off + lane * 16) = R[g];
        });
      };
      ld(R0, 0); ld(R1, 1);
      st(R0, 0); ld(R0, 2);
      WAIT_L0(); BAR();
      st(R1, ST_BYTES); ld(R1, 3);
      st(R0, 2 * ST_BYTES); ld(R0, 4);
      int lb = 0;
      for (int t = 0; t < NT - 4; t += 2) {
        WAIT_L0(); BAR();
        st(R1, lb); if (t + 5 < NT) ld(R1, t + 5);
        lb = (lb + ST_BYTES == NS * ST_BYTES) ? 0 : lb + ST_BYTES;
        WAIT_L0(); BAR();
        st(R0, lb); if (t + 6 < NT) ld(R0, t + 6);
        lb = (lb + ST_BYTES == NS * ST_BYTES) ? 0 : lb + ST_BYTES;
      }
      WAIT_L0(); BAR();
      st(R1, lb);
      WAIT_L0(); BAR();
      BAR();
      return;
    }
  }

  const int fr = lane & 15, fq = lane >> 4;
  u32x4 rv[4];
  f32x4 gv[NI];
  if constexpr (MODE == 1 || MODE == 3 || MODE == 4) {
#pragma unroll
    for (int i = 0; i < 4; ++i) {
      if constexpr (MODE != 3) rv[i] = *(const u32x4*)(p.res + (size_t)(m0 + i * 32 + (tid >> 3)) * D_ + tn * BN + (tid & 7) * 8);
      else rv[i] = *(const u32x4*)(p.res + (size_t)(m0 + kh * 64 + i * 16 + (tid >> 4)) * D_ + tn * BN + (tid & 15) * 8);
    }
#pragma unroll
    for (int ni = 0; ni < NI; ++ni) { gv[ni] = ld_mod4(p.gate, tn * BN + wn * TN + ni * 16 + 4 * fq); asm volatile("" : "+v"(gv[ni])); }
  }
  const int a_rd0 = (wm * TM + fr) * 128 + ((fq ^ (fr >> 1)) << 4);
  const int a_rd1 = a_rd0 ^ 64;
  int b_rd[(MODE == 2) ? 4 : NI];
  {
    const int q = (lane & 15) >> 2, pp = lane & 3;
    const int gl = ((q >> 1) & 1) | ((fq & 1) << 1);
#pragma unroll
    for (int ni = 0; ni < ((MODE == 2) ? 4 : NI); ++ni) {
      int pn, un;
      if constexpr (MODE == 2) { pn = wn; un = ni & 3; }
      else { const int j = wn * NI + ni; pn = j >> 2; un = j & 3; }
      b_rd[ni] = A_BYTES + pn * 8192 + (8 * fq + q) * 128 + ((un ^ gl) << 5) + pp * 8;
    }
  }

  f32x4 acc[MI][NI];
#pragma unroll
  for (int mi = 0; mi < MI; ++mi)
#pragma unroll
    for (int ni = 0; ni < NI; ++ni) acc[mi][ni] = (f32x4){0.f, 0.f, 0.f, 0.f};

  constexpr int NSP = (MODE == 2) ? 2 : 1;
  constexpr int NH = NI / NSP, NM = MI * NH, NPH = 2 * NSP;
  constexpr int GW = (NS >= 3) ? NPH : NPH / 2;
  constexpr int GPP = (G + GW - 1) / GW;
  bf16x8 a0[MI], a1[MI], b0[NH], b1[NH];
  s16x4 blo[NH];

  auto rd_b = [&](auto ri, bf16x8* BF, const char* sb, int ks, int hf) {
    constexpr int r = decltype(ri)::value, ni = r >> 1;
    const int bo = (MODE == 2) ? b_rd[ni] + hf * 16384 : b_rd[ni];
    if constexpr ((r & 1) == 0) blo[ni] = tr_read(sb + bo + ks * 4096);
    else BF[ni] = __builtin_shufflevector(blo[ni], tr_read(sb + bo + ks * 4096 + 512), 0, 1, 2, 3, 4, 5, 6, 7);
  };
  auto rd_a = [&](auto ri, bf16x8* AF, const char* sb, int ks) {
    constexpr int mi = decltype(ri)::value;
    AF[mi] = lds_read16(sb + (ks ? a_rd1 : a_rd0) + mi * 2048);
  };
  auto phase = [&](auto hfc, auto rdm, auto g0c, auto g1c, bf16x8* AF, bf16x8* BF, bf16x8* AN_, bf16x8* BN_,
                   const char* rsb, int rks, int rhf, int gt, int gbuf) {
    constexpr int hf = decltype(hfc)::value, RDM = decltype(rdm)::value;
    constexpr int g0 = decltype(g0c)::value, g1 = decltype(g1c)::value, NG = g1 - g0;
    constexpr int NR = RDM == 0 ? 0 : (RDM == 1 ? 2 * NH : 2 * NH + MI);
    sfor<0, NM>([&](auto ic) {
      constexpr int i = decltype(ic)::value, mi = i / NH, ni = i % NH;
      MFMA16(acc[mi][hf * NH + ni], BF[ni], AF[mi]);
      sfor<0, NR>([&](auto rc) {
        constexpr int r = decltype(rc)::value;
        if constexpr ((r * (NM - NM / 4)) / (NR > 0 ? NR : 1) == i) {
          if constexpr (r < 2 * NH) rd_b(rc, BN_, rsb, rks, rhf);
          else rd_a(IC<r - 2 * NH>{}, AN_, rsb, rks);
        }
      });
      sfor<0, NG>([&](auto gc) {
        constexpr int g = decltype(gc)::value;
        if constexpr ((g * NM) / (NG > 0 ? NG : 1) == i) issue1(IC<g0 + g>{}, gt, gbuf);
      });
      SBAR();
    });
  };
  constexpr auto gmin = [](int a, int b) { return a < b ? a : b; };
  int cur = 0, prv = 0;
  auto ktile = [&](auto vmc, auto restc, auto firstc, int t) {
    constexpr int VM = decltype(vmc)::value;
    constexpr bool REST = decltype(restc)::value && !LW, FIRST = decltype(firstc)::value && !LW;
    const int nxt = (cur + ST_BYTES == NS * ST_BYTES) ? 0 : cur + ST_BYTES;
    const char* sc = smem + cur;
    const char* sn = smem + nxt;
    constexpr int R1 = REST ? gmin(G, GPP) : 0, R2 = REST ? gmin(G, 2 * GPP) : 0, R3 = REST ? gmin(G, 3 * GPP) : 0, R4 = REST ? gmin(G, 4 * GPP) : 0;
    constexpr int F1 = FIRST ? gmin(G, GPP) : 0;
    if constexpr (NSP == 1) {
      phase(IC<0>{}, IC<2>{}, IC<R1>{}, IC<R2>{}, a0, b0, a1, b1, sc, 1, 0, t - 1 + NS, prv);
      if constexpr (VM >= 0) {
        if constexpr (!LW) WAIT_V(VM);
        WAIT_L0(); BAR();
        phase(IC<0>{}, IC<2>{}, IC<0>{}, IC<F1>{}, a1, b1, a0, b0, sn, 0, 0, t + NS, cur);
      } else {
        phase(IC<0>{}, IC<0>{}, IC<0>{}, IC<0>{}, a1, b1, a0, b0, sn, 0, 0, 0, 0);
      }
    } else {
      phase(IC<0>{}, IC<1>{}, IC<R1>{}, IC<R2>{}, a0, b0, a1, b1, sc, 0, 1, t - 1 + NS, prv);
      phase(IC<1>{}, IC<2>{}, IC<R2>{}, IC<R3>{}, a0, b1, a1, b0, sc, 1, 0, t - 1 + NS, prv);
      phase(IC<0>{}, IC<1>{}, IC<R3>{}, IC<R4>{}, a1, b0, a1, b1, sc, 1, 1, t - 1 + NS, prv);
      if constexpr (VM >= 0) {
        WAIT_V(VM); WAIT_L0(); BAR();
        phase(IC<1>{}, IC<2>{}, IC<0>{}, IC<F1>{}, a1, b1, a0, b0, sn, 0, 0, t + NS, cur);
      } else {
        phase(IC<1>{}, IC<0>{}, IC<0>{}, IC<0>{}, a1, b1, a0, b0, sn, 0, 0, 0, 0);
      }
    }
    prv = cur; cur = nxt;
  };

  if constexpr (AD > 0) {
    static_assert(LW && NSP == 1 && NT % AD == 0, "A-direct path");
    bf16x8 ar[AD][2][MI];
    const char* Aw = (MODE == 1) ? Abase + ((size_t)(wm * TM + fr) * 32 + fq * 8) * 2
                                 : Abase + ((size_t)(wm * TM + fr) * C::LDA + fq * 8) * 2;
    auto lda = [&](auto dc, int t) {
      constexpr int d = decltype(dc)::value;
#pragma unroll
      for (int ks = 0; ks < 2; ++ks)
#pragma unroll
        for (int mi = 0; mi < MI; ++mi) {
          if constexpr (MODE == 1) ar[d][ks][mi] = *(const bf16x8*)(Aw + ((size_t)(2 * t + ks) * S_ + mi * 16) * 64);
          else ar[d][ks][mi] = *(const bf16x8*)(Aw + (size_t)mi * 16 * C::LDA * 2 + t * 128 + ks * 64);
        }
    };
    sfor<0, AD>([&](auto dc) { lda(dc, decltype(dc)::value); });
    BAR();
    STAMP_MID(MODE == 0 ? 2 : MODE == 1 ? 4 : MODE == 2 ? 6 : 7, 0);
    sfor<0, 2 * NH>([&](auto rc) { rd_b(rc, b0, smem, 0, 0); });
    auto ktile_ad = [&](auto dc, auto ldc, auto lastc, int t) {
      constexpr int d = decltype(dc)::value;
      const int nxt = (cur + ST_BYTES == NS * ST_BYTES) ? 0 : cur + ST_BYTES;
      phase(IC<0>{}, IC<1>{}, IC<0>{}, IC<0>{}, ar[d][0], b0, a1, b1, smem + cur, 1, 0, 0, 0);
      if constexpr (!decltype(lastc)::value) {
        WAIT_L0(); BAR();
        phase(IC<0>{}, IC<1>{}, IC<0>{}, IC<0>{}, ar[d][1], b1, a0, b0, smem + nxt, 0, 0, 0, 0);
      } else {
        phase(IC<0>{}, IC<0>{}, IC<0>{}, IC<0>{}, ar[d][1], b1, a0, b0, smem + nxt, 0, 0, 0, 0);
      }
      if constexpr (decltype(ldc)::value) { lda(dc, t + AD); SBAR(); }
      cur = nxt;
    };
    for (int t = 0; t < NT - AD; t += AD) sfor<0, AD>([&](auto dc) { ktile_ad(dc, IC<1>{}, IC<0>{}, t + decltype(dc)::value); });
    sfor<0, AD>([&](auto dc) { ktile_ad(dc, IC<0>{}, IC<(decltype(dc)::value == AD - 1)>{}, 0); });
  } else {
  if constexpr (!LW) {
    sfor<0, NS>([&](auto sc_) { sfor<0, G>([&](auto gc) { issue1(gc, decltype(sc_)::value, decltype(sc_)::value * ST_BYTES); }); });
    WAIT_V((NS - 1) * G);
  }
  BAR();
  STAMP_MID(MODE == 0 ? 2 : MODE == 1 ? 4 : MODE == 2 ? 6 : 7, 0);
  sfor<0, 2 * NH>([&](auto rc) { rd_b(rc, b0, smem, 0, 0); });
  sfor<0, MI>([&](auto rc) { rd_a(rc, a0, smem, 0); });
  using TT = IC<1>; using FF = IC<0>;
  ktile(IC<(NS - 2) * G>{}, FF{}, TT{}, 0);
  for (int t = 1; t < NT - NS; ++t) ktile(IC<(NS - 2) * G>{}, TT{}, TT{}, t);
  ktile(IC<(NS - 2) * G>{}, TT{}, FF{}, NT - NS);
  sfor<0, NS - 2>([&](auto rc) { ktile(IC<(NS - 3 - decltype(rc)::value) * G>{}, FF{}, FF{}, 0); });
  ktile(IC<-1>{}, FF{}, FF{}, 0);
  }

  STAMP_MID(MODE == 0 ? 2 : MODE == 1 ? 4 : MODE == 2 ? 6 : 7, 1);
  if constexpr (MODE == 0) {
    const float qs = 0.17677669529663687f * 1.4426950408889634f;
    BAR();
    char* st = smem + wid * (64 * 208);
#pragma unroll
    for (int ni = 0; ni < NI; ++ni) {
      const int n = tn * BN + wn * TN + ni * 16;
      const float sc = n < 1024 ? qs : 1.0f;
#pragma unroll
      for (int mi = 0; mi < MI; ++mi) {
        u32x2 w;
        w[0] = pack2(acc[mi][ni][0] * sc, acc[mi][ni][1] * sc);
        w[1] = pack2(acc[mi][ni][2] * sc, acc[mi][ni][3] * sc);
        *(LAS u32x2*)(st + (mi * 16 + fr) * 208 + ni * 32 + fq * 8) = w;
      }
    }
#pragma unroll
    for (int j = 0; j < 3; ++j) {
      const int hh = (tn * BN + wn * TN + j * 32) >> 5;
      u16* ob = p.out + ((size_t)hh * S_ + m0 + wm * TM) * 32 + lane * 8;
#pragma unroll
      for (int i = 0; i < 4; ++i) {
        const u32x4 v = *(const LAS u32x4*)(st + (i * 16 + (lane >> 2)) * 208 + j * 64 + (lane & 3) * 16);
        ST16(ob + i * 512, v);
      }
    }
  } else if constexpr (MODE == 3) {
    const int tile = tm * 8 + tn;
    if (wm != kh) {
      float* dst = p.slab + ((size_t)(tile * 2 + kh) * 2 + wn) * 4096 + lane * 4;
#pragma unroll
      for (int mi = 0; mi < MI; ++mi)
#pragma unroll
        for (int ni = 0; ni < NI; ++ni) ST16(dst + (mi * NI + ni) * 256, __builtin_bit_cast(u32x4, acc[mi][ni]));
      WAIT_V(0);
      if (lane == 0) __hip_atomic_fetch_add(p.flags + (tile * 2 + kh) * 32, 1u, __ATOMIC_RELAXED, __HIP_MEMORY_SCOPE_AGENT);
      BAR();
    } else {
      const unsigned* fl = p.flags + (tile * 2 + (kh ^ 1)) * 32;
      for (int spin = 0; spin < (1 << 22); ++spin) {
        if (__builtin_amdgcn_readfirstlane(__hip_atomic_load(fl, __ATOMIC_RELAXED, __HIP_MEMORY_SCOPE_AGENT)) >= 2u) break;
        __builtin_amdgcn_s_sleep(1);
      }
      const float* src = p.slab + ((size_t)(tile * 2 + (kh ^ 1)) * 2 + wn) * 4096 + lane * 4;
      f32x4 pv[MI][NI];
#pragma unroll
      for (int mi = 0; mi < MI; ++mi)
#pragma unroll
        for (int ni = 0; ni < NI; ++ni)
          asm volatile("global_load_dwordx4 %0, %1, off sc0 sc1" : "=v"(pv[mi][ni]) : "v"(src + (mi * NI + ni) * 256) : "memory");
      BAR();
      asm volatile("s_waitcnt vmcnt(0)" ::: "memory");
      SBAR();
#pragma unroll
      for (int ni = 0; ni < NI; ++ni)
#pragma unroll
        for (int mi = 0; mi < MI; ++mi) {
          asm volatile("" : "+v"(pv[mi][ni]));
          const f32x4 a = acc[mi][ni] + pv[mi][ni];
          u32x2 w;
          w[0] = pack2(gv[ni][0] * rbf(a[0]), gv[ni][1] * rbf(a[1]));
          w[1] = pack2(gv[ni][2] * rbf(a[2]), gv[ni][3] * rbf(a[3]));
          *(LAS u32x2*)(smem + (mi * 16 + fr) * 272 + wn * 128 + ni * 32 + fq * 8) = w;
        }
    }
    WAIT_L0();
    BAR();
#pragma unroll
    for (int i = 0; i < 4; ++i) {
      const u32x4 v = *(const LAS u32x4*)(smem + (i * 16 + (tid >> 4)) * 272 + (tid & 15) * 16);
      u32x4 w;
#pragma unroll
      for (int j = 0; j < 4; ++j) w[j] = pack2(lo_f(rv[i][j]) + lo_f(v[j]), hi_f(rv[i][j]) + hi_f(v[j]));
      ST16(p.out + (size_t)(m0 + kh * 64 + i * 16 + (tid >> 4)) * D_ + tn * BN + (tid & 15) * 8, w);
    }
  } else if constexpr (MODE == 1 || MODE == 4) {
    BAR();
#pragma unroll
    for (int ni = 0; ni < NI; ++ni) {
#pragma unroll
      for (int mi = 0; mi < MI; ++mi) {
        u32x2 w;
        w[0] = pack2(gv[ni][0] * rbf(acc[mi][ni][0]), gv[ni][1] * rbf(acc[mi][ni][1]));
        w[1] = pack2(gv[ni][2] * rbf(acc[mi][ni][2]), gv[ni][3] * rbf(acc[mi][ni][3]));
        *(LAS u32x2*)(smem + (wm * TM + mi * 16 + fr) * 144 + wn * 64 + ni * 32 + fq * 8) = w;
      }
    }
    WAIT_L0();
    BAR();
    u32x4 xw[4];
#pragma unroll
    for (int i = 0; i < 4; ++i) {
      const u32x4 v = *(const LAS u32x4*)(smem + (i * 32 + (tid >> 3)) * 144 + (tid & 7) * 16);
#pragma unroll
      for (int j = 0; j < 4; ++j) xw[i][j] = pack2(lo_f(rv[i][j]) + lo_f(v[j]), hi_f(rv[i][j]) + hi_f(v[j]));
      ST16(p.out + (size_t)(m0 + i * 32 + (tid >> 3)) * D_ + tn * BN + (tid & 7) * 8, xw[i]);
    }
  } else {
    BAR();
    char* st = smem + wid * (64 * 144);
#pragma unroll
    for (int ni = 0; ni < 4; ++ni)
#pragma unroll
      for (int mi = 0; mi < MI; ++mi) {
        float o[4];
#pragma unroll
        for (int r = 0; r < 4; ++r) o[r] = rbf(acc[mi][ni][r]) * rbf(acc[mi][ni + 4][r]);
        u32x2 w;
        w[0] = pack2(o[0], o[1]);
        w[1] = pack2(o[2], o[3]);
        *(LAS u32x2*)(st + (mi * 16 + fr) * 144 + ni * 32 + fq * 8) = w;
      }
    u16* ob = p.out + (size_t)(m0 + wm * TM + (lane >> 3)) * F_ + tn * 128 + wn * 64 + (lane & 7) * 8;
#pragma unroll
    for (int i = 0; i < 8; ++i) {
      const u32x4 v = *(const LAS u32x4*)(st + (i * 8 + (lane >> 3)) * 144 + (lane & 7) * 16);
      ST16(ob + (size_t)i * 8 * F_, v);
    }
  }
  STAMP_END(MODE == 0 ? 2 : MODE == 1 ? 4 : MODE == 2 ? 6 : 7);
}

#define AT_NSTAGE 6
constexpr int AT_KT = 128, AT_NS = AT_NSTAGE, AT_ST = 16384;
#define MFMA32(a, b, c) __builtin_amdgcn_mfma_f32_32x32x16_bf16(a, b, c, 0, 0, 0)
#define AEXP(x) __builtin_amdgcn_exp2f(x)
#define MX3(a, b, c) __builtin_fmaxf(__builtin_fmaxf((a), (b)), (c))

__device__ __forceinline__ float tile_rowmax(const f32x16& s0, const f32x16& s1) {
  float a = MX3(s0[0], s0[1], s1[0]), b = MX3(s0[2], s0[3], s1[1]);
  a = MX3(a, s1[2], s1[3]);
#pragma unroll
  for (int r = 4; r < 16; r += 4) {
    a = MX3(a, s0[r], s0[r + 1]); b = MX3(b, s0[r + 2], s0[r + 3]);
    a = MX3(a, s1[r], s1[r + 1]); b = MX3(b, s1[r + 2], s1[r + 3]);
  }
  float mx = __builtin_fmaxf(a, b);
  auto rr = __builtin_amdgcn_permlane32_swap(__float_as_uint(mx), __float_as_uint(mx), false, false);
  return __builtin_fmaxf(__uint_as_float(rr[0]), __uint_as_float(rr[1]));
}

__global__ __launch_bounds__(512, 2) void attn_kernel(const u16* __restrict__ qkv, u16* __restrict__ o) {
  extern __shared__ __attribute__((aligned(16))) char smem[];
  STAMP_BEGIN();
  const int tid = threadIdx.x, lane = tid & 63;
  const int wid = __builtin_amdgcn_readfirstlane(tid >> 6);
  const int x = blockIdx.x & 7, idx = blockIdx.x >> 3;
  const int hd = x * 4 + (idx >> 3), qb = idx & 7;
  const int r32 = lane & 31, hi = lane >> 5;
  const u16* Q = qkv + ((size_t)(0 * 32 + hd) * S_) * 32;
  const u16* Kp = qkv + ((size_t)(1 * 32 + hd) * S_) * 32;
  const u16* Vp = qkv + ((size_t)(2 * 32 + hd) * S_) * 32;
  const int q0 = qb * 256 + wid * 32;

  const unsigned k_voff = (unsigned)(((lane >> 2) * 32 + 8 * ((lane & 3) ^ ((lane >> 4) & 3))) * 2);
  const unsigned v_voff = (unsigned)(lane * 16);
  const unsigned lds0 = (unsigned)(size_t)(LAS char*)smem;
  auto issue = [&](int t, int bufoff) {
    const char* gk = (const char*)(Kp + ((size_t)t * AT_KT + wid * 16) * 32);
    const char* gv = (const char*)(Vp + ((size_t)t * AT_KT + wid * 16) * 32);
    glds16(gk, k_voff, lds0 + bufoff + wid * 1024);
    glds16(gv, v_voff, lds0 + bufoff + 8192 + wid * 1024);
  };
#pragma unroll
  for (int s = 0; s < AT_NS - 1; ++s) issue(s, s * AT_ST);
  const bf16x8 qf0 = *(const bf16x8*)(Q + (size_t)(q0 + r32) * 32 + hi * 8);
  const bf16x8 qf1 = *(const bf16x8*)(Q + (size_t)(q0 + r32) * 32 + 16 + hi * 8);

  const int k_rd0 = r32 * 64 + ((hi ^ ((r32 >> 2) & 3)) << 4);
  const int k_rd1 = k_rd0 ^ 32;
  const int v_rd = 8192 + (4 * hi + ((lane & 15) >> 2)) * 64 + ((lane >> 4) & 1) * 32 + (lane & 3) * 8;

  f32x16 oacc = {};
  f32x16 negm;
  float m, l = 0.f;
  f32x16 sa0, sa1, sb0, sb1;
  u32x4 pp0 = {}, pp1 = {}, pp2 = {}, pp3 = {};
  bf16x8 vf0, vf1, vf2, vf3;
#define AT_BIG 1.0e30f
  constexpr float BIG = AT_BIG;
  constexpr int NT = S_ / AT_KT;

#define SCORES(D0, D1, kb, CIN)                                                         \
  do {                                                                                  \
    bf16x8 k00 = lds_read16((kb) + k_rd0), k01 = lds_read16((kb) + k_rd1);              \
    bf16x8 k10 = lds_read16((kb) + 2048 + k_rd0), k11 = lds_read16((kb) + 2048 + k_rd1);\
    D0 = MFMA32(k00, qf0, CIN); D1 = MFMA32(k10, qf0, CIN);                             \
    D0 = MFMA32(k01, qf1, D0);  D1 = MFMA32(k11, qf1, D1);                              \
  } while (0)
#define VFRAG(vb, s) __builtin_shufflevector(tr_read((vb) + (s) * 1024), tr_read((vb) + (s) * 1024 + 512), 0, 1, 2, 3, 4, 5, 6, 7)
#define READV(vb) do { vf0 = VFRAG(vb, 0); vf1 = VFRAG(vb, 1); vf2 = VFRAG(vb, 2); vf3 = VFRAG(vb, 3); } while (0)
#define PV()                                                                            \
  do {                                                                                  \
    oacc = MFMA32(vf0, __builtin_bit_cast(bf16x8, pp0), oacc);                          \
    oacc = MFMA32(vf1, __builtin_bit_cast(bf16x8, pp1), oacc);                          \
    oacc = MFMA32(vf2, __builtin_bit_cast(bf16x8, pp2), oacc);                          \
    oacc = MFMA32(vf3, __builtin_bit_cast(bf16x8, pp3), oacc);                          \
  } while (0)
#define EXPSUM(C0, C1, LS)                                                              \
  do {                                                                                  \
    f32x2 ls2_ = {0.f, 0.f}, ls3_ = {0.f, 0.f};                                         \
    _Pragma("unroll") for (int r = 0; r < 16; r += 2) {                                 \
      C0[r] = AEXP(C0[r]); C0[r + 1] = AEXP(C0[r + 1]);                                 \
      C1[r] = AEXP(C1[r]); C1[r + 1] = AEXP(C1[r + 1]);                                 \
      ls2_ += (f32x2){C0[r], C0[r + 1]};                                                \
      ls3_ += (f32x2){C1[r], C1[r + 1]};                                                \
    }                                                                                   \
    ls2_ += ls3_;                                                                       \
    LS = ls2_[0] + ls2_[1];                                                             \
  } while (0)
#define PACK(C0, C1)                                                                    \
  do {                                                                                  \
    _Pragma("unroll") for (int j = 0; j < 4; ++j) {                                     \
      pp0[j] = pack2(C0[2 * j], C0[2 * j + 1]);                                         \
      pp1[j] = pack2(C0[8 + 2 * j], C0[8 + 2 * j + 1]);                                 \
      pp2[j] = pack2(C1[2 * j], C1[2 * j + 1]);                                         \
      pp3[j] = pack2(C1[8 + 2 * j], C1[8 + 2 * j + 1]);                                 \
    }                                                                                   \
  } while (0)
#define STEP(C0, C1, N0, N1, kcur, knext, vprev, HASN)                                  \
  do {                                                                                  \
    READV(vprev);                                                                       \
    if (HASN) SCORES(N0, N1, knext, negm);                                              \
    PV();                                                                               \
    float ls;                                                                           \
    EXPSUM(C0, C1, ls);                                                                 \
    if (__builtin_expect(__any(!(ls <= BIG)), 0)) {                                     \
                  \
      f32x16 z_ = {};                                                                   \
      SCORES(C0, C1, kcur, z_);                                                         \
      const float mn = __builtin_fmaxf(m, tile_rowmax(C0, C1));                         \
      const float dm = m - mn;                                                          \
      const float f = __builtin_amdgcn_exp2f(dm);                                       \
      m = mn; l *= f;                                                                   \
      _Pragma("unroll") for (int r = 0; r < 16; ++r) {                                  \
        oacc[r] *= f; negm[r] = -mn; C0[r] -= mn; C1[r] -= mn;                          \
        if (HASN) { N0[r] += dm; N1[r] += dm; }                                         \
      }                                                                                 \
      EXPSUM(C0, C1, ls);                                                               \
    }                                                                                   \
    l += ls;                                                                            \
    PACK(C0, C1);                                                                       \
  } while (0)

  WAIT_V(2 * (AT_NS - 2));
  BAR();
  {
    f32x16 z = {};
    SCORES(sa0, sa1, smem, z);
    m = tile_rowmax(sa0, sa1);
#pragma unroll
    for (int r = 0; r < 16; ++r) { negm[r] = -m; sa0[r] -= m; sa1[r] -= m; }
  }
  int cur = 0, prv = 0;
  for (int t = 0; t < NT - 1; ++t) {
    const char* sb = smem + cur;
    int nx = cur + AT_ST; if (nx >= AT_NS * AT_ST) nx = 0;
    int prv2 = cur - AT_ST; if (prv2 < 0) prv2 = (AT_NS - 1) * AT_ST;
    STEP(sa0, sa1, sb0, sb1, sb, sb + 4096, smem + prv + 4096 + v_rd, true);
    if (t + AT_NS - 1 < NT) {
      WAIT_V(2 * (AT_NS - 3));
      WAIT_L0();
      BAR();
      issue(t + AT_NS - 1, prv2);
    } else {
      WAIT_V(0);
      WAIT_L0();
      BAR();
    }
    STEP(sb0, sb1, sa0, sa1, sb + 4096, smem + nx, sb + v_rd, true);
    prv = cur; cur = nx;
  }
  {
    const char* sb = smem + cur;
    STEP(sa0, sa1, sb0, sb1, sb, sb + 4096, smem + prv + 4096 + v_rd, true);
    STEP(sb0, sb1, sa0, sa1, sb + 4096, sb, sb + v_rd, false);
    prv = cur;
  }
  READV(smem + prv + 4096 + v_rd);
  PV();
#undef SCORES
#undef VFRAG
#undef READV
#undef PV
#undef EXPSUM
#undef PACK
#undef STEP
  {
    auto rr = __builtin_amdgcn_permlane32_swap(__float_as_uint(l), __float_as_uint(l), false, false);
    l = __uint_as_float(rr[0]) + __uint_as_float(rr[1]);
  }
  const float inv = 1.0f / l;
  u16* op = o + (size_t)(q0 + r32) * D_ + hd * 32 + 4 * hi;
#pragma unroll
  for (int g = 0; g < 4; ++g) {
    u32x2 w;
    w[0] = pack2(oacc[4 * g] * inv, oacc[4 * g + 1] * inv);
    w[1] = pack2(oacc[4 * g + 2] * inv, oacc[4 * g + 3] * inv);
    *(u32x2*)(op + 8 * g) = w;
  }
  STAMP_END(3);
}

constexpr int A2_KT = 256, A2_NS = 3, A2_ST = 32768, A2_MOD = A2_NS * A2_ST + 1024, A2_RED = A2_MOD + 32768, A2_FLG = A2_RED + 4096, A2_LDS = A2_FLG + 64;
template <bool SAFE>
__device__ __forceinline__ void attn_pass(char* smem, const u16* Kp, const u16* Vp, bf16x8 qf0, bf16x8 qf1, int wid, int lane,
                                          f32x16& oacc, float& m, float& l,
                                          const u16* pw0, const u16* pw1, const u16* pw2, const u16* pw3, const u16* wm, unsigned cc) {
  const int wk = wid >> 3, r32 = lane & 31, hi = lane >> 5;
  const unsigned k_voff = (unsigned)(((lane >> 2) * 32 + 8 * ((lane & 3) ^ ((lane >> 4) & 3))) * 2);
  const unsigned v_voff = (unsigned)(lane * 16);
  const unsigned lds0 = (unsigned)(size_t)(LAS char*)smem;
  auto issue = [&](int t, int bufoff) {
    const char* gk = (const char*)(Kp + ((size_t)t * A2_KT + wid * 16) * 32);
    const char* gv = (const char*)(Vp + ((size_t)t * A2_KT + wid * 16) * 32);
    glds16(gk, k_voff, lds0 + bufoff + wid * 1024);
    glds16(gv, v_voff, lds0 + bufoff + 16384 + wid * 1024);
  };
  issue(0, 0);
  issue(1, A2_ST);
  const int k_rd0 = wk * 8192 + r32 * 64 + ((hi ^ ((r32 >> 2) & 3)) << 4);
  const int k_rd1 = k_rd0 ^ 32;
  const int v_rd = 16384 + wk * 8192 + (4 * hi + ((lane & 15) >> 2)) * 64 + ((lane >> 4) & 1) * 32 + (lane & 3) * 8;
  f32x16 negm = {};
  f32x2 lsa = {0.f, 0.f}, lsb = {0.f, 0.f};
  oacc = (f32x16){};
  m = 0.f; l = 0.f;
  constexpr int NT = S_ / A2_KT;
  int cur = 0;
  for (int t = 0; t < NT; ++t) {
    if (t == 0) WAIT_V(2); else if (t + 1 < NT) WAIT_V(3); else WAIT_V(0);
    BAR();
    {
      int ck = (blockIdx.x * NT + t) * 16 + wid;
      if (ck >= 2048 + 3 * 7168) ck -= 16384;
      const u16* pb = pw0;
      if (ck >= 2048) { pb = pw1; ck -= 2048; if (ck >= 7168) { pb = pw2; ck -= 7168; if (ck >= 7168) { pb = pw3; ck -= 7168; } } }
      glds16((const char*)pb + (size_t)ck * 1024, v_voff, lds0 + A2_NS * A2_ST);
      int nb = cur + 2 * A2_ST; if (nb >= A2_NS * A2_ST) nb -= A2_NS * A2_ST;
      if (t + 2 < NT) issue(t + 2, nb);
    }
#pragma unroll
    for (int hb = 0; hb < 2; ++hb) {
      const char* kb = smem + cur + hb * 4096;
      const bf16x8 k00 = lds_read16(kb + k_rd0), k01 = lds_read16(kb + k_rd1);
      const bf16x8 k10 = lds_read16(kb + 2048 + k_rd0), k11 = lds_read16(kb + 2048 + k_rd1);
      f32x16 s0, s1;
      if (SAFE || (t == 0 && hb == 0)) {
        f32x16 z_ = {};
        s0 = MFMA32(k00, qf0, z_); s1 = MFMA32(k10, qf0, z_);
        s0 = MFMA32(k01, qf1, s0); s1 = MFMA32(k11, qf1, s1);
        const float tm = tile_rowmax(s0, s1);
        const bool first = (t == 0 && hb == 0);
        const float mn = first ? tm : __builtin_fmaxf(m, tm);
        if (SAFE) {
          const float f = first ? 0.f : __builtin_amdgcn_exp2f(m - mn);
          lsa *= f; lsb *= f;
          oacc *= f;
        }
        m = mn;
#pragma unroll
        for (int r = 0; r < 16; ++r) { negm[r] = -mn; s0[r] -= mn; s1[r] -= mn; }
      } else {
        s0 = MFMA32(k00, qf0, negm); s1 = MFMA32(k10, qf0, negm);
        s0 = MFMA32(k01, qf1, s0); s1 = MFMA32(k11, qf1, s1);
      }
#pragma unroll
      for (int r = 0; r < 16; r += 2) {
        s0[r] = AEXP(s0[r]); s0[r + 1] = AEXP(s0[r + 1]);
        s1[r] = AEXP(s1[r]); s1[r + 1] = AEXP(s1[r + 1]);
        lsa += (f32x2){s0[r], s0[r + 1]};
        lsb += (f32x2){s1[r], s1[r + 1]};
      }
      u32x4 p0, p1, p2, p3;
#pragma unroll
      for (int j = 0; j < 4; ++j) {
        p0[j] = pack2(s0[2 * j], s0[2 * j + 1]);
        p1[j] = pack2(s0[8 + 2 * j], s0[8 + 2 * j + 1]);
        p2[j] = pack2(s1[2 * j], s1[2 * j + 1]);
        p3[j] = pack2(s1[8 + 2 * j], s1[8 + 2 * j + 1]);
      }
      const char* vb = smem + cur + hb * 4096 + v_rd;
#define VFRAG(s) __builtin_shufflevector(tr_read(vb + (s) * 1024), tr_read(vb + (s) * 1024 + 512), 0, 1, 2, 3, 4, 5, 6, 7)
      oacc = MFMA32(VFRAG(0), __builtin_bit_cast(bf16x8, p0), oacc);
      oacc = MFMA32(VFRAG(1), __builtin_bit_cast(bf16x8, p1), oacc);
      oacc = MFMA32(VFRAG(2), __builtin_bit_cast(bf16x8, p2), oacc);
      oacc = MFMA32(VFRAG(3), __builtin_bit_cast(bf16x8, p3), oacc);
#undef VFRAG
    }
    WAIT_L0();
    cur += A2_ST; if (cur >= A2_NS * A2_ST) cur = 0;
  }
  lsa += lsb;
  l = lsa[0] + lsa[1];
  {
    auto rr = __builtin_amdgcn_permlane32_swap(__float_as_uint(l), __float_as_uint(l), false, false);
    l = __uint_as_float(rr[0]) + __uint_as_float(rr[1]);
  }
  WAIT_V(0);
}

__global__ __launch_bounds__(1024, 4) void attn2_kernel(const u16* __restrict__ qkv, u16* __restrict__ o,
                                                        const u16* pw0, const u16* pw1, const u16* pw2, const u16* pw3,
                                                        const u16* __restrict__ cvec, const u16* wmod, float* __restrict__ modp) {
  extern __shared__ __attribute__((aligned(16))) char smem[];
  STAMP_BEGIN();
  const int tid = threadIdx.x, lane = tid & 63;
  const int wid = __builtin_amdgcn_readfirstlane(tid >> 6);
  const int wq = wid & 7, wk = wid >> 3;
  const int x = blockIdx.x & 7, idx = blockIdx.x >> 3;
  const int hd = x * 4 + (idx >> 3), qb = idx & 7;
  const int r32 = lane & 31, hi = lane >> 5;
  const u16* Q = qkv + ((size_t)(0 * 32 + hd) * S_) * 32;
  const u16* Kp = qkv + ((size_t)(1 * 32 + hd) * S_) * 32;
  const u16* Vp = qkv + ((size_t)(2 * 32 + hd) * S_) * 32;
  const int q0 = qb * 256 + wq * 32;
  const bf16x8 qf0 = *(const bf16x8*)(Q + (size_t)(q0 + r32) * 32 + hi * 8);
  const bf16x8 qf1 = *(const bf16x8*)(Q + (size_t)(q0 + r32) * 32 + 16 + hi * 8);

  f32x16 oacc;
  float m, l;
  const int mcg = 32 + (blockIdx.x >> 2), mks = blockIdx.x & 3;
  const u16* wm = wmod + (size_t)(mks * 256) * 6144 + mcg * 64;
  unsigned cc = *(const unsigned*)(cvec + mks * 256 + 2 * (tid >> 3));
  asm volatile("" : "+v"(cc));
  attn_pass<false>(smem, Kp, Vp, qf0, qf1, wid, lane, oacc, m, l, pw0, pw1, pw2, pw3, wm, cc);
  float* xb = (float*)smem + wq * (20 * 64);
  const float* mred = (const float*)(smem + A2_RED);
  volatile LAS int* badf = (volatile LAS int*)(smem + A2_FLG);
  {
    float chk = l;
#pragma unroll
    for (int r = 0; r < 16; ++r) chk += __builtin_fabsf(oacc[r]);
    const int bad = __any(!(chk < 1.0e37f));
    if (lane == 0) badf[wid] = bad;
    __syncthreads();
    const int anybad = __any(badf[lane & 15] != 0);
    if (anybad) {
      __syncthreads();
      attn_pass<true>(smem, Kp, Vp, qf0, qf1, wid, lane, oacc, m, l, pw0, pw1, pw2, pw3, wm, cc);
      __syncthreads();
    }
  }
  xb[wk * 64 + lane] = m; xb[(2 + wk) * 64 + lane] = l;
#pragma unroll
  for (int r = 0; r < 8; ++r) xb[(4 + 8 * wk + r) * 64 + lane] = wk ? oacc[r] : oacc[8 + r];
  __syncthreads();
  {
    const float m2 = xb[(wk ^ 1) * 64 + lane], l2 = xb[(2 + (wk ^ 1)) * 64 + lane];
    const float mn = __builtin_fmaxf(m, m2);
    const float f1 = __builtin_amdgcn_exp2f((m - mn) * 0.5f), f2 = __builtin_amdgcn_exp2f((m2 - mn) * 0.5f);
    const float inv = 1.0f / ((l * f1) * f1 + (l2 * f2) * f2);
    u16* op = o + ((size_t)hd * S_ + q0 + r32) * 32 + 4 * hi + 16 * wk;
#pragma unroll
    for (int g = 0; g < 2; ++g) {
      float v[4];
#pragma unroll
      for (int e = 0; e < 4; ++e) {
        const float own = wk ? oacc[8 + 4 * g + e] : oacc[4 * g + e];
        v[e] = ((own * f1) * f1 + (xb[(4 + 8 * (wk ^ 1) + 4 * g + e) * 64 + lane] * f2) * f2) * inv;
      }
      u32x2 w;
      w[0] = pack2(v[0], v[1]);
      w[1] = pack2(v[2], v[3]);
      *(u32x2*)(op + 8 * g) = w;
    }
  }
  STAMP_END(3);
}

template <int MODE> static void launch_gemm(const GemmP& p, hipStream_t stream) {
  using C = GCfg<MODE>;
  constexpr int lds0_ = C::NS * ((C::AD ? 0 : C::BM) + C::BN) * 128, lds = lds0_ < 20480 ? 20480 : lds0_;
  hipFuncSetAttribute((const void*)gemm_kernel<MODE>, hipFuncAttributeMaxDynamicSharedMemorySize, lds);
  gemm_kernel<MODE><<<C::NTM * C::NTN, C::WM * C::WN * 64 * (C::LW ? 2 : 1), lds, stream>>>(p);
}

extern "C" void kernel_launch(void* const* d_in, const int* in_sizes, int n_in, void* d_out, int out_size,
                              void* d_ws, size_t ws_size, hipStream_t stream) {
  const u16* x = (const u16*)d_in[0];
  const u16* wqkv = (const u16*)d_in[1];
  const u16* wo = (const u16*)d_in[2];
  const u16* wg = (const u16*)d_in[4];
  const u16* wu = (const u16*)d_in[5];
  const u16* wd = (const u16*)d_in[6];
  const u16* c = (const u16*)d_in[7];
  const u16* wmod = (const u16*)d_in[8];
  u16* out = (u16*)d_out;

  char* ws = (char*)d_ws;
  float* mod = (float*)ws;
  u16* h = (u16*)(ws + (1 << 20));
  u16* qkv = (u16*)(ws + (8 << 20));
  u16* ao = (u16*)(ws + (24 << 20));
  u16* x1 = (u16*)(ws + (32 << 20));
  u16* hh = (u16*)(ws + (40 << 20));
  float* slab = (float*)(ws + (64 << 20));
  unsigned* flags = (unsigned*)(ws + (512 << 10));

  mod_kernel<<<256, 256, 0, stream>>>(c, wmod, mod, x, wqkv, flags);
  ln_kernel<<<512 + 192 + 256, 256, 0, stream>>>(x, mod, 0, h, wqkv, c, wmod, mod, 1);
  { GemmP p{h, wqkv, nullptr, nullptr, nullptr, qkv, nullptr, nullptr, nullptr}; launch_gemm<0>(p, stream); }
  hipFuncSetAttribute((const void*)attn2_kernel, hipFuncAttributeMaxDynamicSharedMemorySize, A2_LDS);
  attn2_kernel<<<256, 1024, A2_LDS, stream>>>(qkv, ao, wo, wg, wu, wd, c, wmod, mod);
  { GemmP p{ao, wo, nullptr, x, mod + 2 * 1024, x1, slab, flags, h}; launch_gemm<1>(p, stream);
  }
  ln_kernel<<<512, 256, 0, stream>>>(x1, mod, 3 * 1024, h, wqkv, c, wmod, mod, 5);
  { GemmP p{h, wg, wu, nullptr, nullptr, hh, nullptr, nullptr, nullptr}; launch_gemm<2>(p, stream); }
  { GemmP p{hh, wd, nullptr, x1, mod + 5 * 1024, out, slab, flags, nullptr}; launch_gemm<4>(p, stream); }
}
```

```cpp
#define MOD_IN_ATTN
#include <hip/hip_runtime.h>
#include <hip/hip_bf16.h>
#include <stdint.h>
#define NO_LN2_FUSE

typedef unsigned short u16;
typedef short bf16x8 __attribute__((ext_vector_type(8)));
typedef short s16x4 __attribute__((ext_vector_type(4)));
typedef float f32x2 __attribute__((ext_vector_type(2)));
typedef float f32x4 __attribute__((ext_vector_type(4)));
typedef float f32x16 __attribute__((ext_vector_type(16)));
typedef unsigned u32x2 __attribute__((ext_vector_type(2)));
typedef unsigned u32x4 __attribute__((ext_vector_type(4)));
typedef __bf16 bf16x2_t __attribute__((ext_vector_type(2)));
#define LAS __attribute__((address_space(3)))
#define GAS __attribute__((address_space(1)))

constexpr int S_ = 2048, D_ = 1024, H_ = 32, DH_ = 32, F_ = 3584;

__device__ __forceinline__ float bf2f(u16 u) { return __uint_as_float((unsigned)u << 16); }
__device__ __forceinline__ unsigned pack2(float lo, float hi) {
  f32x2 v = {lo, hi};
  bf16x2_t b = __builtin_convertvector(v, bf16x2_t);
  return __builtin_bit_cast(unsigned, b);
}
__device__ __forceinline__ float rbf(float f) {
  __bf16 b = (__bf16)f;
  return __uint_as_float((unsigned)__builtin_bit_cast(u16, b) << 16);
}
__device__ __forceinline__ float lo_f(unsigned u) { return __uint_as_float(u << 16); }
__device__ __forceinline__ float hi_f(unsigned u) { return __uint_as_float(u & 0xffff0000u); }

#define STAMP_BEGIN()
#define STAMP_END(kid)
#define STAMP_MID(kid, i)
#define WAIT_V(n) asm volatile("s_waitcnt vmcnt(%0)" ::"n"(n) : "memory")
#define WAIT_L0() asm volatile("s_waitcnt lgkmcnt(0)" ::: "memory")
#define BAR() __builtin_amdgcn_s_barrier()

__device__ __forceinline__ void glds16(const void* sbase, unsigned voff, unsigned ldsaddr) {
  asm volatile("s_mov_b32 m0, %2\n\ts_nop 0\n\tglobal_load_lds_dwordx4 %0, %1" ::"v"(voff), "s"(sbase), "s"(ldsaddr) : "memory");
}
#define MFMA16(c, a, b) c = __builtin_amdgcn_mfma_f32_16x16x32_bf16(a, b, c, 0, 0, 0)
#define ST16(p, v) asm volatile("global_store_dwordx4 %0, %1, off sc0 sc1\n\ts_nop 1" ::"v"(p), "v"(v) : "memory")
#define SBAR() __builtin_amdgcn_sched_barrier(0)
#define SGB(mask, n) __builtin_amdgcn_sched_group_barrier(mask, n, 0)
template <int I> struct IC { static constexpr int value = I; };
template <int I, int N, class F> __device__ __forceinline__ void sfor(F&& f) {
  if constexpr (I < N) { f(IC<I>{}); sfor<I + 1, N>(f); }
}
__device__ __forceinline__ s16x4 tr_read(const char* p) {
  return __builtin_amdgcn_ds_read_tr16_b64_v4i16((LAS s16x4*)p);
}
__device__ __forceinline__ bf16x8 lds_read16(const char* p) { return *(const LAS bf16x8*)p; }

__device__ __forceinline__ void mod_block(const u16* __restrict__ c, const u16* __restrict__ wmod, float* __restrict__ modp,
                                          int cg, int ks, float* sh) {
  const int tid = threadIdx.x;
  const int cl = tid & 7, rg = tid >> 3;
  const int col0 = cg * 64 + cl * 8;
  u32x4 w[8];
#pragma unroll
  for (int i = 0; i < 8; ++i)
    w[i] = __builtin_nontemporal_load((const u32x4*)(wmod + (size_t)(ks * 256 + rg + i * 32) * 6144 + col0));
  {
    const float v = bf2f(c[ks * 256 + tid]);
    sh[tid] = rbf(v / (1.0f + __expf(-v)));
  }
  __syncthreads();
  float acc[8];
#pragma unroll
  for (int j = 0; j < 8; ++j) acc[j] = 0.f;
#pragma unroll
  for (int i = 0; i < 8; ++i) {
    const float s = sh[rg + i * 32];
#pragma unroll
    for (int j = 0; j < 4; ++j) {
      acc[2 * j] += s * lo_f(w[i][j]);
      acc[2 * j + 1] += s * hi_f(w[i][j]);
    }
  }
  float* red = sh + 256;
#pragma unroll
  for (int j = 0; j < 8; ++j) red[rg * 65 + cl * 8 + j] = acc[j];
  __syncthreads();
  if (tid < 64) {
    float s = 0.f;
#pragma unroll 8
    for (int r = 0; r < 32; ++r) s += red[r * 65 + tid];
    modp[ks * 6144 + cg * 64 + tid] = s;
  }
}
__device__ __forceinline__ void touch_block(const char* src) {
  u32x4 v[8];
#pragma unroll
  for (int i = 0; i < 8; ++i) v[i] = *(const u32x4*)(src + i * 4096 + threadIdx.x * 16);
#pragma unroll
  for (int i = 0; i < 8; ++i) asm volatile("" ::"v"(v[i]));
}
__device__ __forceinline__ f32x4 ld_mod4(const float* modp, int idx) {
  f32x4 a = *(const f32x4*)(modp + idx), b = *(const f32x4*)(modp + 6144 + idx);
  f32x4 c = *(const f32x4*)(modp + 2 * 6144 + idx), d = *(const f32x4*)(modp + 3 * 6144 + idx);
  f32x4 s = (a + b) + (c + d);
  return (f32x4){rbf(s[0]), rbf(s[1]), rbf(s[2]), rbf(s[3])};
}

__global__ __launch_bounds__(256) void mod_kernel(const u16* __restrict__ c, const u16* __restrict__ wmod,
                                                  float* __restrict__ modp, const u16* __restrict__ pf0,
                                                  const u16* __restrict__ pf1, unsigned* flags) {
  __shared__ float sh[256 + 32 * 65];
  STAMP_BEGIN();
  const int b = blockIdx.x;
  if (threadIdx.x == 0 && b < 256) flags[b * 32] = 0u;
  if (b < 128) mod_block(c, wmod, modp, b >> 2, b & 3, sh);
  else if (b < 256) touch_block((const char*)pf0 + (size_t)(b - 128) * 32768);
  else touch_block((const char*)pf1 + (size_t)(b - 256) * 32768);
  STAMP_END(0);
}

__global__ __launch_bounds__(256) void ln_kernel(const u16* __restrict__ x, const float* modp, int moff, u16* __restrict__ h,
                                                 const u16* __restrict__ pf, const u16* __restrict__ c,
                                                 const u16* __restrict__ wmod, float* modp_w, int kid) {
  __shared__ float sh[256 + 32 * 65];
  STAMP_BEGIN();
  const int tid = threadIdx.x;
  if (blockIdx.x >= 512) {
    const int b = blockIdx.x - 512;
    if (b < 192) touch_block((const char*)pf + (size_t)b * 32768);
    else mod_block(c, wmod, modp_w, 32 + ((b - 192) >> 2), b & 3, sh);
    return;
  }
  const int lane = tid & 63, wid = tid >> 6;
  const int row = blockIdx.x * 4 + wid;
  const u16* xr = x + (size_t)row * D_;
  const u32x4 v0 = *(const u32x4*)(xr + lane * 8);
  const u32x4 v1 = *(const u32x4*)(xr + 512 + lane * 8);
  {
    const f32x4 s0 = ld_mod4(modp, moff + tid * 4);
    const f32x4 s1 = ld_mod4(modp, moff + 1024 + tid * 4);
    *(f32x4*)(sh + tid * 4) = s0;
    *(f32x4*)(sh + 1024 + tid * 4) = (f32x4){rbf(1.0f + s1[0]), rbf(1.0f + s1[1]), rbf(1.0f + s1[2]), rbf(1.0f + s1[3])};
  }
  float f[16];
#pragma unroll
  for (int j = 0; j < 4; ++j) {
    f[2 * j] = lo_f(v0[j]); f[2 * j + 1] = hi_f(v0[j]);
    f[8 + 2 * j] = lo_f(v1[j]); f[8 + 2 * j + 1] = hi_f(v1[j]);
  }
  float s = 0.f;
#pragma unroll
  for (int j = 0; j < 16; ++j) s += f[j];
#pragma unroll
  for (int o = 32; o >= 1; o >>= 1) s += __shfl_xor(s, o);
  const float mean = s * (1.0f / D_);
  float q = 0.f;
#pragma unroll
  for (int j = 0; j < 16; ++j) { float d = f[j] - mean; q += d * d; }
#pragma unroll
  for (int o = 32; o >= 1; o >>= 1) q += __shfl_xor(q, o);
  const float mb = rbf(mean);
  const float vb = rbf(q * (1.0f / D_));
  const float sd = rbf(sqrtf(rbf(vb + 1e-5f)));
  const float inv = 1.0f / sd;
  __syncthreads();
  float o[16];
#pragma unroll
  for (int half = 0; half < 2; ++half) {
    const int c0 = half * 512 + lane * 8;
    const f32x4 sa0 = *(const f32x4*)(sh + c0), sa1 = *(const f32x4*)(sh + c0 + 4);
    const f32x4 ca0 = *(const f32x4*)(sh + 1024 + c0), ca1 = *(const f32x4*)(sh + 1024 + c0 + 4);
#pragma unroll
    for (int j = 0; j < 8; ++j) {
      const float shf = j < 4 ? sa0[j & 3] : sa1[j & 3];
      const float sc = j < 4 ? ca0[j & 3] : ca1[j & 3];
      float t = rbf(f[half * 8 + j] - mb);
      t = rbf(t * inv);
      t = rbf(t * sc);
      o[half * 8 + j] = t + shf;
    }
  }
  u16* hr = h + (size_t)row * D_;
  u32x4 w0, w1;
#pragma unroll
  for (int j = 0; j < 4; ++j) {
    w0[j] = pack2(o[2 * j], o[2 * j + 1]);
    w1[j] = pack2(o[8 + 2 * j], o[8 + 2 * j + 1]);
  }
  ST16(hr + lane * 8, w0);
  ST16(hr + 512 + lane * 8, w1);
  STAMP_END(kid);
}

struct GemmP {
  const u16* A; const u16* B0; const u16* B1; const u16* res; const float* gate; u16* out; float* slab; unsigned* flags; u16* out2;
};

#define NS0 3
#define NS1 3
#define NS3 3
#define LW0 1
#define LW1 1
#define LW3 1
#define AD1 0
#define AD4 0
template <int MODE> struct GCfg;
template <> struct GCfg<0> { static constexpr int BM = 128, BN = 192, WM = 2, WN = 2, NS = NS0, K = 1024, LDA = 1024, LDB = 3072, NTM = 16, NTN = 16, RM = 8, RN = 4, LW = LW0, AD = 0; };
template <> struct GCfg<1> { static constexpr int BM = 128, BN = 64, WM = AD1 ? 4 : 2, WN = AD1 ? 1 : 2, NS = NS1, K = 1024, LDA = 1024, LDB = 1024, NTM = 16, NTN = 16, RM = 4, RN = 8, LW = LW1, AD = AD1; };
template <> struct GCfg<2> { static constexpr int BM = 256, BN = 256, WM = 4, WN = 2, NS = 2, K = 1024, LDA = 1024, LDB = 3584, NTM = 8, NTN = 28, RM = 4, RN = 7, LW = 0, AD = 0; };
template <> struct GCfg<3> { static constexpr int BM = 128, BN = 128, WM = 2, WN = 2, NS = NS3, K = 1792, LDA = 3584, LDB = 1024, NTM = 16, NTN = 16, RM = 4, RN = 8, LW = LW3, AD = 0; };

template <> struct GCfg<4> { static constexpr int BM = 128, BN = 64, WM = AD4 ? 4 : 2, WN = AD4 ? 1 : 2, NS = NS3, K = 3584, LDA = 3584, LDB = 1024, NTM = 16, NTN = 16, RM = 4, RN = 8, LW = LW3, AD = AD4; };

template <int MODE>
__global__ __launch_bounds__(GCfg<MODE>::WM* GCfg<MODE>::WN * 64 * (GCfg<MODE>::LW ? 2 : 1), 2) void gemm_kernel(GemmP p) {
  using C = GCfg<MODE>;
  constexpr int BM = C::BM, BN = C::BN, WM = C::WM, WN = C::WN, NS = C::NS, NW = WM * WN;
  constexpr int TM = BM / WM, TN = BN / WN, MI = TM / 16, NI = TN / 16;
  constexpr int AD = C::AD;
  constexpr int A_BYTES = AD ? 0 : BM * 128, B_BYTES = BN * 128, ST_BYTES = A_BYTES + B_BYTES;
  constexpr int GA = AD ? 0 : BM / 8 / NW, GB = BN / 8 / NW, G = GA + GB;
  constexpr int NT = C::K / 64;
  static_assert(BM % (8 * NW) == 0 && BN % (8 * NW) == 0, "stage split");
  static_assert(NT >= NS + 1, "K too small");
  extern __shared__ __attribute__((aligned(16))) char smem[];
  STAMP_BEGIN();

  const int tid = threadIdx.x, lane = tid & 63;
  constexpr bool LW = C::LW != 0;
  const int wid_all = __builtin_amdgcn_readfirstlane(tid >> 6);
  const int wid = wid_all % NW;
  const int wm = wid / WN, wn = wid % WN;
  int tm, tn;
  {
    const int x = blockIdx.x & 7, idx = blockIdx.x >> 3;
    constexpr int NRM = C::NTM / C::RM;
    const int rm = x % NRM, rn = x / NRM;
    tm = rm * C::RM + idx % C::RM;
    tn = rn * C::RN + idx / C::RM;
  }
  int kh = 0;
  if constexpr (MODE == 3) { kh = tn & 1; tn >>= 1; }
  const int m0 = tm * BM;

  const int par = wid & 1;
  const int a_ch = (lane & 7) ^ ((4 * par + (lane >> 4)) & 7);
  const unsigned a_voff = (MODE == 1) ? (unsigned)((((a_ch >> 2) * S_ + (lane >> 3)) * 32 + (a_ch & 3) * 8) * 2)
                                      : (unsigned)(((lane >> 3) * C::LDA + 8 * a_ch) * 2);
  const unsigned b_voff = (unsigned)(((lane >> 3) * C::LDB + 8 * ((lane & 7) ^ ((((lane >> 4) & 1) | (par << 1)) << 1))) * 2);
  const char* Abase = (MODE == 1) ? (const char*)(p.A + (size_t)m0 * 32) : (const char*)(p.A + (size_t)m0 * C::LDA + kh * C::K);
  const unsigned lds0 = (unsigned)(size_t)(LAS char*)smem;

  auto issue1 = [&](auto gi, int t, int bufoff) {
    constexpr int g = decltype(gi)::value;
    if constexpr (g < GA) {
      const int i = wid + NW * g;
      const char* gp = (MODE == 1) ? Abase + ((size_t)(8 * i) * 32 + (size_t)t * 2 * S_ * 32) * 2
                                   : Abase + ((size_t)(8 * i) * C::LDA + (size_t)t * 64) * 2;
      glds16(gp, a_voff, lds0 + bufoff + i * 1024);
    } else {
      const int i = wid + NW * (g - GA);
      const int pn = i >> 3, kr = 8 * (i & 7);
      const u16* bp;
      if constexpr (MODE == 2) bp = (pn < 2 ? p.B0 : p.B1) + tn * 128 + (pn & 1) * 64;
      else bp = p.B0 + (size_t)kh * C::K * C::LDB + tn * BN + pn * 64;
      const char* gp = (const char*)(bp + ((size_t)t * 64 + kr) * C::LDB);
      glds16(gp, b_voff, lds0 + bufoff + A_BYTES + i * 1024);
    }
  };

  if constexpr (C::LW == 1) {
    if (wid_all >= NW) {
      sfor<0, NS>([&](auto sc_) { sfor<0, G>([&](auto gc) { issue1(gc, decltype(sc_)::value, decltype(sc_)::value * ST_BYTES); }); });
      WAIT_V((NS - 1) * G);
      BAR();
      int lb = 0;
      for (int t = 0; t < NT - NS; ++t) {
        WAIT_V((NS - 2) * G);
        BAR();
        sfor<0, G>([&](auto gc) { issue1(gc, t + NS, lb); });
        lb = (lb + ST_BYTES == NS * ST_BYTES) ? 0 : lb + ST_BYTES;
      }
      sfor<0, NS - 1>([&](auto rc) { WAIT_V((NS - 2 - decltype(rc)::value) * G); BAR(); });
      return;
    }
  }
  if constexpr (C::LW == 2) {
    if (wid_all >= NW) {
      static_assert(NS == 3 && (NT & 1) == 0, "reg loader");
      u32x4 R0[G], R1[G];
      auto ld = [&](u32x4* R, int t) {
        sfor<0, G>([&](auto gi) {
          constexpr int g = decltype(gi)::value;
          if constexpr (g < GA) {
            const int i = wid + NW * g;
            R[g] = *(const u32x4*)(Abase + ((size_t)(8 * i) * C::LDA + (size_t)t * 64) * 2 + a_voff);
          } else {
            const int i = wid + NW * (g - GA);
            const int pn = i >> 3, kr = 8 * (i & 7);
            const u16* bp = p.B0 + (size_t)kh * C::K * C::LDB + tn * BN + pn * 64;
            R[g] = *(const u32x4*)((const char*)(bp + ((size_t)t * 64 + kr) * C::LDB) + b_voff);
          }
        });
      };
      auto st = [&](const u32x4* R, int bufoff) {
        sfor<0, G>([&](auto gi) {
          constexpr int g = decltype(gi)::value;
          const int off = g < GA ? (wid + NW * g) * 1024 : A_BYTES + (wid + NW * (g - GA)) * 1024;
          *(LAS u32x4*)(smem + bufoff + off + lane * 16) = R[g];
        });
      };
      ld(R0, 0); ld(R1, 1);
      st(R0, 0); ld(R0, 2);
      WAIT_L0(); BAR();
      st(R1, ST_BYTES); ld(R1, 3);
      st(R0, 2 * ST_BYTES); ld(R0, 4);
      int lb = 0;
      for (int t = 0; t < NT - 4; t += 2) {
        WAIT_L0(); BAR();
        st(R1, lb); if (t + 5 < NT) ld(R1, t + 5);
        lb = (lb + ST_BYTES == NS * ST_BYTES) ? 0 : lb + ST_BYTES;
        WAIT_L0(); BAR();
        st(R0, lb); if (t + 6 < NT) ld(R0, t + 6);
        lb = (lb + ST_BYTES == NS * ST_BYTES) ? 0 : lb + ST_BYTES;
      }
      WAIT_L0(); BAR();
      st(R1, lb);
      WAIT_L0(); BAR();
      BAR();
      return;
    }
  }

  const int fr = lane & 15, fq = lane >> 4;
  u32x4 rv[4];
  f32x4 gv[NI];
  if constexpr (MODE == 1 || MODE == 3 || MODE == 4) {
#pragma unroll
    for (int i = 0; i < 4; ++i) {
      if constexpr (MODE != 3) rv[i] = *(const u32x4*)(p.res + (size_t)(m0 + i * 32 + (tid >> 3)) * D_ + tn * BN + (tid & 7) * 8);
      else rv[i] = *(const u32x4*)(p.res + (size_t)(m0 + kh * 64 + i * 16 + (tid >> 4)) * D_ + tn * BN + (tid & 15) * 8);
    }
#pragma unroll
    for (int ni = 0; ni < NI; ++ni) { gv[ni] = ld_mod4(p.gate, tn * BN + wn * TN + ni * 16 + 4 * fq); asm volatile("" : "+v"(gv[ni])); }
  }
  const int a_rd0 = (wm * TM + fr) * 128 + ((fq ^ (fr >> 1)) << 4);
  const int a_rd1 = a_rd0 ^ 64;
  int b_rd[(MODE == 2) ? 4 : NI];
  {
    const int q = (lane & 15) >> 2, pp = lane & 3;
    const int gl = ((q >> 1) & 1) | ((fq & 1) << 1);
#pragma unroll
    for (int ni = 0; ni < ((MODE == 2) ? 4 : NI); ++ni) {
      int pn, un;
      if constexpr (MODE == 2) { pn = wn; un = ni & 3; }
      else { const int j = wn * NI + ni; pn = j >> 2; un = j & 3; }
      b_rd[ni] = A_BYTES + pn * 8192 + (8 * fq + q) * 128 + ((un ^ gl) << 5) + pp * 8;
    }
  }

  f32x4 acc[MI][NI];
#pragma unroll
  for (int mi = 0; mi < MI; ++mi)
#pragma unroll
    for (int ni = 0; ni < NI; ++ni) acc[mi][ni] = (f32x4){0.f, 0.f, 0.f, 0.f};

  constexpr int NSP = (MODE == 2) ? 2 : 1;
  constexpr int NH = NI / NSP, NM = MI * NH, NPH = 2 * NSP;
  constexpr int GW = (NS >= 3) ? NPH : NPH / 2;
  constexpr int GPP = (G + GW - 1) / GW;
  bf16x8 a0[MI], a1[MI], b0[NH], b1[NH];
  s16x4 blo[NH];

  auto rd_b = [&](auto ri, bf16x8* BF, const char* sb, int ks, int hf) {
    constexpr int r = decltype(ri)::value, ni = r >> 1;
    const int bo = (MODE == 2) ? b_rd[ni] + hf * 16384 : b_rd[ni];
    if constexpr ((r & 1) == 0) blo[ni] = tr_read(sb + bo + ks * 4096);
    else BF[ni] = __builtin_shufflevector(blo[ni], tr_read(sb + bo + ks * 4096 + 512), 0, 1, 2, 3, 4, 5, 6, 7);
  };
  auto rd_a = [&](auto ri, bf16x8* AF, const char* sb, int ks) {
    constexpr int mi = decltype(ri)::value;
    AF[mi] = lds_read16(sb + (ks ? a_rd1 : a_rd0) + mi * 2048);
  };
  auto phase = [&](auto hfc, auto rdm, auto g0c, auto g1c, bf16x8* AF, bf16x8* BF, bf16x8* AN_, bf16x8* BN_,
                   const char* rsb, int rks, int rhf, int gt, int gbuf) {
    constexpr int hf = decltype(hfc)::value, RDM = decltype(rdm)::value;
    constexpr int g0 = decltype(g0c)::value, g1 = decltype(g1c)::value, NG = g1 - g0;
    constexpr int NR = RDM == 0 ? 0 : (RDM == 1 ? 2 * NH : 2 * NH + MI);
    sfor<0, NM>([&](auto ic) {
      constexpr int i = decltype(ic)::value, mi = i / NH, ni = i % NH;
      MFMA16(acc[mi][hf * NH + ni], BF[ni], AF[mi]);
      sfor<0, NR>([&](auto rc) {
        constexpr int r = decltype(rc)::value;
        if constexpr ((r * (NM - NM / 4)) / (NR > 0 ? NR : 1) == i) {
          if constexpr (r < 2 * NH) rd_b(rc, BN_, rsb, rks, rhf);
          else rd_a(IC<r - 2 * NH>{}, AN_, rsb, rks);
        }
      });
      sfor<0, NG>([&](auto gc) {
        constexpr int g = decltype(gc)::value;
        if constexpr ((g * NM) / (NG > 0 ? NG : 1) == i) issue1(IC<g0 + g>{}, gt, gbuf);
      });
      SBAR();
    });
  };
  constexpr auto gmin = [](int a, int b) { return a < b ? a : b; };
  int cur = 0, prv = 0;
  auto ktile = [&](auto vmc, auto restc, auto firstc, int t) {
    constexpr int VM = decltype(vmc)::value;
    constexpr bool REST = decltype(restc)::value && !LW, FIRST = decltype(firstc)::value && !LW;
    const int nxt = (cur + ST_BYTES == NS * ST_BYTES) ? 0 : cur + ST_BYTES;
    const char* sc = smem + cur;
    const char* sn = smem + nxt;
    constexpr int R1 = REST ? gmin(G, GPP) : 0, R2 = REST ? gmin(G, 2 * GPP) : 0, R3 = REST ? gmin(G, 3 * GPP) : 0, R4 = REST ? gmin(G, 4 * GPP) : 0;
    constexpr int F1 = FIRST ? gmin(G, GPP) : 0;
    if constexpr (NSP == 1) {
      phase(IC<0>{}, IC<2>{}, IC<R1>{}, IC<R2>{}, a0, b0, a1, b1, sc, 1, 0, t - 1 + NS, prv);
      if constexpr (VM >= 0) {
        if constexpr (!LW) WAIT_V(VM);
        WAIT_L0(); BAR();
        phase(IC<0>{}, IC<2>{}, IC<0>{}, IC<F1>{}, a1, b1, a0, b0, sn, 0, 0, t + NS, cur);
      } else {
        phase(IC<0>{}, IC<0>{}, IC<0>{}, IC<0>{}, a1, b1, a0, b0, sn, 0, 0, 0, 0);
      }
    } else {
      phase(IC<0>{}, IC<1>{}, IC<R1>{}, IC<R2>{}, a0, b0, a1, b1, sc, 0, 1, t - 1 + NS, prv);
      phase(IC<1>{}, IC<2>{}, IC<R2>{}, IC<R3>{}, a0, b1, a1, b0, sc, 1, 0, t - 1 + NS, prv);
      phase(IC<0>{}, IC<1>{}, IC<R3>{}, IC<R4>{}, a1, b0, a1, b1, sc, 1, 1, t - 1 + NS, prv);
      if constexpr (VM >= 0) {
        WAIT_V(VM); WAIT_L0(); BAR();
        phase(IC<1>{}, IC<2>{}, IC<0>{}, IC<F1>{}, a1, b1, a0, b0, sn, 0, 0, t + NS, cur);
      } else {
        phase(IC<1>{}, IC<0>{}, IC<0>{}, IC<0>{}, a1, b1, a0, b0, sn, 0, 0, 0, 0);
      }
    }
    prv = cur; cur = nxt;
  };

  if constexpr (AD > 0) {
    static_assert(LW && NSP == 1 && NT % AD == 0, "A-direct path");
    bf16x8 ar[AD][2][MI];
    const char* Aw = (MODE == 1) ? Abase + ((size_t)(wm * TM + fr) * 32 + fq * 8) * 2
                                 : Abase + ((size_t)(wm * TM + fr) * C::LDA + fq * 8) * 2;
    auto lda = [&](auto dc, int t) {
      constexpr int d = decltype(dc)::value;
#pragma unroll
      for (int ks = 0; ks < 2; ++ks)
#pragma unroll
        for (int mi = 0; mi < MI; ++mi) {
          if constexpr (MODE == 1) ar[d][ks][mi] = *(const bf16x8*)(Aw + ((size_t)(2 * t + ks) * S_ + mi * 16) * 64);
          else ar[d][ks][mi] = *(const bf16x8*)(Aw + (size_t)mi * 16 * C::LDA * 2 + t * 128 + ks * 64);
        }
    };
    sfor<0, AD>([&](auto dc) { lda(dc, decltype(dc)::value); });
    BAR();
    STAMP_MID(MODE == 0 ? 2 : MODE == 1 ? 4 : MODE == 2 ? 6 : 7, 0);
    sfor<0, 2 * NH>([&](auto rc) { rd_b(rc, b0, smem, 0, 0); });
    auto ktile_ad = [&](auto dc, auto ldc, auto lastc, int t) {
      constexpr int d = decltype(dc)::value;
      const int nxt = (cur + ST_BYTES == NS * ST_BYTES) ? 0 : cur + ST_BYTES;
      phase(IC<0>{}, IC<1>{}, IC<0>{}, IC<0>{}, ar[d][0], b0, a1, b1, smem + cur, 1, 0, 0, 0);
      if constexpr (!decltype(lastc)::value) {
        WAIT_L0(); BAR();
        phase(IC<0>{}, IC<1>{}, IC<0>{}, IC<0>{}, ar[d][1], b1, a0, b0, smem + nxt, 0, 0, 0, 0);
      } else {
        phase(IC<0>{}, IC<0>{}, IC<0>{}, IC<0>{}, ar[d][1], b1, a0, b0, smem + nxt, 0, 0, 0, 0);
      }
      if constexpr (decltype(ldc)::value) { lda(dc, t + AD); SBAR(); }
      cur = nxt;
    };
    for (int t = 0; t < NT - AD; t += AD) sfor<0, AD>([&](auto dc) { ktile_ad(dc, IC<1>{}, IC<0>{}, t + decltype(dc)::value); });
    sfor<0, AD>([&](auto dc) { ktile_ad(dc, IC<0>{}, IC<(decltype(dc)::value == AD - 1)>{}, 0); });
  } else {
  if constexpr (!LW) {
    sfor<0, NS>([&](auto sc_) { sfor<0, G>([&](auto gc) { issue1(gc, decltype(sc_)::value, decltype(sc_)::value * ST_BYTES); }); });
    WAIT_V((NS - 1) * G);
  }
  BAR();
  STAMP_MID(MODE == 0 ? 2 : MODE == 1 ? 4 : MODE == 2 ? 6 : 7, 0);
  sfor<0, 2 * NH>([&](auto rc) { rd_b(rc, b0, smem, 0, 0); });
  sfor<0, MI>([&](auto rc) { rd_a(rc, a0, smem, 0); });
  using TT = IC<1>; using FF = IC<0>;
  ktile(IC<(NS - 2) * G>{}, FF{}, TT{}, 0);
  for (int t = 1; t < NT - NS; ++t) ktile(IC<(NS - 2) * G>{}, TT{}, TT{}, t);
  ktile(IC<(NS - 2) * G>{}, TT{}, FF{}, NT - NS);
  sfor<0, NS - 2>([&](auto rc) { ktile(IC<(NS - 3 - decltype(rc)::value) * G>{}, FF{}, FF{}, 0); });
  ktile(IC<-1>{}, FF{}, FF{}, 0);
  }

  STAMP_MID(MODE == 0 ? 2 : MODE == 1 ? 4 : MODE == 2 ? 6 : 7, 1);
  if constexpr (MODE == 0) {
    const float qs = 0.17677669529663687f * 1.4426950408889634f;
    BAR();
    char* st = smem + wid * (64 * 208);
#pragma unroll
    for (int ni = 0; ni < NI; ++ni) {
      const int n = tn * BN + wn * TN + ni * 16;
      const float sc = n < 1024 ? qs : 1.0f;
#pragma unroll
      for (int mi = 0; mi < MI; ++mi) {
        u32x2 w;
        w[0] = pack2(acc[mi][ni][0] * sc, acc[mi][ni][1] * sc);
        w[1] = pack2(acc[mi][ni][2] * sc, acc[mi][ni][3] * sc);
        *(LAS u32x2*)(st + (mi * 16 + fr) * 208 + ni * 32 + fq * 8) = w;
      }
    }
#pragma unroll
    for (int j = 0; j < 3; ++j) {
      const int hh = (tn * BN + wn * TN + j * 32) >> 5;
      u16* ob = p.out + ((size_t)hh * S_ + m0 + wm * TM) * 32 + lane * 8;
#pragma unroll
      for (int i = 0; i < 4; ++i) {
        const u32x4 v = *(const LAS u32x4*)(st + (i * 16 + (lane >> 2)) * 208 + j * 64 + (lane & 3) * 16);
        ST16(ob + i * 512, v);
      }
    }
  } else if constexpr (MODE == 3) {
    const int tile = tm * 8 + tn;
    if (wm != kh) {
      float* dst = p.slab + ((size_t)(tile * 2 + kh) * 2 + wn) * 4096 + lane * 4;
#pragma unroll
      for (int mi = 0; mi < MI; ++mi)
#pragma unroll
        for (int ni = 0; ni < NI; ++ni) ST16(dst + (mi * NI + ni) * 256, __builtin_bit_cast(u32x4, acc[mi][ni]));
      WAIT_V(0);
      if (lane == 0) __hip_atomic_fetch_add(p.flags + (tile * 2 + kh) * 32, 1u, __ATOMIC_RELAXED, __HIP_MEMORY_SCOPE_AGENT);
      BAR();
    } else {
      const unsigned* fl = p.flags + (tile * 2 + (kh ^ 1)) * 32;
      for (int spin = 0; spin < (1 << 22); ++spin) {
        if (__builtin_amdgcn_readfirstlane(__hip_atomic_load(fl, __ATOMIC_RELAXED, __HIP_MEMORY_SCOPE_AGENT)) >= 2u) break;
        __builtin_amdgcn_s_sleep(1);
      }
      const float* src = p.slab + ((size_t)(tile * 2 + (kh ^ 1)) * 2 + wn) * 4096 + lane * 4;
      f32x4 pv[MI][NI];
#pragma unroll
      for (int mi = 0; mi < MI; ++mi)
#pragma unroll
        for (int ni = 0; ni < NI; ++ni)
          asm volatile("global_load_dwordx4 %0, %1, off sc0 sc1" : "=v"(pv[mi][ni]) : "v"(src + (mi * NI + ni) * 256) : "memory");
      BAR();
      asm volatile("s_waitcnt vmcnt(0)" ::: "memory");
      SBAR();
#pragma unroll
      for (int ni = 0; ni < NI; ++ni)
#pragma unroll
        for (int mi = 0; mi < MI; ++mi) {
          asm volatile("" : "+v"(pv[mi][ni]));
          const f32x4 a = acc[mi][ni] + pv[mi][ni];
          u32x2 w;
          w[0] = pack2(gv[ni][0] * rbf(a[0]), gv[ni][1] * rbf(a[1]));
          w[1] = pack2(gv[ni][2] * rbf(a[2]), gv[ni][3] * rbf(a[3]));
          *(LAS u32x2*)(smem + (mi * 16 + fr) * 272 + wn * 128 + ni * 32 + fq * 8) = w;
        }
    }
    WAIT_L0();
    BAR();
#pragma unroll
    for (int i = 0; i < 4; ++i) {
      const u32x4 v = *(const LAS u32x4*)(smem + (i * 16 + (tid >> 4)) * 272 + (tid & 15) * 16);
      u32x4 w;
#pragma unroll
      for (int j = 0; j < 4; ++j) w[j] = pack2(lo_f(rv[i][j]) + lo_f(v[j]), hi_f(rv[i][j]) + hi_f(v[j]));
      ST16(p.out + (size_t)(m0 + kh * 64 + i * 16 + (tid >> 4)) * D_ + tn * BN + (tid & 15) * 8, w);
    }
  } else if constexpr (MODE == 1 || MODE == 4) {
    BAR();
#pragma unroll
    for (int ni = 0; ni < NI; ++ni) {
#pragma unroll
      for (int mi = 0; mi < MI; ++mi) {
        u32x2 w;
        w[0] = pack2(gv[ni][0] * rbf(acc[mi][ni][0]), gv[ni][1] * rbf(acc[mi][ni][1]));
        w[1] = pack2(gv[ni][2] * rbf(acc[mi][ni][2]), gv[ni][3] * rbf(acc[mi][ni][3]));
        *(LAS u32x2*)(smem + (wm * TM + mi * 16 + fr) * 144 + wn * 64 + ni * 32 + fq * 8) = w;
      }
    }
    WAIT_L0();
    BAR();
    u32x4 xw[4];
#pragma unroll
    for (int i = 0; i < 4; ++i) {
      const u32x4 v = *(const LAS u32x4*)(smem + (i * 32 + (tid >> 3)) * 144 + (tid & 7) * 16);
#pragma unroll
      for (int j = 0; j < 4; ++j) xw[i][j] = pack2(lo_f(rv[i][j]) + lo_f(v[j]), hi_f(rv[i][j]) + hi_f(v[j]));
      ST16(p.out + (size_t)(m0 + i * 32 + (tid >> 3)) * D_ + tn * BN + (tid & 7) * 8, xw[i]);
    }
  } else {
    BAR();
    char* st = smem + wid * (64 * 144);
#pragma unroll
    for (int ni = 0; ni < 4; ++ni)
#pragma unroll
      for (int mi = 0; mi < MI; ++mi) {
        float o[4];
#pragma unroll
        for (int r = 0; r < 4; ++r) o[r] = rbf(acc[mi][ni][r]) * rbf(acc[mi][ni + 4][r]);
        u32x2 w;
        w[0] = pack2(o[0], o[1]);
        w[1] = pack2(o[2], o[3]);
        *(LAS u32x2*)(st + (mi * 16 + fr) * 144 + ni * 32 + fq * 8) = w;
      }
    u16* ob = p.out + (size_t)(m0 + wm * TM + (lane >> 3)) * F_ + tn * 128 + wn * 64 + (lane & 7) * 8;
#pragma unroll
    for (int i = 0; i < 8; ++i) {
      const u32x4 v = *(const LAS u32x4*)(st + (i * 8 + (lane >> 3)) * 144 + (lane & 7) * 16);
      ST16(ob + (size_t)i * 8 * F_, v);
    }
  }
  STAMP_END(MODE == 0 ? 2 : MODE == 1 ? 4 : MODE == 2 ? 6 : 7);
}

#define AT_NSTAGE 6
constexpr int AT_KT = 128, AT_NS = AT_NSTAGE, AT_ST = 16384;
#define MFMA32(a, b, c) __builtin_amdgcn_mfma_f32_32x32x16_bf16(a, b, c, 0, 0, 0)
#define AEXP(x) __builtin_amdgcn_exp2f(x)
#define MX3(a, b, c) __builtin_fmaxf(__builtin_fmaxf((a), (b)), (c))

__device__ __forceinline__ float tile_rowmax(const f32x16& s0, const f32x16& s1) {
  float a = MX3(s0[0], s0[1], s1[0]), b = MX3(s0[2], s0[3], s1[1]);
  a = MX3(a, s1[2], s1[3]);
#pragma unroll
  for (int r = 4; r < 16; r += 4) {
    a = MX3(a, s0[r], s0[r + 1]); b = MX3(b, s0[r + 2], s0[r + 3]);
    a = MX3(a, s1[r], s1[r + 1]); b = MX3(b, s1[r + 2], s1[r + 3]);
  }
  float mx = __builtin_fmaxf(a, b);
  auto rr = __builtin_amdgcn_permlane32_swap(__float_as_uint(mx), __float_as_uint(mx), false, false);
  return __builtin_fmaxf(__uint_as_float(rr[0]), __uint_as_float(rr[1]));
}

__global__ __launch_bounds__(512, 2) void attn_kernel(const u16* __restrict__ qkv, u16* __restrict__ o) {
  extern __shared__ __attribute__((aligned(16))) char smem[];
  STAMP_BEGIN();
  const int tid = threadIdx.x, lane = tid & 63;
  const int wid = __builtin_amdgcn_readfirstlane(tid >> 6);
  const int x = blockIdx.x & 7, idx = blockIdx.x >> 3;
  const int hd = x * 4 + (idx >> 3), qb = idx & 7;
  const int r32 = lane & 31, hi = lane >> 5;
  const u16* Q = qkv + ((size_t)(0 * 32 + hd) * S_) * 32;
  const u16* Kp = qkv + ((size_t)(1 * 32 + hd) * S_) * 32;
  const u16* Vp = qkv + ((size_t)(2 * 32 + hd) * S_) * 32;
  const int q0 = qb * 256 + wid * 32;

  const unsigned k_voff = (unsigned)(((lane >> 2) * 32 + 8 * ((lane & 3) ^ ((lane >> 4) & 3))) * 2);
  const unsigned v_voff = (unsigned)(lane * 16);
  const unsigned lds0 = (unsigned)(size_t)(LAS char*)smem;
  auto issue = [&](int t, int bufoff) {
    const char* gk = (const char*)(Kp + ((size_t)t * AT_KT + wid * 16) * 32);
    const char* gv = (const char*)(Vp + ((size_t)t * AT_KT + wid * 16) * 32);
    glds16(gk, k_voff, lds0 + bufoff + wid * 1024);
    glds16(gv, v_voff, lds0 + bufoff + 8192 + wid * 1024);
  };
#pragma unroll
  for (int s = 0; s < AT_NS - 1; ++s) issue(s, s * AT_ST);
  const bf16x8 qf0 = *(const bf16x8*)(Q + (size_t)(q0 + r32) * 32 + hi * 8);
  const bf16x8 qf1 = *(const bf16x8*)(Q + (size_t)(q0 + r32) * 32 + 16 + hi * 8);

  const int k_rd0 = r32 * 64 + ((hi ^ ((r32 >> 2) & 3)) << 4);
  const int k_rd1 = k_rd0 ^ 32;
  const int v_rd = 8192 + (4 * hi + ((lane & 15) >> 2)) * 64 + ((lane >> 4) & 1) * 32 + (lane & 3) * 8;

  f32x16 oacc = {};
  f32x16 negm;
  float m, l = 0.f;
  f32x16 sa0, sa1, sb0, sb1;
  u32x4 pp0 = {}, pp1 = {}, pp2 = {}, pp3 = {};
  bf16x8 vf0, vf1, vf2, vf3;
#define AT_BIG 1.0e30f
  constexpr float BIG = AT_BIG;
  constexpr int NT = S_ / AT_KT;

#define SCORES(D0, D1, kb, CIN)                                                         \
  do {                                                                                  \
    bf16x8 k00 = lds_read16((kb) + k_rd0), k01 = lds_read16((kb) + k_rd1);              \
    bf16x8 k10 = lds_read16((kb) + 2048 + k_rd0), k11 = lds_read16((kb) + 2048 + k_rd1);\
    D0 = MFMA32(k00, qf0, CIN); D1 = MFMA32(k10, qf0, CIN);                             \
    D0 = MFMA32(k01, qf1, D0);  D1 = MFMA32(k11, qf1, D1);                              \
  } while (0)
#define VFRAG(vb, s) __builtin_shufflevector(tr_read((vb) + (s) * 1024), tr_read((vb) + (s) * 1024 + 512), 0, 1, 2, 3, 4, 5, 6, 7)
#define READV(vb) do { vf0 = VFRAG(vb, 0); vf1 = VFRAG(vb, 1); vf2 = VFRAG(vb, 2); vf3 = VFRAG(vb, 3); } while (0)
#define PV()                                                                            \
  do {                                                                                  \
    oacc = MFMA32(vf0, __builtin_bit_cast(bf16x8, pp0), oacc);                          \
    oacc = MFMA32(vf1, __builtin_bit_cast(bf16x8, pp1), oacc);                          \
    oacc = MFMA32(vf2, __builtin_bit_cast(bf16x8, pp2), oacc);                          \
    oacc = MFMA32(vf3, __builtin_bit_cast(bf16x8, pp3), oacc);                          \
  } while (0)
#define EXPSUM(C0, C1, LS)                                                              \
  do {                                                                                  \
    f32x2 ls2_ = {0.f, 0.f}, ls3_ = {0.f, 0.f};                                         \
    _Pragma("unroll") for (int r = 0; r < 16; r += 2) {                                 \
      C0[r] = AEXP(C0[r]); C0[r + 1] = AEXP(C0[r + 1]);                                 \
      C1[r] = AEXP(C1[r]); C1[r + 1] = AEXP(C1[r + 1]);                                 \
      ls2_ += (f32x2){C0[r], C0[r + 1]};                                                \
      ls3_ += (f32x2){C1[r], C1[r + 1]};                                                \
    }                                                                                   \
    ls2_ += ls3_;                                                                       \
    LS = ls2_[0] + ls2_[1];                                                             \
  } while (0)
#define PACK(C0, C1)                                                                    \
  do {                                                                                  \
    _Pragma("unroll") for (int j = 0; j < 4; ++j) {                                     \
      pp0[j] = pack2(C0[2 * j], C0[2 * j + 1]);                                         \
      pp1[j] = pack2(C0[8 + 2 * j], C0[8 + 2 * j + 1]);                                 \
      pp2[j] = pack2(C1[2 * j], C1[2 * j + 1]);                                         \
      pp3[j] = pack2(C1[8 + 2 * j], C1[8 + 2 * j + 1]);                                 \
    }                                                                                   \
  } while (0)
#define STEP(C0, C1, N0, N1, kcur, knext, vprev, HASN)                                  \
  do {                                                                                  \
    READV(vprev);                                                                       \
    if (HASN) SCORES(N0, N1, knext, negm);                                              \
    PV();                                                                               \
    float ls;                                                                           \
    EXPSUM(C0, C1, ls);                                                                 \
    if (__builtin_expect(__any(!(ls <= BIG)), 0)) {                                     \
                  \
      f32x16 z_ = {};                                                                   \
      SCORES(C0, C1, kcur, z_);                                                         \
      const float mn = __builtin_fmaxf(m, tile_rowmax(C0, C1));                         \
      const float dm = m - mn;                                                          \
      const float f = __builtin_amdgcn_exp2f(dm);                                       \
      m = mn; l *= f;                                                                   \
      _Pragma("unroll") for (int r = 0; r < 16; ++r) {                                  \
        oacc[r] *= f; negm[r] = -mn; C0[r] -= mn; C1[r] -= mn;                          \
        if (HASN) { N0[r] += dm; N1[r] += dm; }                                         \
      }                                                                                 \
      EXPSUM(C0, C1, ls);                                                               \
    }                                                                                   \
    l += ls;                                                                            \
    PACK(C0, C1);                                                                       \
  } while (0)

  WAIT_V(2 * (AT_NS - 2));
  BAR();
  {
    f32x16 z = {};
    SCORES(sa0, sa1, smem, z);
    m = tile_rowmax(sa0, sa1);
#pragma unroll
    for (int r = 0; r < 16; ++r) { negm[r] = -m; sa0[r] -= m; sa1[r] -= m; }
  }
  int cur = 0, prv = 0;
  for (int t = 0; t < NT - 1; ++t) {
    const char* sb = smem + cur;
    int nx = cur + AT_ST; if (nx >= AT_NS * AT_ST) nx = 0;
    int prv2 = cur - AT_ST; if (prv2 < 0) prv2 = (AT_NS - 1) * AT_ST;
    STEP(sa0, sa1, sb0, sb1, sb, sb + 4096, smem + prv + 4096 + v_rd, true);
    if (t + AT_NS - 1 < NT) {
      WAIT_V(2 * (AT_NS - 3));
      WAIT_L0();
      BAR();
      issue(t + AT_NS - 1, prv2);
    } else {
      WAIT_V(0);
      WAIT_L0();
      BAR();
    }
    STEP(sb0, sb1, sa0, sa1, sb + 4096, smem + nx, sb + v_rd, true);
    prv = cur; cur = nx;
  }
  {
    const char* sb = smem + cur;
    STEP(sa0, sa1, sb0, sb1, sb, sb + 4096, smem + prv + 4096 + v_rd, true);
    STEP(sb0, sb1, sa0, sa1, sb + 4096, sb, sb + v_rd, false);
    prv = cur;
  }
  READV(smem + prv + 4096 + v_rd);
  PV();
#undef SCORES
#undef VFRAG
#undef READV
#undef PV
#undef EXPSUM
#undef PACK
#undef STEP
  {
    auto rr = __builtin_amdgcn_permlane32_swap(__float_as_uint(l), __float_as_uint(l), false, false);
    l = __uint_as_float(rr[0]) + __uint_as_float(rr[1]);
  }
  const float inv = 1.0f / l;
  u16* op = o + (size_t)(q0 + r32) * D_ + hd * 32 + 4 * hi;
#pragma unroll
  for (int g = 0; g < 4; ++g) {
    u32x2 w;
    w[0] = pack2(oacc[4 * g] * inv, oacc[4 * g + 1] * inv);
    w[1] = pack2(oacc[4 * g + 2] * inv, oacc[4 * g + 3] * inv);
    *(u32x2*)(op + 8 * g) = w;
  }
  STAMP_END(3);
}

constexpr int A2_KT = 256, A2_NS = 3, A2_ST = 32768, A2_MOD = A2_NS * A2_ST + 1024, A2_RED = A2_MOD + 32768, A2_FLG = A2_RED + 4096, A2_LDS = A2_FLG + 64;
template <bool SAFE>
__device__ __forceinline__ void attn_pass(char* smem, const u16* Kp, const u16* Vp, bf16x8 qf0, bf16x8 qf1, int wid, int lane,
                                          f32x16& oacc, float& m, float& l,
                                          const u16* pw0, const u16* pw1, const u16* pw2, const u16* pw3, const u16* wm, unsigned cc) {
  const int wk = wid >> 3, r32 = lane & 31, hi = lane >> 5;
  const unsigned k_voff = (unsigned)(((lane >> 2) * 32 + 8 * ((lane & 3) ^ ((lane >> 4) & 3))) * 2);
  const unsigned v_voff = (unsigned)(lane * 16);
  const unsigned lds0 = (unsigned)(size_t)(LAS char*)smem;
  auto issue = [&](int t, int bufoff) {
    const char* gk = (const char*)(Kp + ((size_t)t * A2_KT + wid * 16) * 32);
    const char* gv = (const char*)(Vp + ((size_t)t * A2_KT + wid * 16) * 32);
    glds16(gk, k_voff, lds0 + bufoff + wid * 1024);
    glds16(gv, v_voff, lds0 + bufoff + 16384 + wid * 1024);
  };
  issue(0, 0);
  issue(1, A2_ST);
  const int k_rd0 = wk * 8192 + r32 * 64 + ((hi ^ ((r32 >> 2) & 3)) << 4);
  const int k_rd1 = k_rd0 ^ 32;
  const int v_rd = 16384 + wk * 8192 + (4 * hi + ((lane & 15) >> 2)) * 64 + ((lane >> 4) & 1) * 32 + (lane & 3) * 8;
  f32x16 negm = {};
  f32x2 lsa = {0.f, 0.f}, lsb = {0.f, 0.f};
  oacc = (f32x16){};
  m = 0.f; l = 0.f;
  constexpr int NT = S_ / A2_KT;
  int cur = 0;
  for (int t = 0; t < NT; ++t) {
    if (t == 0) WAIT_V(2); else if (!SAFE && t == 4) WAIT_V(5); else if (t + 1 < NT) WAIT_V(3); else WAIT_V(0);
    BAR();
    if (!SAFE && t == 6) {
      const int rg = wid * 8 + (lane >> 3), cl = lane & 7;
      const float c0 = lo_f(cc), c1 = hi_f(cc);
      const float s0 = rbf(c0 / (1.0f + __expf(-c0))), s1 = rbf(c1 / (1.0f + __expf(-c1)));
      const u32x4 w0 = *(const LAS u32x4*)(smem + A2_MOD + (2 * rg) * 128 + cl * 16);
      const u32x4 w1 = *(const LAS u32x4*)(smem + A2_MOD + (2 * rg + 1) * 128 + cl * 16);
      float a[8];
#pragma unroll
      for (int j = 0; j < 4; ++j) {
        a[2 * j] = s0 * lo_f(w0[j]) + s1 * lo_f(w1[j]);
        a[2 * j + 1] = s0 * hi_f(w0[j]) + s1 * hi_f(w1[j]);
      }
#pragma unroll
      for (int j = 0; j < 8; ++j) {
        a[j] += __shfl_xor(a[j], 8); a[j] += __shfl_xor(a[j], 16); a[j] += __shfl_xor(a[j], 32);
      }
      if (lane < 8) {
        float* mred = (float*)(smem + A2_RED);
#pragma unroll
        for (int j = 0; j < 8; ++j) mred[wid * 64 + lane * 8 + j] = a[j];
      }
    }
    if (!SAFE && t == 3) {
      const unsigned m_voff = (unsigned)(((lane >> 3) * 6144 + (lane & 7) * 8) * 2);
      glds16(wm + (size_t)(wid * 16) * 6144, m_voff, lds0 + A2_MOD + wid * 2048);
      glds16(wm + (size_t)(wid * 16 + 8) * 6144, m_voff, lds0 + A2_MOD + wid * 2048 + 1024);
    }
    {
      int ck = (blockIdx.x * NT + t) * 16 + wid;
      if (ck >= 2048 + 3 * 7168) ck -= 16384;
      const u16* pb = pw0;
      if (ck >= 2048) { pb = pw1; ck -= 2048; if (ck >= 7168) { pb = pw2; ck -= 7168; if (ck >= 7168) { pb = pw3; ck -= 7168; } } }
      glds16((const char*)pb + (size_t)ck * 1024, v_voff, lds0 + A2_NS * A2_ST);
      int nb = cur + 2 * A2_ST; if (nb >= A2_NS * A2_ST) nb -= A2_NS * A2_ST;
      if (t + 2 < NT) issue(t + 2, nb);
    }
#pragma unroll
    for (int hb = 0; hb < 2; ++hb) {
      const char* kb = smem + cur + hb * 4096;
      const bf16x8 k00 = lds_read16(kb + k_rd0), k01 = lds_read16(kb + k_rd1);
      const bf16x8 k10 = lds_read16(kb + 2048 + k_rd0), k11 = lds_read16(kb + 2048 + k_rd1);
      f32x16 s0, s1;
      if (SAFE || (t == 0 && hb == 0)) {
        f32x16 z_ = {};
        s0 = MFMA32(k00, qf0, z_); s1 = MFMA32(k10, qf0, z_);
        s0 = MFMA32(k01, qf1, s0); s1 = MFMA32(k11, qf1, s1);
        const float tm = tile_rowmax(s0, s1);
        const bool first = (t == 0 && hb == 0);
        const float mn = first ? tm : __builtin_fmaxf(m, tm);
        if (SAFE) {
          const float f = first ? 0.f : __builtin_amdgcn_exp2f(m - mn);
          lsa *= f; lsb *= f;
          oacc *= f;
        }
        m = mn;
#pragma unroll
        for (int r = 0; r < 16; ++r) { negm[r] = -mn; s0[r] -= mn; s1[r] -= mn; }
      } else {
        s0 = MFMA32(k00, qf0, negm); s1 = MFMA32(k10, qf0, negm);
        s0 = MFMA32(k01, qf1, s0); s1 = MFMA32(k11, qf1, s1);
      }
#pragma unroll
      for (int r = 0; r < 16; r += 2) {
        s0[r] = AEXP(s0[r]); s0[r + 1] = AEXP(s0[r + 1]);
        s1[r] = AEXP(s1[r]); s1[r + 1] = AEXP(s1[r + 1]);
        lsa += (f32x2){s0[r], s0[r + 1]};
        lsb += (f32x2){s1[r], s1[r + 1]};
      }
      u32x4 p0, p1, p2, p3;
#pragma unroll
      for (int j = 0; j < 4; ++j) {
        p0[j] = pack2(s0[2 * j], s0[2 * j + 1]);
        p1[j] = pack2(s0[8 + 2 * j], s0[8 + 2 * j + 1]);
        p2[j] = pack2(s1[2 * j], s1[2 * j + 1]);
        p3[j] = pack2(s1[8 + 2 * j], s1[8 + 2 * j + 1]);
      }
      const char* vb = smem + cur + hb * 4096 + v_rd;
#define VFRAG(s) __builtin_shufflevector(tr_read(vb + (s) * 1024), tr_read(vb + (s) * 1024 + 512), 0, 1, 2, 3, 4, 5, 6, 7)
      oacc = MFMA32(VFRAG(0), __builtin_bit_cast(bf16x8, p0), oacc);
      oacc = MFMA32(VFRAG(1), __builtin_bit_cast(bf16x8, p1), oacc);
      oacc = MFMA32(VFRAG(2), __builtin_bit_cast(bf16x8, p2), oacc);
      oacc = MFMA32(VFRAG(3), __builtin_bit_cast(bf16x8, p3), oacc);
#undef VFRAG
    }
    WAIT_L0();
    cur += A2_ST; if (cur >= A2_NS * A2_ST) cur = 0;
  }
  lsa += lsb;
  l = lsa[0] + lsa[1];
  {
    auto rr = __builtin_amdgcn_permlane32_swap(__float_as_uint(l), __float_as_uint(l), false, false);
    l = __uint_as_float(rr[0]) + __uint_as_float(rr[1]);
  }
  WAIT_V(0);
}

__global__ __launch_bounds__(1024, 4) void attn2_kernel(const u16* __restrict__ qkv, u16* __restrict__ o,
                                                        const u16* pw0, const u16* pw1, const u16* pw2, const u16* pw3,
                                                        const u16* __restrict__ cvec, const u16* wmod, float* __restrict__ modp) {
  extern __shared__ __attribute__((aligned(16))) char smem[];
  STAMP_BEGIN();
  const int tid = threadIdx.x, lane = tid & 63;
  const int wid = __builtin_amdgcn_readfirstlane(tid >> 6);
  const int wq = wid & 7, wk = wid >> 3;
  const int x = blockIdx.x & 7, idx = blockIdx.x >> 3;
  const int hd = x * 4 + (idx >> 3), qb = idx & 7;
  const int r32 = lane & 31, hi = lane >> 5;
  const u16* Q = qkv + ((size_t)(0 * 32 + hd) * S_) * 32;
  const u16* Kp = qkv + ((size_t)(1 * 32 + hd) * S_) * 32;
  const u16* Vp = qkv + ((size_t)(2 * 32 + hd) * S_) * 32;
  const int q0 = qb * 256 + wq * 32;
  const bf16x8 qf0 = *(const bf16x8*)(Q + (size_t)(q0 + r32) * 32 + hi * 8);
  const bf16x8 qf1 = *(const bf16x8*)(Q + (size_t)(q0 + r32) * 32 + 16 + hi * 8);

  f32x16 oacc;
  float m, l;
  const int mcg = 32 + (blockIdx.x >> 2), mks = blockIdx.x & 3;
  const u16* wm = wmod + (size_t)(mks * 256) * 6144 + mcg * 64;
  unsigned cc = *(const unsigned*)(cvec + mks * 256 + 2 * (tid >> 3));
  asm volatile("" : "+v"(cc));
  attn_pass<false>(smem, Kp, Vp, qf0, qf1, wid, lane, oacc, m, l, pw0, pw1, pw2, pw3, wm, cc);
  float* xb = (float*)smem + wq * (20 * 64);
  const float* mred = (const float*)(smem + A2_RED);
  volatile LAS int* badf = (volatile LAS int*)(smem + A2_FLG);
  {
    float chk = l;
#pragma unroll
    for (int r = 0; r < 16; ++r) chk += __builtin_fabsf(oacc[r]);
    const int bad = __any(!(chk < 1.0e37f));
    if (lane == 0) badf[wid] = bad;
    __syncthreads();
    const int anybad = __any(badf[lane & 15] != 0);
    if (anybad) {
      __syncthreads();
      attn_pass<true>(smem, Kp, Vp, qf0, qf1, wid, lane, oacc, m, l, pw0, pw1, pw2, pw3, wm, cc);
      __syncthreads();
    }
  }
  xb[wk * 64 + lane] = m; xb[(2 + wk) * 64 + lane] = l;
#pragma unroll
  for (int r = 0; r < 8; ++r) xb[(4 + 8 * wk + r) * 64 + lane] = wk ? oacc[r] : oacc[8 + r];
  __syncthreads();
  {
    const float m2 = xb[(wk ^ 1) * 64 + lane], l2 = xb[(2 + (wk ^ 1)) * 64 + lane];
    const float mn = __builtin_fmaxf(m, m2);
    const float f1 = __builtin_amdgcn_exp2f((m - mn) * 0.5f), f2 = __builtin_amdgcn_exp2f((m2 - mn) * 0.5f);
    const float inv = 1.0f / ((l * f1) * f1 + (l2 * f2) * f2);
    u16* op = o + ((size_t)hd * S_ + q0 + r32) * 32 + 4 * hi + 16 * wk;
#pragma unroll
    for (int g = 0; g < 2; ++g) {
      float v[4];
#pragma unroll
      for (int e = 0; e < 4; ++e) {
        const float own = wk ? oacc[8 + 4 * g + e] : oacc[4 * g + e];
        v[e] = ((own * f1) * f1 + (xb[(4 + 8 * (wk ^ 1) + 4 * g + e) * 64 + lane] * f2) * f2) * inv;
      }
      u32x2 w;
      w[0] = pack2(v[0], v[1]);
      w[1] = pack2(v[2], v[3]);
      *(u32x2*)(op + 8 * g) = w;
    }
  }
  if (wid == 15) {
    float sacc = 0.f;
#pragma unroll
    for (int w = 0; w < 16; ++w) sacc += mred[w * 64 + lane];
    modp[mks * 6144 + mcg * 64 + lane] = sacc;
  }
  STAMP_END(3);
}

template <int MODE> static void launch_gemm(const GemmP& p, hipStream_t stream) {
  using C = GCfg<MODE>;
  constexpr int lds0_ = C::NS * ((C::AD ? 0 : C::BM) + C::BN) * 128, lds = lds0_ < 20480 ? 20480 : lds0_;
  hipFuncSetAttribute((const void*)gemm_kernel<MODE>, hipFuncAttributeMaxDynamicSharedMemorySize, lds);
  gemm_kernel<MODE><<<C::NTM * C::NTN, C::WM * C::WN * 64 * (C::LW ? 2 : 1), lds, stream>>>(p);
}

extern "C" void kernel_launch(void* const* d_in, const int* in_sizes, int n_in, void* d_out, int out_size,
                              void* d_ws, size_t ws_size, hipStream_t stream) {
  const u16* x = (const u16*)d_in[0];
  const u16* wqkv = (const u16*)d_in[1];
  const u16* wo = (const u16*)d_in[2];
  const u16* wg = (const u16*)d_in[4];
  const u16* wu = (const u16*)d_in[5];
  const u16* wd = (const u16*)d_in[6];
  const u16* c = (const u16*)d_in[7];
  const u16* wmod = (const u16*)d_in[8];
  u16* out = (u16*)d_out;

  char* ws = (char*)d_ws;
  float* mod = (float*)ws;
  u16* h = (u16*)(ws + (1 << 20));
  u16* qkv = (u16*)(ws + (8 << 20));
  u16* ao = (u16*)(ws + (24 << 20));
  u16* x1 = (u16*)(ws + (32 << 20));
  u16* hh = (u16*)(ws + (40 << 20));
  float* slab = (float*)(ws + (64 << 20));
  unsigned* flags = (unsigned*)(ws + (512 << 10));

  mod_kernel<<<256, 256, 0, stream>>>(c, wmod, mod, x, wqkv, flags);
  ln_kernel<<<512 + 192, 256, 0, stream>>>(x, mod, 0, h, wqkv, c, wmod, mod, 1);
  { GemmP p{h, wqkv, nullptr, nullptr, nullptr, qkv, nullptr, nullptr, nullptr}; launch_gemm<0>(p, stream); }
  hipFuncSetAttribute((const void*)attn2_kernel, hipFuncAttributeMaxDynamicSharedMemorySize, A2_LDS);
  attn2_kernel<<<256, 1024, A2_LDS, stream>>>(qkv, ao, wo, wg, wu, wd, c, wmod, mod);
  { GemmP p{ao, wo, nullptr, x, mod + 2 * 1024, x1, slab, flags, h}; launch_gemm<1>(p, stream);
  }
  ln_kernel<<<512, 256, 0, stream>>>(x1, mod, 3 * 1024, h, wqkv, c, wmod, mod, 5);
  { GemmP p{h, wg, wu, nullptr, nullptr, hh, nullptr, nullptr, nullptr}; launch_gemm<2>(p, stream); }
  { GemmP p{hh, wd, nullptr, x1, mod + 5 * 1024, out, slab, flags, nullptr}; launch_gemm<4>(p, stream); }
}
```

```cpp
#include <hip/hip_runtime.h>
#include <hip/hip_bf16.h>
#include <stdint.h>
#define NO_LN2_FUSE
#define MOD_IN_ATTN

typedef unsigned short u16;
typedef short bf16x8 __attribute__((ext_vector_type(8)));
typedef short s16x4 __attribute__((ext_vector_type(4)));
typedef float f32x2 __attribute__((ext_vector_type(2)));
typedef float f32x4 __attribute__((ext_vector_type(4)));
typedef float f32x16 __attribute__((ext_vector_type(16)));
typedef unsigned u32x2 __attribute__((ext_vector_type(2)));
typedef unsigned u32x4 __attribute__((ext_vector_type(4)));
typedef __bf16 bf16x2_t __attribute__((ext_vector_type(2)));
#define LAS __attribute__((address_space(3)))
#define GAS __attribute__((address_space(1)))

constexpr int S_ = 2048, D_ = 1024, H_ = 32, DH_ = 32, F_ = 3584;

__device__ __forceinline__ float bf2f(u16 u) { return __uint_as_float((unsigned)u << 16); }
__device__ __forceinline__ unsigned pack2(float lo, float hi) {
  f32x2 v = {lo, hi};
  bf16x2_t b = __builtin_convertvector(v, bf16x2_t);
  return __builtin_bit_cast(unsigned, b);
}
__device__ __forceinline__ float rbf(float f) {
  __bf16 b = (__bf16)f;
  return __uint_as_float((unsigned)__builtin_bit_cast(u16, b) << 16);
}
__device__ __forceinline__ float lo_f(unsigned u) { return __uint_as_float(u << 16); }
__device__ __forceinline__ float hi_f(unsigned u) { return __uint_as_float(u & 0xffff0000u); }

#define STAMP_BEGIN()
#define STAMP_END(kid)
#define STAMP_MID(kid, i)
#define WAIT_V(n) asm volatile("s_waitcnt vmcnt(%0)" ::"n"(n) : "memory")
#define WAIT_L0() asm volatile("s_waitcnt lgkmcnt(0)" ::: "memory")
#define BAR() __builtin_amdgcn_s_barrier()

__device__ __forceinline__ void glds16(const void* sbase, unsigned voff, unsigned ldsaddr) {
  asm volatile("s_mov_b32 m0, %2\n\ts_nop 0\n\tglobal_load_lds_dwordx4 %0, %1" ::"v"(voff), "s"(sbase), "s"(ldsaddr) : "memory");
}
#define MFMA16(c, a, b) c = __builtin_amdgcn_mfma_f32_16x16x32_bf16(a, b, c, 0, 0, 0)
#define ST16(p, v) asm volatile("global_store_dwordx4 %0, %1, off sc0 sc1\n\ts_nop 1" ::"v"(p), "v"(v) : "memory")
#define SBAR() __builtin_amdgcn_sched_barrier(0)
#define SGB(mask, n) __builtin_amdgcn_sched_group_barrier(mask, n, 0)
template <int I> struct IC { static constexpr int value = I; };
template <int I, int N, class F> __device__ __forceinline__ void sfor(F&& f) {
  if constexpr (I < N) { f(IC<I>{}); sfor<I + 1, N>(f); }
}
__device__ __forceinline__ s16x4 tr_read(const char* p) {
  return __builtin_amdgcn_ds_read_tr16_b64_v4i16((LAS s16x4*)p);
}
__device__ __forceinline__ bf16x8 lds_read16(const char* p) { return *(const LAS bf16x8*)p; }

__device__ __forceinline__ void mod_block(const u16* __restrict__ c, const u16* __restrict__ wmod, float* __restrict__ modp,
                                          int cg, int ks, float* sh) {
  const int tid = threadIdx.x;
  const int cl = tid & 7, rg = tid >> 3;
  const int col0 = cg * 64 + cl * 8;
  u32x4 w[8];
#pragma unroll
  for (int i = 0; i < 8; ++i)
    w[i] = __builtin_nontemporal_load((const u32x4*)(wmod + (size_t)(ks * 256 + rg + i * 32) * 6144 + col0));
  {
    const float v = bf2f(c[ks * 256 + tid]);
    sh[tid] = rbf(v / (1.0f + __expf(-v)));
  }
  __syncthreads();
  float acc[8];
#pragma unroll
  for (int j = 0; j < 8; ++j) acc[j] = 0.f;
#pragma unroll
  for (int i = 0; i < 8; ++i) {
    const float s = sh[rg + i * 32];
#pragma unroll
    for (int j = 0; j < 4; ++j) {
      acc[2 * j] += s * lo_f(w[i][j]);
      acc[2 * j + 1] += s * hi_f(w[i][j]);
    }
  }
  float* red = sh + 256;
#pragma unroll
  for (int j = 0; j < 8; ++j) red[rg * 65 + cl * 8 + j] = acc[j];
  __syncthreads();
  if (tid < 64) {
    float s = 0.f;
#pragma unroll 8
    for (int r = 0; r < 32; ++r) s += red[r * 65 + tid];
    modp[ks * 6144 + cg * 64 + tid] = s;
  }
}
__device__ __forceinline__ void touch_block(const char* src) {
  u32x4 v[8];
#pragma unroll
  for (int i = 0; i < 8; ++i) v[i] = *(const u32x4*)(src + i * 4096 + threadIdx.x * 16);
#pragma unroll
  for (int i = 0; i < 8; ++i) asm volatile("" ::"v"(v[i]));
}
__device__ __forceinline__ f32x4 ld_mod4(const float* modp, int idx) {
  f32x4 a = *(const f32x4*)(modp + idx), b = *(const f32x4*)(modp + 6144 + idx);
  f32x4 c = *(const f32x4*)(modp + 2 * 6144 + idx), d = *(const f32x4*)(modp + 3 * 6144 + idx);
  f32x4 s = (a + b) + (c + d);
  return (f32x4){rbf(s[0]), rbf(s[1]), rbf(s[2]), rbf(s[3])};
}

__global__ __launch_bounds__(256) void mod_kernel(const u16* __restrict__ c, const u16* __restrict__ wmod,
                                                  float* __restrict__ modp, const u16* __restrict__ pf0,
                                                  const u16* __restrict__ pf1, unsigned* flags) {
  __shared__ float sh[256 + 32 * 65];
  STAMP_BEGIN();
  const int b = blockIdx.x;
  if (threadIdx.x == 0 && b < 256) flags[b * 32] = 0u;
  if (b < 128) mod_block(c, wmod, modp, b >> 2, b & 3, sh);
  else if (b < 256) touch_block((const char*)pf0 + (size_t)(b - 128) * 32768);
  else touch_block((const char*)pf1 + (size_t)(b - 256) * 32768);
  STAMP_END(0);
}

__global__ __launch_bounds__(256) void ln_kernel(const u16* __restrict__ x, const float* modp, int moff, u16* __restrict__ h,
                                                 const u16* __restrict__ pf, const u16* __restrict__ c,
                                                 const u16* __restrict__ wmod, float* modp_w, int kid) {
  __shared__ float sh[256 + 32 * 65];
  STAMP_BEGIN();
  const int tid = threadIdx.x;
  if (blockIdx.x >= 512) {
    const int b = blockIdx.x - 512;
    if (b < 192) touch_block((const char*)pf + (size_t)b * 32768);
    else mod_block(c, wmod, modp_w, 32 + ((b - 192) >> 2), b & 3, sh);
    return;
  }
  const int lane = tid & 63, wid = tid >> 6;
  const int row = blockIdx.x * 4 + wid;
  const u16* xr = x + (size_t)row * D_;
  const u32x4 v0 = *(const u32x4*)(xr + lane * 8);
  const u32x4 v1 = *(const u32x4*)(xr + 512 + lane * 8);
  {
    const f32x4 s0 = ld_mod4(modp, moff + tid * 4);
    const f32x4 s1 = ld_mod4(modp, moff + 1024 + tid * 4);
    *(f32x4*)(sh + tid * 4) = s0;
    *(f32x4*)(sh + 1024 + tid * 4) = (f32x4){rbf(1.0f + s1[0]), rbf(1.0f + s1[1]), rbf(1.0f + s1[2]), rbf(1.0f + s1[3])};
  }
  float f[16];
#pragma unroll
  for (int j = 0; j < 4; ++j) {
    f[2 * j] = lo_f(v0[j]); f[2 * j + 1] = hi_f(v0[j]);
    f[8 + 2 * j] = lo_f(v1[j]); f[8 + 2 * j + 1] = hi_f(v1[j]);
  }
  float s = 0.f;
#pragma unroll
  for (int j = 0; j < 16; ++j) s += f[j];
#pragma unroll
  for (int o = 32; o >= 1; o >>= 1) s += __shfl_xor(s, o);
  const float mean = s * (1.0f / D_);
  float q = 0.f;
#pragma unroll
  for (int j = 0; j < 16; ++j) { float d = f[j] - mean; q += d * d; }
#pragma unroll
  for (int o = 32; o >= 1; o >>= 1) q += __shfl_xor(q, o);
  const float mb = rbf(mean);
  const float vb = rbf(q * (1.0f / D_));
  const float sd = rbf(sqrtf(rbf(vb + 1e-5f)));
  const float inv = 1.0f / sd;
  __syncthreads();
  float o[16];
#pragma unroll
  for (int half = 0; half < 2; ++half) {
    const int c0 = half * 512 + lane * 8;
    const f32x4 sa0 = *(const f32x4*)(sh + c0), sa1 = *(const f32x4*)(sh + c0 + 4);
    const f32x4 ca0 = *(const f32x4*)(sh + 1024 + c0), ca1 = *(const f32x4*)(sh + 1024 + c0 + 4);
#pragma unroll
    for (int j = 0; j < 8; ++j) {
      const float shf = j < 4 ? sa0[j & 3] : sa1[j & 3];
      const float sc = j < 4 ? ca0[j & 3] : ca1[j & 3];
      float t = rbf(f[half * 8 + j] - mb);
      t = rbf(t * inv);
      t = rbf(t * sc);
      o[half * 8 + j] = t + shf;
    }
  }
  u16* hr = h + (size_t)row * D_;
  u32x4 w0, w1;
#pragma unroll
  for (int j = 0; j < 4; ++j) {
    w0[j] = pack2(o[2 * j], o[2 * j + 1]);
    w1[j] = pack2(o[8 + 2 * j], o[8 + 2 * j + 1]);
  }
  ST16(hr + lane * 8, w0);
  ST16(hr + 512 + lane * 8, w1);
  STAMP_END(kid);
}

struct GemmP {
  const u16* A; const u16* B0; const u16* B1; const u16* res; const float* gate; u16* out; float* slab; unsigned* flags; u16* out2;
};

#define NS0 3
#define NS1 3
#define NS3 3
#define LW0 1
#define LW1 1
#define LW3 1
#define AD1 0
#define AD4 0
template <int MODE> struct GCfg;
template <> struct GCfg<0> { static constexpr int BM = 128, BN = 192, WM = 2, WN = 2, NS = NS0, K = 1024, LDA = 1024, LDB = 3072, NTM = 16, NTN = 16, RM = 8, RN = 4, LW = LW0, AD = 0; };
template <> struct GCfg<1> { static constexpr int BM = 128, BN = 64, WM = AD1 ? 4 : 2, WN = AD1 ? 1 : 2, NS = NS1, K = 1024, LDA = 1024, LDB = 1024, NTM = 16, NTN = 16, RM = 4, RN = 8, LW = LW1, AD = AD1; };
template <> struct GCfg<2> { static constexpr int BM = 256, BN = 256, WM = 4, WN = 2, NS = 2, K = 1024, LDA = 1024, LDB = 3584, NTM = 8, NTN = 28, RM = 4, RN = 7, LW = 0, AD = 0; };
template <> struct GCfg<3> { static constexpr int BM = 128, BN = 128, WM = 2, WN = 2, NS = NS3, K = 1792, LDA = 3584, LDB = 1024, NTM = 16, NTN = 16, RM = 4, RN = 8, LW = LW3, AD = 0; };

template <> struct GCfg<4> { static constexpr int BM = 128, BN = 64, WM = AD4 ? 4 : 2, WN = AD4 ? 1 : 2, NS = NS3, K = 3584, LDA = 3584, LDB = 1024, NTM = 16, NTN = 16, RM = 4, RN = 8, LW = LW3, AD = AD4; };

template <> struct GCfg<5> { static constexpr int BM = 256, BN = 256, WM = 8, WN = 1, NS = 2, K = 1024, LDA = 1024, LDB = 3584, NTM = 8, NTN = 32, RM = 4, RN = 8, LW = 0, AD = 0; };

template <int MODE>
__global__ __launch_bounds__(GCfg<MODE>::WM* GCfg<MODE>::WN * 64 * (GCfg<MODE>::LW ? 2 : 1), 2) void gemm_kernel(GemmP p) {
  using C = GCfg<MODE>;
  constexpr int BM = C::BM, BN = C::BN, WM = C::WM, WN = C::WN, NS = C::NS, NW = WM * WN;
  constexpr int TM = BM / WM, TN = BN / WN, MI = TM / 16, NI = (MODE == 5) ? 14 : TN / 16;
  constexpr int AD = C::AD;
  constexpr int A_BYTES = AD ? 0 : BM * 128, B_BYTES = BN * 128, ST_BYTES = A_BYTES + B_BYTES;
  constexpr int GA = AD ? 0 : BM / 8 / NW, GB = BN / 8 / NW, G = GA + GB;
  constexpr int NT = C::K / 64;
  static_assert(BM % (8 * NW) == 0 && BN % (8 * NW) == 0, "stage split");
  static_assert(NT >= NS + 1, "K too small");
  extern __shared__ __attribute__((aligned(16))) char smem[];
  STAMP_BEGIN();

  const int tid = threadIdx.x, lane = tid & 63;
  constexpr bool LW = C::LW != 0;
  const int wid_all = __builtin_amdgcn_readfirstlane(tid >> 6);
  const int wid = wid_all % NW;
  const int wm = wid / WN, wn = wid % WN;
  int tm, tn;
  {
    const int x = blockIdx.x & 7, idx = blockIdx.x >> 3;
    constexpr int NRM = C::NTM / C::RM;
    const int rm = x % NRM, rn = x / NRM;
    tm = rm * C::RM + idx % C::RM;
    tn = rn * C::RN + idx / C::RM;
  }
  int kh = 0;
  if constexpr (MODE == 3) { kh = tn & 1; tn >>= 1; }
  const int m0 = tm * BM;

  const int par = wid & 1;
  const int a_ch = (lane & 7) ^ ((4 * par + (lane >> 4)) & 7);
  const unsigned a_voff = (MODE == 1) ? (unsigned)((((a_ch >> 2) * S_ + (lane >> 3)) * 32 + (a_ch & 3) * 8) * 2)
                                      : (unsigned)(((lane >> 3) * C::LDA + 8 * a_ch) * 2);
  const unsigned b_voff = (unsigned)(((lane >> 3) * C::LDB + 8 * ((lane & 7) ^ ((((lane >> 4) & 1) | (par << 1)) << 1))) * 2);
  const int b_ch = (lane & 7) ^ ((((lane >> 4) & 1) | (par << 1)) << 1);
  const unsigned b_voff2 = (unsigned)(((lane >> 3) * C::LDB + 8 * (b_ch < 6 ? b_ch : b_ch - 2)) * 2);
  const char* Abase = (MODE == 1) ? (const char*)(p.A + (size_t)m0 * 32) : (const char*)(p.A + (size_t)m0 * C::LDA + kh * C::K);
  const unsigned lds0 = (unsigned)(size_t)(LAS char*)smem;

  auto issue1 = [&](auto gi, int t, int bufoff) {
    constexpr int g = decltype(gi)::value;
    if constexpr (g < GA) {
      const int i = wid + NW * g;
      const char* gp = (MODE == 1) ? Abase + ((size_t)(8 * i) * 32 + (size_t)t * 2 * S_ * 32) * 2
                                   : Abase + ((size_t)(8 * i) * C::LDA + (size_t)t * 64) * 2;
      glds16(gp, a_voff, lds0 + bufoff + i * 1024);
    } else {
      const int i = wid + NW * (g - GA);
      const int pn = i >> 3, kr = 8 * (i & 7);
      const u16* bp;
      if constexpr (MODE == 2) bp = (pn < 2 ? p.B0 : p.B1) + tn * 128 + (pn & 1) * 64;
      else if constexpr (MODE == 5) bp = (pn < 2 ? p.B0 : p.B1) + tn * 112 + (pn & 1) * 64;
      else bp = p.B0 + (size_t)kh * C::K * C::LDB + tn * BN + pn * 64;
      const char* gp = (const char*)(bp + ((size_t)t * 64 + kr) * C::LDB);
      glds16(gp, (MODE == 5 && ((g - GA) & 1)) ? b_voff2 : b_voff, lds0 + bufoff + A_BYTES + i * 1024);
    }
  };

  if constexpr (C::LW == 1) {
    if (wid_all >= NW) {
      sfor<0, NS>([&](auto sc_) { sfor<0, G>([&](auto gc) { issue1(gc, decltype(sc_)::value, decltype(sc_)::value * ST_BYTES); }); });
      WAIT_V((NS - 1) * G);
      BAR();
      int lb = 0;
      for (int t = 0; t < NT - NS; ++t) {
        WAIT_V((NS - 2) * G);
        BAR();
        sfor<0, G>([&](auto gc) { issue1(gc, t + NS, lb); });
        lb = (lb + ST_BYTES == NS * ST_BYTES) ? 0 : lb + ST_BYTES;
      }
      sfor<0, NS - 1>([&](auto rc) { WAIT_V((NS - 2 - decltype(rc)::value) * G); BAR(); });
      return;
    }
  }
  if constexpr (C::LW == 2) {
    if (wid_all >= NW) {
      static_assert(NS == 3 && (NT & 1) == 0, "reg loader");
      u32x4 R0[G], R1[G];
      auto ld = [&](u32x4* R, int t) {
        sfor<0, G>([&](auto gi) {
          constexpr int g = decltype(gi)::value;
          if constexpr (g < GA) {
            const int i = wid + NW * g;
            R[g] = *(const u32x4*)(Abase + ((size_t)(8 * i) * C::LDA + (size_t)t * 64) * 2 + a_voff);
          } else {
            const int i = wid + NW * (g - GA);
            const int pn = i >> 3, kr = 8 * (i & 7);
            const u16* bp = p.B0 + (size_t)kh * C::K * C::LDB + tn * BN + pn * 64;
            R[g] = *(const u32x4*)((const char*)(bp + ((size_t)t * 64 + kr) * C::LDB) + b_voff);
          }
        });
      };
      auto st = [&](const u32x4* R, int bufoff) {
        sfor<0, G>([&](auto gi) {
          constexpr int g = decltype(gi)::value;
          const int off = g < GA ? (wid + NW * g) * 1024 : A_BYTES + (wid + NW * (g - GA)) * 1024;
          *(LAS u32x4*)(smem + bufoff + off + lane * 16) = R[g];
        });
      };
      ld(R0, 0); ld(R1, 1);
      st(R0, 0); ld(R0, 2);
      WAIT_L0(); BAR();
      st(R1, ST_BYTES); ld(R1, 3);
      st(R0, 2 * ST_BYTES); ld(R0, 4);
      int lb = 0;
      for (int t = 0; t < NT - 4; t += 2) {
        WAIT_L0(); BAR();
        st(R1, lb); if (t + 5 < NT) ld(R1, t + 5);
        lb = (lb + ST_BYTES == NS * ST_BYTES) ? 0 : lb + ST_BYTES;
        WAIT_L0(); BAR();
        st(R0, lb); if (t + 6 < NT) ld(R0, t + 6);
        lb = (lb + ST_BYTES == NS * ST_BYTES) ? 0 : lb + ST_BYTES;
      }
      WAIT_L0(); BAR();
      st(R1, lb);
      WAIT_L0(); BAR();
      BAR();
      return;
    }
  }

  const int fr = lane & 15, fq = lane >> 4;
  u32x4 rv[4];
  f32x4 gv[NI];
  if constexpr (MODE == 1 || MODE == 3 || MODE == 4) {
#pragma unroll
    for (int i = 0; i < 4; ++i) {
      if constexpr (MODE != 3) rv[i] = *(const u32x4*)(p.res + (size_t)(m0 + i * 32 + (tid >> 3)) * D_ + tn * BN + (tid & 7) * 8);
      else rv[i] = *(const u32x4*)(p.res + (size_t)(m0 + kh * 64 + i * 16 + (tid >> 4)) * D_ + tn * BN + (tid & 15) * 8);
    }
#pragma unroll
    for (int ni = 0; ni < NI; ++ni) { gv[ni] = ld_mod4(p.gate, tn * BN + wn * TN + ni * 16 + 4 * fq); asm volatile("" : "+v"(gv[ni])); }
  }
  const int a_rd0 = (wm * TM + fr) * 128 + ((fq ^ (fr >> 1)) << 4);
  const int a_rd1 = a_rd0 ^ 64;
  int b_rd[(MODE == 2 || MODE == 5) ? 4 : NI];
  {
    const int q = (lane & 15) >> 2, pp = lane & 3;
    const int gl = ((q >> 1) & 1) | ((fq & 1) << 1);
#pragma unroll
    for (int ni = 0; ni < ((MODE == 2 || MODE == 5) ? 4 : NI); ++ni) {
      int pn, un;
      if constexpr (MODE == 2 || MODE == 5) { pn = wn; un = ni & 3; }
      else { const int j = wn * NI + ni; pn = j >> 2; un = j & 3; }
      b_rd[ni] = A_BYTES + pn * 8192 + (8 * fq + q) * 128 + ((un ^ gl) << 5) + pp * 8;
    }
  }

  f32x4 acc[MI][NI];
#pragma unroll
  for (int mi = 0; mi < MI; ++mi)
#pragma unroll
    for (int ni = 0; ni < NI; ++ni) acc[mi][ni] = (f32x4){0.f, 0.f, 0.f, 0.f};

  constexpr int NSP = (MODE == 2 || MODE == 5) ? 2 : 1;
  constexpr int NH = NI / NSP, NM = MI * NH, NPH = 2 * NSP;
  constexpr int GW = (NS >= 3) ? NPH : NPH / 2;
  constexpr int GPP = (G + GW - 1) / GW;
  bf16x8 a0[MI], a1[MI], b0[NH], b1[NH];
  s16x4 blo[NH];

  auto rd_b = [&](auto ri, bf16x8* BF, const char* sb, int ks, int hf) {
    constexpr int r = decltype(ri)::value, ni = r >> 1;
    const int bo = (MODE == 2) ? b_rd[ni] + hf * 16384 : (MODE == 5) ? b_rd[ni & 3] + (ni >> 2) * 8192 + hf * 16384 : b_rd[ni];
    if constexpr ((r & 1) == 0) blo[ni] = tr_read(sb + bo + ks * 4096);
    else BF[ni] = __builtin_shufflevector(blo[ni], tr_read(sb + bo + ks * 4096 + 512), 0, 1, 2, 3, 4, 5, 6, 7);
  };
  auto rd_a = [&](auto ri, bf16x8* AF, const char* sb, int ks) {
    constexpr int mi = decltype(ri)::value;
    AF[mi] = lds_read16(sb + (ks ? a_rd1 : a_rd0) + mi * 2048);
  };
  auto phase = [&](auto hfc, auto rdm, auto g0c, auto g1c, bf16x8* AF, bf16x8* BF, bf16x8* AN_, bf16x8* BN_,
                   const char* rsb, int rks, int rhf, int gt, int gbuf) {
    constexpr int hf = decltype(hfc)::value, RDM = decltype(rdm)::value;
    constexpr int g0 = decltype(g0c)::value, g1 = decltype(g1c)::value, NG = g1 - g0;
    constexpr int NR = RDM == 0 ? 0 : (RDM == 1 ? 2 * NH : 2 * NH + MI);
    sfor<0, NM>([&](auto ic) {
      constexpr int i = decltype(ic)::value, mi = i / NH, ni = i % NH;
      MFMA16(acc[mi][hf * NH + ni], BF[ni], AF[mi]);
      sfor<0, NR>([&](auto rc) {
        constexpr int r = decltype(rc)::value;
        if constexpr ((r * (NM - NM / 4)) / (NR > 0 ? NR : 1) == i) {
          if constexpr (r < 2 * NH) rd_b(rc, BN_, rsb, rks, rhf);
          else rd_a(IC<r - 2 * NH>{}, AN_, rsb, rks);
        }
      });
      sfor<0, NG>([&](auto gc) {
        constexpr int g = decltype(gc)::value;
        if constexpr ((g * NM) / (NG > 0 ? NG : 1) == i) issue1(IC<g0 + g>{}, gt, gbuf);
      });
      SBAR();
    });
  };
  constexpr auto gmin = [](int a, int b) { return a < b ? a : b; };
  int cur = 0, prv = 0;
  auto ktile = [&](auto vmc, auto restc, auto firstc, int t) {
    constexpr int VM = decltype(vmc)::value;
    constexpr bool REST = decltype(restc)::value && !LW, FIRST = decltype(firstc)::value && !LW;
    const int nxt = (cur + ST_BYTES == NS * ST_BYTES) ? 0 : cur + ST_BYTES;
    const char* sc = smem + cur;
    const char* sn = smem + nxt;
    constexpr int R1 = REST ? gmin(G, GPP) : 0, R2 = REST ? gmin(G, 2 * GPP) : 0, R3 = REST ? gmin(G, 3 * GPP) : 0, R4 = REST ? gmin(G, 4 * GPP) : 0;
    constexpr int F1 = FIRST ? gmin(G, GPP) : 0;
    if constexpr (NSP == 1) {
      phase(IC<0>{}, IC<2>{}, IC<R1>{}, IC<R2>{}, a0, b0, a1, b1, sc, 1, 0, t - 1 + NS, prv);
      if constexpr (VM >= 0) {
        if constexpr (!LW) WAIT_V(VM);
        WAIT_L0(); BAR();
        phase(IC<0>{}, IC<2>{}, IC<0>{}, IC<F1>{}, a1, b1, a0, b0, sn, 0, 0, t + NS, cur);
      } else {
        phase(IC<0>{}, IC<0>{}, IC<0>{}, IC<0>{}, a1, b1, a0, b0, sn, 0, 0, 0, 0);
      }
    } else {
      phase(IC<0>{}, IC<1>{}, IC<R1>{}, IC<R2>{}, a0, b0, a1, b1, sc, 0, 1, t - 1 + NS, prv);
      phase(IC<1>{}, IC<2>{}, IC<R2>{}, IC<R3>{}, a0, b1, a1, b0, sc, 1, 0, t - 1 + NS, prv);
      phase(IC<0>{}, IC<1>{}, IC<R3>{}, IC<R4>{}, a1, b0, a1, b1, sc, 1, 1, t - 1 + NS, prv);
      if constexpr (VM >= 0) {
        WAIT_V(VM); WAIT_L0(); BAR();
        phase(IC<1>{}, IC<2>{}, IC<0>{}, IC<F1>{}, a1, b1, a0, b0, sn, 0, 0, t + NS, cur);
      } else {
        phase(IC<1>{}, IC<0>{}, IC<0>{}, IC<0>{}, a1, b1, a0, b0, sn, 0, 0, 0, 0);
      }
    }
    prv = cur; cur = nxt;
  };

  if constexpr (AD > 0) {
    static_assert(LW && NSP == 1 && NT % AD == 0, "A-direct path");
    bf16x8 ar[AD][2][MI];
    const char* Aw = (MODE == 1) ? Abase + ((size_t)(wm * TM + fr) * 32 + fq * 8) * 2
                                 : Abase + ((size_t)(wm * TM + fr) * C::LDA + fq * 8) * 2;
    auto lda = [&](auto dc, int t) {
      constexpr int d = decltype(dc)::value;
#pragma unroll
      for (int ks = 0; ks < 2; ++ks)
#pragma unroll
        for (int mi = 0; mi < MI; ++mi) {
          if constexpr (MODE == 1) ar[d][ks][mi] = *(const bf16x8*)(Aw + ((size_t)(2 * t + ks) * S_ + mi * 16) * 64);
          else ar[d][ks][mi] = *(const bf16x8*)(Aw + (size_t)mi * 16 * C::LDA * 2 + t * 128 + ks * 64);
        }
    };
    sfor<0, AD>([&](auto dc) { lda(dc, decltype(dc)::value); });
    BAR();
    STAMP_MID(MODE == 0 ? 2 : MODE == 1 ? 4 : (MODE == 2 || MODE == 5) ? 6 : 7, 0);
    sfor<0, 2 * NH>([&](auto rc) { rd_b(rc, b0, smem, 0, 0); });
    auto ktile_ad = [&](auto dc, auto ldc, auto lastc, int t) {
      constexpr int d = decltype(dc)::value;
      const int nxt = (cur + ST_BYTES == NS * ST_BYTES) ? 0 : cur + ST_BYTES;
      phase(IC<0>{}, IC<1>{}, IC<0>{}, IC<0>{}, ar[d][0], b0, a1, b1, smem + cur, 1, 0, 0, 0);
      if constexpr (!decltype(lastc)::value) {
        WAIT_L0(); BAR();
        phase(IC<0>{}, IC<1>{}, IC<0>{}, IC<0>{}, ar[d][1], b1, a0, b0, smem + nxt, 0, 0, 0, 0);
      } else {
        phase(IC<0>{}, IC<0>{}, IC<0>{}, IC<0>{}, ar[d][1], b1, a0, b0, smem + nxt, 0, 0, 0, 0);
      }
      if constexpr (decltype(ldc)::value) { lda(dc, t + AD); SBAR(); }
      cur = nxt;
    };
    for (int t = 0; t < NT - AD; t += AD) sfor<0, AD>([&](auto dc) { ktile_ad(dc, IC<1>{}, IC<0>{}, t + decltype(dc)::value); });
    sfor<0, AD>([&](auto dc) { ktile_ad(dc, IC<0>{}, IC<(decltype(dc)::value == AD - 1)>{}, 0); });
  } else {
  if constexpr (!LW) {
    sfor<0, NS>([&](auto sc_) { sfor<0, G>([&](auto gc) { issue1(gc, decltype(sc_)::value, decltype(sc_)::value * ST_BYTES); }); });
    WAIT_V((NS - 1) * G);
  }
  BAR();
  STAMP_MID(MODE == 0 ? 2 : MODE == 1 ? 4 : (MODE == 2 || MODE == 5) ? 6 : 7, 0);
  sfor<0, 2 * NH>([&](auto rc) { rd_b(rc, b0, smem, 0, 0); });
  sfor<0, MI>([&](auto rc) { rd_a(rc, a0, smem, 0); });
  using TT = IC<1>; using FF = IC<0>;
  ktile(IC<(NS - 2) * G>{}, FF{}, TT{}, 0);
  for (int t = 1; t < NT - NS; ++t) ktile(IC<(NS - 2) * G>{}, TT{}, TT{}, t);
  ktile(IC<(NS - 2) * G>{}, TT{}, FF{}, NT - NS);
  sfor<0, NS - 2>([&](auto rc) { ktile(IC<(NS - 3 - decltype(rc)::value) * G>{}, FF{}, FF{}, 0); });
  ktile(IC<-1>{}, FF{}, FF{}, 0);
  }

  STAMP_MID(MODE == 0 ? 2 : MODE == 1 ? 4 : (MODE == 2 || MODE == 5) ? 6 : 7, 1);
  if constexpr (MODE == 0) {
    const float qs = 0.17677669529663687f * 1.4426950408889634f;
    BAR();
    char* st = smem + wid * (64 * 208);
#pragma unroll
    for (int ni = 0; ni < NI; ++ni) {
      const int n = tn * BN + wn * TN + ni * 16;
      const float sc = n < 1024 ? qs : 1.0f;
#pragma unroll
      for (int mi = 0; mi < MI; ++mi) {
        u32x2 w;
        w[0] = pack2(acc[mi][ni][0] * sc, acc[mi][ni][1] * sc);
        w[1] = pack2(acc[mi][ni][2] * sc, acc[mi][ni][3] * sc);
        *(LAS u32x2*)(st + (mi * 16 + fr) * 208 + ni * 32 + fq * 8) = w;
      }
    }
#pragma unroll
    for (int j = 0; j < 3; ++j) {
      const int hh = (tn * BN + wn * TN + j * 32) >> 5;
      u16* ob = p.out + ((size_t)hh * S_ + m0 + wm * TM) * 32 + lane * 8;
#pragma unroll
      for (int i = 0; i < 4; ++i) {
        const u32x4 v = *(const LAS u32x4*)(st + (i * 16 + (lane >> 2)) * 208 + j * 64 + (lane & 3) * 16);
        ST16(ob + i * 512, v);
      }
    }
  } else if constexpr (MODE == 3) {
    const int tile = tm * 8 + tn;
    if (wm != kh) {
      float* dst = p.slab + ((size_t)(tile * 2 + kh) * 2 + wn) * 4096 + lane * 4;
#pragma unroll
      for (int mi = 0; mi < MI; ++mi)
#pragma unroll
        for (int ni = 0; ni < NI; ++ni) ST16(dst + (mi * NI + ni) * 256, __builtin_bit_cast(u32x4, acc[mi][ni]));
      WAIT_V(0);
      if (lane == 0) __hip_atomic_fetch_add(p.flags + (tile * 2 + kh) * 32, 1u, __ATOMIC_RELAXED, __HIP_MEMORY_SCOPE_AGENT);
      BAR();
    } else {
      const unsigned* fl = p.flags + (tile * 2 + (kh ^ 1)) * 32;
      for (int spin = 0; spin < (1 << 22); ++spin) {
        if (__builtin_amdgcn_readfirstlane(__hip_atomic_load(fl, __ATOMIC_RELAXED, __HIP_MEMORY_SCOPE_AGENT)) >= 2u) break;
        __builtin_amdgcn_s_sleep(1);
      }
      const float* src = p.slab + ((size_t)(tile * 2 + (kh ^ 1)) * 2 + wn) * 4096 + lane * 4;
      f32x4 pv[MI][NI];
#pragma unroll
      for (int mi = 0; mi < MI; ++mi)
#pragma unroll
        for (int ni = 0; ni < NI; ++ni)
          asm volatile("global_load_dwordx4 %0, %1, off sc0 sc1" : "=v"(pv[mi][ni]) : "v"(src + (mi * NI + ni) * 256) : "memory");
      BAR();
      asm volatile("s_waitcnt vmcnt(0)" ::: "memory");
      SBAR();
#pragma unroll
      for (int ni = 0; ni < NI; ++ni)
#pragma unroll
        for (int mi = 0; mi < MI; ++mi) {
          asm volatile("" : "+v"(pv[mi][ni]));
          const f32x4 a = acc[mi][ni] + pv[mi][ni];
          u32x2 w;
          w[0] = pack2(gv[ni][0] * rbf(a[0]), gv[ni][1] * rbf(a[1]));
          w[1] = pack2(gv[ni][2] * rbf(a[2]), gv[ni][3] * rbf(a[3]));
          *(LAS u32x2*)(smem + (mi * 16 + fr) * 272 + wn * 128 + ni * 32 + fq * 8) = w;
        }
    }
    WAIT_L0();
    BAR();
#pragma unroll
    for (int i = 0; i < 4; ++i) {
      const u32x4 v = *(const LAS u32x4*)(smem + (i * 16 + (tid >> 4)) * 272 + (tid & 15) * 16);
      u32x4 w;
#pragma unroll
      for (int j = 0; j < 4; ++j) w[j] = pack2(lo_f(rv[i][j]) + lo_f(v[j]), hi_f(rv[i][j]) + hi_f(v[j]));
      ST16(p.out + (size_t)(m0 + kh * 64 + i * 16 + (tid >> 4)) * D_ + tn * BN + (tid & 15) * 8, w);
    }
  } else if constexpr (MODE == 1 || MODE == 4) {
    BAR();
#pragma unroll
    for (int ni = 0; ni < NI; ++ni) {
#pragma unroll
      for (int mi = 0; mi < MI; ++mi) {
        u32x2 w;
        w[0] = pack2(gv[ni][0] * rbf(acc[mi][ni][0]), gv[ni][1] * rbf(acc[mi][ni][1]));
        w[1] = pack2(gv[ni][2] * rbf(acc[mi][ni][2]), gv[ni][3] * rbf(acc[mi][ni][3]));
        *(LAS u32x2*)(smem + (wm * TM + mi * 16 + fr) * 144 + wn * 64 + ni * 32 + fq * 8) = w;
      }
    }
    WAIT_L0();
    BAR();
    u32x4 xw[4];
#pragma unroll
    for (int i = 0; i < 4; ++i) {
      const u32x4 v = *(const LAS u32x4*)(smem + (i * 32 + (tid >> 3)) * 144 + (tid & 7) * 16);
#pragma unroll
      for (int j = 0; j < 4; ++j) xw[i][j] = pack2(lo_f(rv[i][j]) + lo_f(v[j]), hi_f(rv[i][j]) + hi_f(v[j]));
      ST16(p.out + (size_t)(m0 + i * 32 + (tid >> 3)) * D_ + tn * BN + (tid & 7) * 8, xw[i]);
    }
  } else if constexpr (MODE == 5) {
    BAR();
    char* st = smem + wid * (32 * 240);
#pragma unroll
    for (int ni = 0; ni < 7; ++ni)
#pragma unroll
      for (int mi = 0; mi < MI; ++mi) {
        float o[4];
#pragma unroll
        for (int r = 0; r < 4; ++r) o[r] = rbf(acc[mi][ni][r]) * rbf(acc[mi][ni + 7][r]);
        u32x2 w;
        w[0] = pack2(o[0], o[1]);
        w[1] = pack2(o[2], o[3]);
        *(LAS u32x2*)(st + (mi * 16 + fr) * 240 + ni * 32 + fq * 8) = w;
      }
#pragma unroll
    for (int i = 0; i < 7; ++i) {
      const int cidx = i * 64 + lane, row = cidx / 14, ch = cidx - row * 14;
      const u32x4 v = *(const LAS u32x4*)(st + row * 240 + ch * 16);
      ST16(p.out + (size_t)(m0 + wm * TM + row) * F_ + tn * 112 + ch * 8, v);
    }
  } else {
    BAR();
    char* st = smem + wid * (64 * 144);
#pragma unroll
    for (int ni = 0; ni < 4; ++ni)
#pragma unroll
      for (int mi = 0; mi < MI; ++mi) {
        float o[4];
#pragma unroll
        for (int r = 0; r < 4; ++r) o[r] = rbf(acc[mi][ni][r]) * rbf(acc[mi][ni + 4][r]);
        u32x2 w;
        w[0] = pack2(o[0], o[1]);
        w[1] = pack2(o[2], o[3]);
        *(LAS u32x2*)(st + (mi * 16 + fr) * 144 + ni * 32 + fq * 8) = w;
      }
    u16* ob = p.out + (size_t)(m0 + wm * TM + (lane >> 3)) * F_ + tn * 128 + wn * 64 + (lane & 7) * 8;
#pragma unroll
    for (int i = 0; i < 8; ++i) {
      const u32x4 v = *(const LAS u32x4*)(st + (i * 8 + (lane >> 3)) * 144 + (lane & 7) * 16);
      ST16(ob + (size_t)i * 8 * F_, v);
    }
  }
  STAMP_END(MODE == 0 ? 2 : MODE == 1 ? 4 : (MODE == 2 || MODE == 5) ? 6 : 7);
}

#define AT_NSTAGE 6
constexpr int AT_KT = 128, AT_NS = AT_NSTAGE, AT_ST = 16384;
#define MFMA32(a, b, c) __builtin_amdgcn_mfma_f32_32x32x16_bf16(a, b, c, 0, 0, 0)
#define AEXP(x) __builtin_amdgcn_exp2f(x)
#define MX3(a, b, c) __builtin_fmaxf(__builtin_fmaxf((a), (b)), (c))

__device__ __forceinline__ float tile_rowmax(const f32x16& s0, const f32x16& s1) {
  float a = MX3(s0[0], s0[1], s1[0]), b = MX3(s0[2], s0[3], s1[1]);
  a = MX3(a, s1[2], s1[3]);
#pragma unroll
  for (int r = 4; r < 16; r += 4) {
    a = MX3(a, s0[r], s0[r + 1]); b = MX3(b, s0[r + 2], s0[r + 3]);
    a = MX3(a, s1[r], s1[r + 1]); b = MX3(b, s1[r + 2], s1[r + 3]);
  }
  float mx = __builtin_fmaxf(a, b);
  auto rr = __builtin_amdgcn_permlane32_swap(__float_as_uint(mx), __float_as_uint(mx), false, false);
  return __builtin_fmaxf(__uint_as_float(rr[0]), __uint_as_float(rr[1]));
}

__global__ __launch_bounds__(512, 2) void attn_kernel(const u16* __restrict__ qkv, u16* __restrict__ o) {
  extern __shared__ __attribute__((aligned(16))) char smem[];
  STAMP_BEGIN();
  const int tid = threadIdx.x, lane = tid & 63;
  const int wid = __builtin_amdgcn_readfirstlane(tid >> 6);
  const int x = blockIdx.x & 7, idx = blockIdx.x >> 3;
  const int hd = x * 4 + (idx >> 3), qb = idx & 7;
  const int r32 = lane & 31, hi = lane >> 5;
  const u16* Q = qkv + ((size_t)(0 * 32 + hd) * S_) * 32;
  const u16* Kp = qkv + ((size_t)(1 * 32 + hd) * S_) * 32;
  const u16* Vp = qkv + ((size_t)(2 * 32 + hd) * S_) * 32;
  const int q0 = qb * 256 + wid * 32;

  const unsigned k_voff = (unsigned)(((lane >> 2) * 32 + 8 * ((lane & 3) ^ ((lane >> 4) & 3))) * 2);
  const unsigned v_voff = (unsigned)(lane * 16);
  const unsigned lds0 = (unsigned)(size_t)(LAS char*)smem;
  auto issue = [&](int t, int bufoff) {
    const char* gk = (const char*)(Kp + ((size_t)t * AT_KT + wid * 16) * 32);
    const char* gv = (const char*)(Vp + ((size_t)t * AT_KT + wid * 16) * 32);
    glds16(gk, k_voff, lds0 + bufoff + wid * 1024);
    glds16(gv, v_voff, lds0 + bufoff + 8192 + wid * 1024);
  };
#pragma unroll
  for (int s = 0; s < AT_NS - 1; ++s) issue(s, s * AT_ST);
  const bf16x8 qf0 = *(const bf16x8*)(Q + (size_t)(q0 + r32) * 32 + hi * 8);
  const bf16x8 qf1 = *(const bf16x8*)(Q + (size_t)(q0 + r32) * 32 + 16 + hi * 8);

  const int k_rd0 = r32 * 64 + ((hi ^ ((r32 >> 2) & 3)) << 4);
  const int k_rd1 = k_rd0 ^ 32;
  const int v_rd = 8192 + (4 * hi + ((lane & 15) >> 2)) * 64 + ((lane >> 4) & 1) * 32 + (lane & 3) * 8;

  f32x16 oacc = {};
  f32x16 negm;
  float m, l = 0.f;
  f32x16 sa0, sa1, sb0, sb1;
  u32x4 pp0 = {}, pp1 = {}, pp2 = {}, pp3 = {};
  bf16x8 vf0, vf1, vf2, vf3;
#define AT_BIG 1.0e30f
  constexpr float BIG = AT_BIG;
  constexpr int NT = S_ / AT_KT;

#define SCORES(D0, D1, kb, CIN)                                                         \
  do {                                                                                  \
    bf16x8 k00 = lds_read16((kb) + k_rd0), k01 = lds_read16((kb) + k_rd1);              \
    bf16x8 k10 = lds_read16((kb) + 2048 + k_rd0), k11 = lds_read16((kb) + 2048 + k_rd1);\
    D0 = MFMA32(k00, qf0, CIN); D1 = MFMA32(k10, qf0, CIN);                             \
    D0 = MFMA32(k01, qf1, D0);  D1 = MFMA32(k11, qf1, D1);                              \
  } while (0)
#define VFRAG(vb, s) __builtin_shufflevector(tr_read((vb) + (s) * 1024), tr_read((vb) + (s) * 1024 + 512), 0, 1, 2, 3, 4, 5, 6, 7)
#define READV(vb) do { vf0 = VFRAG(vb, 0); vf1 = VFRAG(vb, 1); vf2 = VFRAG(vb, 2); vf3 = VFRAG(vb, 3); } while (0)
#define PV()                                                                            \
  do {                                                                                  \
    oacc = MFMA32(vf0, __builtin_bit_cast(bf16x8, pp0), oacc);                          \
    oacc = MFMA32(vf1, __builtin_bit_cast(bf16x8, pp1), oacc);                          \
    oacc = MFMA32(vf2, __builtin_bit_cast(bf16x8, pp2), oacc);                          \
    oacc = MFMA32(vf3, __builtin_bit_cast(bf16x8, pp3), oacc);                          \
  } while (0)
#define EXPSUM(C0, C1, LS)                                                              \
  do {                                                                                  \
    f32x2 ls2_ = {0.f, 0.f}, ls3_ = {0.f, 0.f};                                         \
    _Pragma("unroll") for (int r = 0; r < 16; r += 2) {                                 \
      C0[r] = AEXP(C0[r]); C0[r + 1] = AEXP(C0[r + 1]);                                 \
      C1[r] = AEXP(C1[r]); C1[r + 1] = AEXP(C1[r + 1]);                                 \
      ls2_ += (f32x2){C0[r], C0[r + 1]};                                                \
      ls3_ += (f32x2){C1[r], C1[r + 1]};                                                \
    }                                                                                   \
    ls2_ += ls3_;                                                                       \
    LS = ls2_[0] + ls2_[1];                                                             \
  } while (0)
#define PACK(C0, C1)                                                                    \
  do {                                                                                  \
    _Pragma("unroll") for (int j = 0; j < 4; ++j) {                                     \
      pp0[j] = pack2(C0[2 * j], C0[2 * j + 1]);                                         \
      pp1[j] = pack2(C0[8 + 2 * j], C0[8 + 2 * j + 1]);                                 \
      pp2[j] = pack2(C1[2 * j], C1[2 * j + 1]);                                         \
      pp3[j] = pack2(C1[8 + 2 * j], C1[8 + 2 * j + 1]);                                 \
    }                                                                                   \
  } while (0)
#define STEP(C0, C1, N0, N1, kcur, knext, vprev, HASN)                                  \
  do {                                                                                  \
    READV(vprev);                                                                       \
    if (HASN) SCORES(N0, N1, knext, negm);                                              \
    PV();                                                                               \
    float ls;                                                                           \
    EXPSUM(C0, C1, ls);                                                                 \
    if (__builtin_expect(__any(!(ls <= BIG)), 0)) {                                     \
                  \
      f32x16 z_ = {};                                                                   \
      SCORES(C0, C1, kcur, z_);                                                         \
      const float mn = __builtin_fmaxf(m, tile_rowmax(C0, C1));                         \
      const float dm = m - mn;                                                          \
      const float f = __builtin_amdgcn_exp2f(dm);                                       \
      m = mn; l *= f;                                                                   \
      _Pragma("unroll") for (int r = 0; r < 16; ++r) {                                  \
        oacc[r] *= f; negm[r] = -mn; C0[r] -= mn; C1[r] -= mn;                          \
        if (HASN) { N0[r] += dm; N1[r] += dm; }                                         \
      }                                                                                 \
      EXPSUM(C0, C1, ls);                                                               \
    }                                                                                   \
    l += ls;                                                                            \
    PACK(C0, C1);                                                                       \
  } while (0)

  WAIT_V(2 * (AT_NS - 2));
  BAR();
  {
    f32x16 z = {};
    SCORES(sa0, sa1, smem, z);
    m = tile_rowmax(sa0, sa1);
#pragma unroll
    for (int r = 0; r < 16; ++r) { negm[r] = -m; sa0[r] -= m; sa1[r] -= m; }
  }
  int cur = 0, prv = 0;
  for (int t = 0; t < NT - 1; ++t) {
    const char* sb = smem + cur;
    int nx = cur + AT_ST; if (nx >= AT_NS * AT_ST) nx = 0;
    int prv2 = cur - AT_ST; if (prv2 < 0) prv2 = (AT_NS - 1) * AT_ST;
    STEP(sa0, sa1, sb0, sb1, sb, sb + 4096, smem + prv + 4096 + v_rd, true);
    if (t + AT_NS - 1 < NT) {
      WAIT_V(2 * (AT_NS - 3));
      WAIT_L0();
      BAR();
      issue(t + AT_NS - 1, prv2);
    } else {
      WAIT_V(0);
      WAIT_L0();
      BAR();
    }
    STEP(sb0, sb1, sa0, sa1, sb + 4096, smem + nx, sb + v_rd, true);
    prv = cur; cur = nx;
  }
  {
    const char* sb = smem + cur;
    STEP(sa0, sa1, sb0, sb1, sb, sb + 4096, smem + prv + 4096 + v_rd, true);
    STEP(sb0, sb1, sa0, sa1, sb + 4096, sb, sb + v_rd, false);
    prv = cur;
  }
  READV(smem + prv + 4096 + v_rd);
  PV();
#undef SCORES
#undef VFRAG
#undef READV
#undef PV
#undef EXPSUM
#undef PACK
#undef STEP
  {
    auto rr = __builtin_amdgcn_permlane32_swap(__float_as_uint(l), __float_as_uint(l), false, false);
    l = __uint_as_float(rr[0]) + __uint_as_float(rr[1]);
  }
  const float inv = 1.0f / l;
  u16* op = o + (size_t)(q0 + r32) * D_ + hd * 32 + 4 * hi;
#pragma unroll
  for (int g = 0; g < 4; ++g) {
    u32x2 w;
    w[0] = pack2(oacc[4 * g] * inv, oacc[4 * g + 1] * inv);
    w[1] = pack2(oacc[4 * g + 2] * inv, oacc[4 * g + 3] * inv);
    *(u32x2*)(op + 8 * g) = w;
  }
  STAMP_END(3);
}

constexpr int A2_KT = 256, A2_NS = 3, A2_ST = 32768, A2_MOD = A2_NS * A2_ST + 1024, A2_RED = A2_MOD + 32768, A2_FLG = A2_RED + 4096, A2_LDS = A2_FLG + 64;
template <bool SAFE>
__device__ __forceinline__ void attn_pass(char* smem, const u16* Kp, const u16* Vp, bf16x8 qf0, bf16x8 qf1, int wid, int lane,
                                          f32x16& oacc, float& m, float& l,
                                          const u16* pw0, const u16* pw1, const u16* pw2, const u16* pw3, const u16* wm, unsigned cc) {
  const int wk = wid >> 3, r32 = lane & 31, hi = lane >> 5;
  const unsigned k_voff = (unsigned)(((lane >> 2) * 32 + 8 * ((lane & 3) ^ ((lane >> 4) & 3))) * 2);
  const unsigned v_voff = (unsigned)(lane * 16);
  const unsigned lds0 = (unsigned)(size_t)(LAS char*)smem;
  auto issue = [&](int t, int bufoff) {
    const char* gk = (const char*)(Kp + ((size_t)t * A2_KT + wid * 16) * 32);
    const char* gv = (const char*)(Vp + ((size_t)t * A2_KT + wid * 16) * 32);
    glds16(gk, k_voff, lds0 + bufoff + wid * 1024);
    glds16(gv, v_voff, lds0 + bufoff + 16384 + wid * 1024);
  };
  issue(0, 0);
  issue(1, A2_ST);
  const int k_rd0 = wk * 8192 + r32 * 64 + ((hi ^ ((r32 >> 2) & 3)) << 4);
  const int k_rd1 = k_rd0 ^ 32;
  const int v_rd = 16384 + wk * 8192 + (4 * hi + ((lane & 15) >> 2)) * 64 + ((lane >> 4) & 1) * 32 + (lane & 3) * 8;
  f32x16 negm = {};
  f32x2 lsa = {0.f, 0.f}, lsb = {0.f, 0.f};
  oacc = (f32x16){};
  m = 0.f; l = 0.f;
  constexpr int NT = S_ / A2_KT;
  int cur = 0;
  for (int t = 0; t < NT; ++t) {
    if (t == 0) WAIT_V(2); else if (!SAFE && t == 4) WAIT_V(5); else if (t + 1 < NT) WAIT_V(3); else WAIT_V(0);
    BAR();
    if (!SAFE && t == 6) {
      const int rg = wid * 8 + (lane >> 3), cl = lane & 7;
      const float c0 = lo_f(cc), c1 = hi_f(cc);
      const float s0 = rbf(c0 / (1.0f + __expf(-c0))), s1 = rbf(c1 / (1.0f + __expf(-c1)));
      const u32x4 w0 = *(const LAS u32x4*)(smem + A2_MOD + (2 * rg) * 128 + cl * 16);
      const u32x4 w1 = *(const LAS u32x4*)(smem + A2_MOD + (2 * rg + 1) * 128 + cl * 16);
      float a[8];
#pragma unroll
      for (int j = 0; j < 4; ++j) {
        a[2 * j] = s0 * lo_f(w0[j]) + s1 * lo_f(w1[j]);
        a[2 * j + 1] = s0 * hi_f(w0[j]) + s1 * hi_f(w1[j]);
      }
#pragma unroll
      for (int j = 0; j < 8; ++j) {
        a[j] += __shfl_xor(a[j], 8); a[j] += __shfl_xor(a[j], 16); a[j] += __shfl_xor(a[j], 32);
      }
      if (lane < 8) {
        float* mred = (float*)(smem + A2_RED);
#pragma unroll
        for (int j = 0; j < 8; ++j) mred[wid * 64 + lane * 8 + j] = a[j];
      }
    }
    if (!SAFE && t == 3) {
      const unsigned m_voff = (unsigned)(((lane >> 3) * 6144 + (lane & 7) * 8) * 2);
      glds16(wm + (size_t)(wid * 16) * 6144, m_voff, lds0 + A2_MOD + wid * 2048);
      glds16(wm + (size_t)(wid * 16 + 8) * 6144, m_voff, lds0 + A2_MOD + wid * 2048 + 1024);
    }
    {
      if (t + 2 < NT) {
        int ck = (blockIdx.x * 6 + t) * 16 + wid;
        if (ck >= 2048 + 3 * 7168) ck -= 16384;
        const u16* pb = pw0;
        if (ck >= 2048) { pb = pw1; ck -= 2048; if (ck >= 7168) { pb = pw2; ck -= 7168; if (ck >= 7168) { pb = pw3; ck -= 7168; } } }
        glds16((const char*)pb + (size_t)ck * 1024, v_voff, lds0 + A2_NS * A2_ST);
      }
      int nb = cur + 2 * A2_ST; if (nb >= A2_NS * A2_ST) nb -= A2_NS * A2_ST;
      if (t + 2 < NT) issue(t + 2, nb);
    }
#pragma unroll
    for (int hb = 0; hb < 2; ++hb) {
      const char* kb = smem + cur + hb * 4096;
      const bf16x8 k00 = lds_read16(kb + k_rd0), k01 = lds_read16(kb + k_rd1);
      const bf16x8 k10 = lds_read16(kb + 2048 + k_rd0), k11 = lds_read16(kb + 2048 + k_rd1);
      f32x16 s0, s1;
      if (SAFE || (t == 0 && hb == 0)) {
        f32x16 z_ = {};
        s0 = MFMA32(k00, qf0, z_); s1 = MFMA32(k10, qf0, z_);
        s0 = MFMA32(k01, qf1, s0); s1 = MFMA32(k11, qf1, s1);
        const float tm = tile_rowmax(s0, s1);
        const bool first = (t == 0 && hb == 0);
        const float mn = first ? tm : __builtin_fmaxf(m, tm);
        if (SAFE) {
          const float f = first ? 0.f : __builtin_amdgcn_exp2f(m - mn);
          lsa *= f; lsb *= f;
          oacc *= f;
        }
        m = mn;
#pragma unroll
        for (int r = 0; r < 16; ++r) { negm[r] = -mn; s0[r] -= mn; s1[r] -= mn; }
      } else {
        s0 = MFMA32(k00, qf0, negm); s1 = MFMA32(k10, qf0, negm);
        s0 = MFMA32(k01, qf1, s0); s1 = MFMA32(k11, qf1, s1);
      }
#pragma unroll
      for (int r = 0; r < 16; r += 2) {
        s0[r] = AEXP(s0[r]); s0[r + 1] = AEXP(s0[r + 1]);
        s1[r] = AEXP(s1[r]); s1[r + 1] = AEXP(s1[r + 1]);
        lsa += (f32x2){s0[r], s0[r + 1]};
        lsb += (f32x2){s1[r], s1[r + 1]};
      }
      u32x4 p0, p1, p2, p3;
#pragma unroll
      for (int j = 0; j < 4; ++j) {
        p0[j] = pack2(s0[2 * j], s0[2 * j + 1]);
        p1[j] = pack2(s0[8 + 2 * j], s0[8 + 2 * j + 1]);
        p2[j] = pack2(s1[2 * j], s1[2 * j + 1]);
        p3[j] = pack2(s1[8 + 2 * j], s1[8 + 2 * j + 1]);
      }
      const char* vb = smem + cur + hb * 4096 + v_rd;
#define VFRAG(s) __builtin_shufflevector(tr_read(vb + (s) * 1024), tr_read(vb + (s) * 1024 + 512), 0, 1, 2, 3, 4, 5, 6, 7)
      oacc = MFMA32(VFRAG(0), __builtin_bit_cast(bf16x8, p0), oacc);
      oacc = MFMA32(VFRAG(1), __builtin_bit_cast(bf16x8, p1), oacc);
      oacc = MFMA32(VFRAG(2), __builtin_bit_cast(bf16x8, p2), oacc);
      oacc = MFMA32(VFRAG(3), __builtin_bit_cast(bf16x8, p3), oacc);
#undef VFRAG
    }
    WAIT_L0();
    cur += A2_ST; if (cur >= A2_NS * A2_ST) cur = 0;
  }
  lsa += lsb;
  l = lsa[0] + lsa[1];
  {
    auto rr = __builtin_amdgcn_permlane32_swap(__float_as_uint(l), __float_as_uint(l), false, false);
    l = __uint_as_float(rr[0]) + __uint_as_float(rr[1]);
  }
  WAIT_V(0);
}

__global__ __launch_bounds__(1024, 4) void attn2_kernel(const u16* __restrict__ qkv, u16* __restrict__ o,
                                                        const u16* pw0, const u16* pw1, const u16* pw2, const u16* pw3,
                                                        const u16* __restrict__ cvec, const u16* wmod, float* __restrict__ modp) {
  extern __shared__ __attribute__((aligned(16))) char smem[];
  STAMP_BEGIN();
  const int tid = threadIdx.x, lane = tid & 63;
  const int wid = __builtin_amdgcn_readfirstlane(tid >> 6);
  const int wq = wid & 7, wk = wid >> 3;
  const int x = blockIdx.x & 7, idx = blockIdx.x >> 3;
  const int hd = x * 4 + (idx >> 3), qb = idx & 7;
  const int r32 = lane & 31, hi = lane >> 5;
  const u16* Q = qkv + ((size_t)(0 * 32 + hd) * S_) * 32;
  const u16* Kp = qkv + ((size_t)(1 * 32 + hd) * S_) * 32;
  const u16* Vp = qkv + ((size_t)(2 * 32 + hd) * S_) * 32;
  const int q0 = qb * 256 + wq * 32;
  const bf16x8 qf0 = *(const bf16x8*)(Q + (size_t)(q0 + r32) * 32 + hi * 8);
  const bf16x8 qf1 = *(const bf16x8*)(Q + (size_t)(q0 + r32) * 32 + 16 + hi * 8);

  f32x16 oacc;
  float m, l;
  const int mcg = 32 + (blockIdx.x >> 2), mks = blockIdx.x & 3;
  const u16* wm = wmod + (size_t)(mks * 256) * 6144 + mcg * 64;
  unsigned cc = *(const unsigned*)(cvec + mks * 256 + 2 * (tid >> 3));
  asm volatile("" : "+v"(cc));
  attn_pass<false>(smem, Kp, Vp, qf0, qf1, wid, lane, oacc, m, l, pw0, pw1, pw2, pw3, wm, cc);
  float* xb = (float*)smem + wq * (20 * 64);
  const float* mred = (const float*)(smem + A2_RED);
  volatile LAS int* badf = (volatile LAS int*)(smem + A2_FLG);
  {
    float chk = l;
#pragma unroll
    for (int r = 0; r < 16; ++r) chk += __builtin_fabsf(oacc[r]);
    const int bad = __any(!(chk < 1.0e37f));
    if (lane == 0) badf[wid] = bad;
    __syncthreads();
    const int anybad = __any(badf[lane & 15] != 0);
    if (anybad) {
      __syncthreads();
      attn_pass<true>(smem, Kp, Vp, qf0, qf1, wid, lane, oacc, m, l, pw0, pw1, pw2, pw3, wm, cc);
      __syncthreads();
    }
  }
  xb[wk * 64 + lane] = m; xb[(2 + wk) * 64 + lane] = l;
#pragma unroll
  for (int r = 0; r < 8; ++r) xb[(4 + 8 * wk + r) * 64 + lane] = wk ? oacc[r] : oacc[8 + r];
  __syncthreads();
  {
    const float m2 = xb[(wk ^ 1) * 64 + lane], l2 = xb[(2 + (wk ^ 1)) * 64 + lane];
    const float mn = __builtin_fmaxf(m, m2);
    const float f1 = __builtin_amdgcn_exp2f((m - mn) * 0.5f), f2 = __builtin_amdgcn_exp2f((m2 - mn) * 0.5f);
    const float inv = 1.0f / ((l * f1) * f1 + (l2 * f2) * f2);
    u16* op = o + ((size_t)hd * S_ + q0 + r32) * 32 + 4 * hi + 16 * wk;
#pragma unroll
    for (int g = 0; g < 2; ++g) {
      float v[4];
#pragma unroll
      for (int e = 0; e < 4; ++e) {
        const float own = wk ? oacc[8 + 4 * g + e] : oacc[4 * g + e];
        v[e] = ((own * f1) * f1 + (xb[(4 + 8 * (wk ^ 1) + 4 * g + e) * 64 + lane] * f2) * f2) * inv;
      }
      u32x2 w;
      w[0] = pack2(v[0], v[1]);
      w[1] = pack2(v[2], v[3]);
      *(u32x2*)(op + 8 * g) = w;
    }
  }
  if (wid == 15) {
    float sacc = 0.f;
#pragma unroll
    for (int w = 0; w < 16; ++w) sacc += mred[w * 64 + lane];
    modp[mks * 6144 + mcg * 64 + lane] = sacc;
  }
  STAMP_END(3);
}

template <int MODE> static void launch_gemm(const GemmP& p, hipStream_t stream) {
  using C = GCfg<MODE>;
  constexpr int lds0_ = C::NS * ((C::AD ? 0 : C::BM) + C::BN) * 128, lds = lds0_ < 20480 ? 20480 : lds0_;
  hipFuncSetAttribute((const void*)gemm_kernel<MODE>, hipFuncAttributeMaxDynamicSharedMemorySize, lds);
  gemm_kernel<MODE><<<C::NTM * C::NTN, C::WM * C::WN * 64 * (C::LW ? 2 : 1), lds, stream>>>(p);
}

extern "C" void kernel_launch(void* const* d_in, const int* in_sizes, int n_in, void* d_out, int out_size,
                              void* d_ws, size_t ws_size, hipStream_t stream) {
  const u16* x = (const u16*)d_in[0];
  const u16* wqkv = (const u16*)d_in[1];
  const u16* wo = (const u16*)d_in[2];
  const u16* wg = (const u16*)d_in[4];
  const u16* wu = (const u16*)d_in[5];
  const u16* wd = (const u16*)d_in[6];
  const u16* c = (const u16*)d_in[7];
  const u16* wmod = (const u16*)d_in[8];
  u16* out = (u16*)d_out;

  char* ws = (char*)d_ws;
  float* mod = (float*)ws;
  u16* h = (u16*)(ws + (1 << 20));
  u16* qkv = (u16*)(ws + (8 << 20));
  u16* ao = (u16*)(ws + (24 << 20));
  u16* x1 = (u16*)(ws + (32 << 20));
  u16* hh = (u16*)(ws + (40 << 20));
  float* slab = (float*)(ws + (64 << 20));
  unsigned* flags = (unsigned*)(ws + (512 << 10));

  mod_kernel<<<256, 256, 0, stream>>>(c, wmod, mod, x, wqkv, flags);
  ln_kernel<<<512 + 192, 256, 0, stream>>>(x, mod, 0, h, wqkv, c, wmod, mod, 1);
  { GemmP p{h, wqkv, nullptr, nullptr, nullptr, qkv, nullptr, nullptr, nullptr}; launch_gemm<0>(p, stream); }
  hipFuncSetAttribute((const void*)attn2_kernel, hipFuncAttributeMaxDynamicSharedMemorySize, A2_LDS);
  attn2_kernel<<<256, 1024, A2_LDS, stream>>>(qkv, ao, wo, wg, wu, wd, c, wmod, mod);
  { GemmP p{ao, wo, nullptr, x, mod + 2 * 1024, x1, slab, flags, h}; launch_gemm<1>(p, stream);
  }
  ln_kernel<<<512, 256, 0, stream>>>(x1, mod, 3 * 1024, h, wqkv, c, wmod, mod, 5);
  { GemmP p{h, wg, wu, nullptr, nullptr, hh, nullptr, nullptr, nullptr}; launch_gemm<2>(p, stream); }
  { GemmP p{hh, wd, nullptr, x1, mod + 5 * 1024, out, slab, flags, nullptr}; launch_gemm<4>(p, stream); }
}
```

```cpp
#include <hip/hip_runtime.h>
#include <hip/hip_bf16.h>
#include <stdint.h>

typedef unsigned short u16;
typedef short bf16x8 __attribute__((ext_vector_type(8)));
typedef short s16x4 __attribute__((ext_vector_type(4)));
typedef float f32x2 __attribute__((ext_vector_type(2)));
typedef float f32x4 __attribute__((ext_vector_type(4)));
typedef float f32x16 __attribute__((ext_vector_type(16)));
typedef unsigned u32x2 __attribute__((ext_vector_type(2)));
typedef unsigned u32x4 __attribute__((ext_vector_type(4)));
typedef __bf16 bf16x2_t __attribute__((ext_vector_type(2)));
#define LAS __attribute__((address_space(3)))

constexpr int S_ = 2048, D_ = 1024, F_ = 3584;

__device__ __forceinline__ float bf2f(u16 u) { return __uint_as_float((unsigned)u << 16); }
__device__ __forceinline__ unsigned pack2(float lo, float hi) {
  f32x2 v = {lo, hi};
  bf16x2_t b = __builtin_convertvector(v, bf16x2_t);
  return __builtin_bit_cast(unsigned, b);
}
__device__ __forceinline__ float rbf(float f) {
  __bf16 b = (__bf16)f;
  return __uint_as_float((unsigned)__builtin_bit_cast(u16, b) << 16);
}
__device__ __forceinline__ float lo_f(unsigned u) { return __uint_as_float(u << 16); }
__device__ __forceinline__ float hi_f(unsigned u) { return __uint_as_float(u & 0xffff0000u); }

#define WAIT_V(n) asm volatile("s_waitcnt vmcnt(%0)" ::"n"(n) : "memory")
#define WAIT_L0() asm volatile("s_waitcnt lgkmcnt(0)" ::: "memory")
#define BAR() __builtin_amdgcn_s_barrier()
#define SBAR() __builtin_amdgcn_sched_barrier(0)
#define MFMA16(c, a, b) c = __builtin_amdgcn_mfma_f32_16x16x32_bf16(a, b, c, 0, 0, 0)
#define MFMA32(a, b, c) __builtin_amdgcn_mfma_f32_32x32x16_bf16(a, b, c, 0, 0, 0)
#define ST16(p, v) asm volatile("global_store_dwordx4 %0, %1, off sc0 sc1\n\ts_nop 1" ::"v"(p), "v"(v) : "memory")

__device__ __forceinline__ void glds16(const void* sbase, unsigned voff, unsigned ldsaddr) {
  asm volatile("s_mov_b32 m0, %2\n\ts_nop 0\n\tglobal_load_lds_dwordx4 %0, %1" ::"v"(voff), "s"(sbase), "s"(ldsaddr) : "memory");
}
template <int I> struct IC { static constexpr int value = I; };
template <int I, int N, class F> __device__ __forceinline__ void sfor(F&& f) {
  if constexpr (I < N) { f(IC<I>{}); sfor<I + 1, N>(f); }
}
__device__ __forceinline__ s16x4 tr_read(const char* p) { return __builtin_amdgcn_ds_read_tr16_b64_v4i16((LAS s16x4*)p); }
__device__ __forceinline__ bf16x8 lds_read16(const char* p) { return *(const LAS bf16x8*)p; }

__device__ __forceinline__ void touch_block(const char* src) {
  u32x4 v[8];
#pragma unroll
  for (int i = 0; i < 8; ++i) v[i] = *(const u32x4*)(src + i * 4096 + threadIdx.x * 16);
#pragma unroll
  for (int i = 0; i < 8; ++i) asm volatile("" ::"v"(v[i]));
}
__device__ __forceinline__ f32x4 ld_mod4(const float* modp, int idx) {
  f32x4 a = *(const f32x4*)(modp + idx), b = *(const f32x4*)(modp + 6144 + idx);
  f32x4 c = *(const f32x4*)(modp + 2 * 6144 + idx), d = *(const f32x4*)(modp + 3 * 6144 + idx);
  f32x4 s = (a + b) + (c + d);
  return (f32x4){rbf(s[0]), rbf(s[1]), rbf(s[2]), rbf(s[3])};
}

__global__ __launch_bounds__(256) void mod_kernel(const u16* __restrict__ c, const u16* __restrict__ wmod,
                                                  float* __restrict__ modp, const u16* __restrict__ pf) {
  __shared__ float sh[256 + 32 * 65];
  const int b = blockIdx.x, tid = threadIdx.x;
  if (b >= 128) { touch_block((const char*)pf + (size_t)(b - 128) * 32768); return; }
  const int cg = b >> 2, ks = b & 3;
  const int cl = tid & 7, rg = tid >> 3;
  u32x4 w[8];
#pragma unroll
  for (int i = 0; i < 8; ++i)
    w[i] = __builtin_nontemporal_load((const u32x4*)(wmod + (size_t)(ks * 256 + rg + i * 32) * 6144 + cg * 64 + cl * 8));
  {
    const float v = bf2f(c[ks * 256 + tid]);
    sh[tid] = rbf(v / (1.0f + __expf(-v)));
  }
  __syncthreads();
  float acc[8];
#pragma unroll
  for (int j = 0; j < 8; ++j) acc[j] = 0.f;
#pragma unroll
  for (int i = 0; i < 8; ++i) {
    const float s = sh[rg + i * 32];
#pragma unroll
    for (int j = 0; j < 4; ++j) {
      acc[2 * j] += s * lo_f(w[i][j]);
      acc[2 * j + 1] += s * hi_f(w[i][j]);
    }
  }
  float* red = sh + 256;
#pragma unroll
  for (int j = 0; j < 8; ++j) red[rg * 65 + cl * 8 + j] = acc[j];
  __syncthreads();
  if (tid < 64) {
    float s = 0.f;
#pragma unroll 8
    for (int r = 0; r < 32; ++r) s += red[r * 65 + tid];
    modp[ks * 6144 + cg * 64 + tid] = s;
  }
}

__global__ __launch_bounds__(256) void ln_kernel(const u16* __restrict__ x, const float* __restrict__ modp, u16* __restrict__ h,
                                                 const u16* __restrict__ pf) {
  __shared__ float sh[2048];
  const int tid = threadIdx.x;
  if (blockIdx.x >= 512) { touch_block((const char*)pf + (size_t)(blockIdx.x - 512) * 32768); return; }
  const int lane = tid & 63, wid = tid >> 6;
  const int row = blockIdx.x * 4 + wid;
  const u16* xr = x + (size_t)row * D_;
  const u32x4 v0 = *(const u32x4*)(xr + lane * 8);
  const u32x4 v1 = *(const u32x4*)(xr + 512 + lane * 8);
  {
    const f32x4 s0 = ld_mod4(modp, tid * 4);
    const f32x4 s1 = ld_mod4(modp, 1024 + tid * 4);
    *(f32x4*)(sh + tid * 4) = s0;
    *(f32x4*)(sh + 1024 + tid * 4) = (f32x4){rbf(1.0f + s1[0]), rbf(1.0f + s1[1]), rbf(1.0f + s1[2]), rbf(1.0f + s1[3])};
  }
  float f[16];
#pragma unroll
  for (int j = 0; j < 4; ++j) {
    f[2 * j] = lo_f(v0[j]); f[2 * j + 1] = hi_f(v0[j]);
    f[8 + 2 * j] = lo_f(v1[j]); f[8 + 2 * j + 1] = hi_f(v1[j]);
  }
  float s = 0.f;
#pragma unroll
  for (int j = 0; j < 16; ++j) s += f[j];
#pragma unroll
  for (int o = 32; o >= 1; o >>= 1) s += __shfl_xor(s, o);
  const float mean = s * (1.0f / D_);
  float q = 0.f;
#pragma unroll
  for (int j = 0; j < 16; ++j) { float d = f[j] - mean; q += d * d; }
#pragma unroll
  for (int o = 32; o >= 1; o >>= 1) q += __shfl_xor(q, o);
  const float mb = rbf(mean);
  const float vb = rbf(q * (1.0f / D_));
  const float sd = rbf(sqrtf(rbf(vb + 1e-5f)));
  const float inv = 1.0f / sd;
  __syncthreads();
  float o[16];
#pragma unroll
  for (int half = 0; half < 2; ++half) {
    const int c0 = half * 512 + lane * 8;
    const f32x4 sa0 = *(const f32x4*)(sh + c0), sa1 = *(const f32x4*)(sh + c0 + 4);
    const f32x4 ca0 = *(const f32x4*)(sh + 1024 + c0), ca1 = *(const f32x4*)(sh + 1024 + c0 + 4);
#pragma unroll
    for (int j = 0; j < 8; ++j) {
      const float shf = j < 4 ? sa0[j & 3] : sa1[j & 3];
      const float sc = j < 4 ? ca0[j & 3] : ca1[j & 3];
      float t = rbf(f[half * 8 + j] - mb);
      t = rbf(t * inv);
      t = rbf(t * sc);
      o[half * 8 + j] = t + shf;
    }
  }
  u16* hr = h + (size_t)row * D_;
  u32x4 w0, w1;
#pragma unroll
  for (int j = 0; j < 4; ++j) {
    w0[j] = pack2(o[2 * j], o[2 * j + 1]);
    w1[j] = pack2(o[8 + 2 * j], o[8 + 2 * j + 1]);
  }
  ST16(hr + lane * 8, w0);
  ST16(hr + 512 + lane * 8, w1);
}

struct GemmP {
  const u16* A; const u16* B0; const u16* B1; const u16* res; const float* gate; u16* out;
};

template <int MODE> struct GCfg;
template <> struct GCfg<0> { static constexpr int BM = 128, BN = 192, WM = 2, WN = 2, NS = 3, K = 1024, LDA = 1024, LDB = 3072, NTM = 16, NTN = 16, RM = 8, RN = 4, LW = 1; };
template <> struct GCfg<1> { static constexpr int BM = 128, BN = 64, WM = 2, WN = 2, NS = 3, K = 1024, LDA = 1024, LDB = 1024, NTM = 16, NTN = 16, RM = 4, RN = 8, LW = 1; };
template <> struct GCfg<2> { static constexpr int BM = 256, BN = 256, WM = 4, WN = 2, NS = 2, K = 1024, LDA = 1024, LDB = 3584, NTM = 8, NTN = 28, RM = 4, RN = 7, LW = 0; };
template <> struct GCfg<3> { static constexpr int BM = 128, BN = 64, WM = 2, WN = 2, NS = 3, K = 3584, LDA = 3584, LDB = 1024, NTM = 16, NTN = 16, RM = 4, RN = 8, LW = 1; };

template <int MODE>
__global__ __launch_bounds__(GCfg<MODE>::WM* GCfg<MODE>::WN * 64 * (GCfg<MODE>::LW ? 2 : 1), 2) void gemm_kernel(GemmP p) {
  using C = GCfg<MODE>;
  constexpr int BM = C::BM, BN = C::BN, WM = C::WM, WN = C::WN, NS = C::NS, NW = WM * WN;
  constexpr int TM = BM / WM, TN = BN / WN, MI = TM / 16, NI = TN / 16;
  constexpr int A_BYTES = BM * 128, B_BYTES = BN * 128, ST_BYTES = A_BYTES + B_BYTES;
  constexpr int GA = BM / 8 / NW, GB = BN / 8 / NW, G = GA + GB;
  constexpr int NT = C::K / 64;
  static_assert(BM % (8 * NW) == 0 && BN % (8 * NW) == 0, "stage split");
  static_assert(NT >= NS + 1, "K too small");
  extern __shared__ __attribute__((aligned(16))) char smem[];

  const int tid = threadIdx.x, lane = tid & 63;
  constexpr bool LW = C::LW != 0;
  const int wid_all = __builtin_amdgcn_readfirstlane(tid >> 6);
  const int wid = wid_all % NW;
  const int wm = wid / WN, wn = wid % WN;
  int tm, tn;
  {
    const int x = blockIdx.x & 7, idx = blockIdx.x >> 3;
    constexpr int NRM = C::NTM / C::RM;
    const int rm = x % NRM, rn = x / NRM;
    tm = rm * C::RM + idx % C::RM;
    tn = rn * C::RN + idx / C::RM;
  }
  const int m0 = tm * BM;

  const int par = wid & 1;
  const int a_ch = (lane & 7) ^ ((4 * par + (lane >> 4)) & 7);
  const unsigned a_voff = (MODE == 1) ? (unsigned)((((a_ch >> 2) * S_ + (lane >> 3)) * 32 + (a_ch & 3) * 8) * 2)
                                      : (unsigned)(((lane >> 3) * C::LDA + 8 * a_ch) * 2);
  const unsigned b_voff = (unsigned)(((lane >> 3) * C::LDB + 8 * ((lane & 7) ^ ((((lane >> 4) & 1) | (par << 1)) << 1))) * 2);
  const char* Abase = (MODE == 1) ? (const char*)(p.A + (size_t)m0 * 32) : (const char*)(p.A + (size_t)m0 * C::LDA);
  const unsigned lds0 = (unsigned)(size_t)(LAS char*)smem;

  auto issue1 = [&](auto gi, int t, int bufoff) {
    constexpr int g = decltype(gi)::value;
    if constexpr (g < GA) {
      const int i = wid + NW * g;
      const char* gp = (MODE == 1) ? Abase + ((size_t)(8 * i) * 32 + (size_t)t * 2 * S_ * 32) * 2
                                   : Abase + ((size_t)(8 * i) * C::LDA + (size_t)t * 64) * 2;
      glds16(gp, a_voff, lds0 + bufoff + i * 1024);
    } else {
      const int i = wid + NW * (g - GA);
      const int pn = i >> 3, kr = 8 * (i & 7);
      const u16* bp;
      if constexpr (MODE == 2) bp = (pn < 2 ? p.B0 : p.B1) + tn * 128 + (pn & 1) * 64;
      else bp = p.B0 + tn * BN + pn * 64;
      const char* gp = (const char*)(bp + ((size_t)t * 64 + kr) * C::LDB);
      glds16(gp, b_voff, lds0 + bufoff + A_BYTES + i * 1024);
    }
  };

  if constexpr (LW) {
    if (wid_all >= NW) {
      sfor<0, NS>([&](auto sc_) { sfor<0, G>([&](auto gc) { issue1(gc, decltype(sc_)::value, decltype(sc_)::value * ST_BYTES); }); });
      WAIT_V((NS - 1) * G);
      BAR();
      int lb = 0;
      for (int t = 0; t < NT - NS; ++t) {
        WAIT_V((NS - 2) * G);
        BAR();
        sfor<0, G>([&](auto gc) { issue1(gc, t + NS, lb); });
        lb = (lb + ST_BYTES == NS * ST_BYTES) ? 0 : lb + ST_BYTES;
      }
      sfor<0, NS - 1>([&](auto rc) { WAIT_V((NS - 2 - decltype(rc)::value) * G); BAR(); });
      return;
    }
  }

  const int fr = lane & 15, fq = lane >> 4;
  u32x4 rv[4];
  f32x4 gv[NI];
  if constexpr (MODE == 1 || MODE == 3) {
#pragma unroll
    for (int i = 0; i < 4; ++i) rv[i] = *(const u32x4*)(p.res + (size_t)(m0 + i * 32 + (tid >> 3)) * D_ + tn * BN + (tid & 7) * 8);
#pragma unroll
    for (int ni = 0; ni < NI; ++ni) { gv[ni] = ld_mod4(p.gate, tn * BN + wn * TN + ni * 16 + 4 * fq); asm volatile("" : "+v"(gv[ni])); }
  }
  const int a_rd0 = (wm * TM + fr) * 128 + ((fq ^ (fr >> 1)) << 4);
  const int a_rd1 = a_rd0 ^ 64;
  int b_rd[(MODE == 2) ? 4 : NI];
  {
    const int q = (lane & 15) >> 2, pp = lane & 3;
    const int gl = ((q >> 1) & 1) | ((fq & 1) << 1);
#pragma unroll
    for (int ni = 0; ni < ((MODE == 2) ? 4 : NI); ++ni) {
      int pn, un;
      if constexpr (MODE == 2) { pn = wn; un = ni & 3; }
      else { const int j = wn * NI + ni; pn = j >> 2; un = j & 3; }
      b_rd[ni] = A_BYTES + pn * 8192 + (8 * fq + q) * 128 + ((un ^ gl) << 5) + pp * 8;
    }
  }

  f32x4 acc[MI][NI];
#pragma unroll
  for (int mi = 0; mi < MI; ++mi)
#pragma unroll
    for (int ni = 0; ni < NI; ++ni) acc[mi][ni] = (f32x4){0.f, 0.f, 0.f, 0.f};

  constexpr int NSP = (MODE == 2) ? 2 : 1;
  constexpr int NH = NI / NSP, NM = MI * NH, NPH = 2 * NSP;
  constexpr int GW = (NS >= 3) ? NPH : NPH / 2;
  constexpr int GPP = (G + GW - 1) / GW;
  bf16x8 a0[MI], a1[MI], b0[NH], b1[NH];
  s16x4 blo[NH];

  auto rd_b = [&](auto ri, bf16x8* BF, const char* sb, int ks, int hf) {
    constexpr int r = decltype(ri)::value, ni = r >> 1;
    const int bo = (MODE == 2) ? b_rd[ni] + hf * 16384 : b_rd[ni];
    if constexpr ((r & 1) == 0) blo[ni] = tr_read(sb + bo + ks * 4096);
    else BF[ni] = __builtin_shufflevector(blo[ni], tr_read(sb + bo + ks * 4096 + 512), 0, 1, 2, 3, 4, 5, 6, 7);
  };
  auto rd_a = [&](auto ri, bf16x8* AF, const char* sb, int ks) {
    constexpr int mi = decltype(ri)::value;
    AF[mi] = lds_read16(sb + (ks ? a_rd1 : a_rd0) + mi * 2048);
  };
  auto phase = [&](auto hfc, auto rdm, auto g0c, auto g1c, bf16x8* AF, bf16x8* BF, bf16x8* AN_, bf16x8* BN_,
                   const char* rsb, int rks, int rhf, int gt, int gbuf) {
    constexpr int hf = decltype(hfc)::value, RDM = decltype(rdm)::value;
    constexpr int g0 = decltype(g0c)::value, g1 = decltype(g1c)::value, NG = g1 - g0;
    constexpr int NR = RDM == 0 ? 0 : (RDM == 1 ? 2 * NH : 2 * NH + MI);
    sfor<0, NM>([&](auto ic) {
      constexpr int i = decltype(ic)::value, mi = i / NH, ni = i % NH;
      MFMA16(acc[mi][hf * NH + ni], BF[ni], AF[mi]);
      sfor<0, NR>([&](auto rc) {
        constexpr int r = decltype(rc)::value;
        if constexpr ((r * (NM - NM / 4)) / (NR > 0 ? NR : 1) == i) {
          if constexpr (r < 2 * NH) rd_b(rc, BN_, rsb, rks, rhf);
          else rd_a(IC<r - 2 * NH>{}, AN_, rsb, rks);
        }
      });
      sfor<0, NG>([&](auto gc) {
        constexpr int g = decltype(gc)::value;
        if constexpr ((g * NM) / (NG > 0 ? NG : 1) == i) issue1(IC<g0 + g>{}, gt, gbuf);
      });
      SBAR();
    });
  };
  constexpr auto gmin = [](int a, int b) { return a < b ? a : b; };
  int cur = 0, prv = 0;
  auto ktile = [&](auto vmc, auto restc, auto firstc, int t) {
    constexpr int VM = decltype(vmc)::value;
    constexpr bool REST = decltype(restc)::value && !LW, FIRST = decltype(firstc)::value && !LW;
    const int nxt = (cur + ST_BYTES == NS * ST_BYTES) ? 0 : cur + ST_BYTES;
    const char* sc = smem + cur;
    const char* sn = smem + nxt;
    constexpr int R1 = REST ? gmin(G, GPP) : 0, R2 = REST ? gmin(G, 2 * GPP) : 0, R3 = REST ? gmin(G, 3 * GPP) : 0, R4 = REST ? gmin(G, 4 * GPP) : 0;
    constexpr int F1 = FIRST ? gmin(G, GPP) : 0;
    if constexpr (NSP == 1) {
      phase(IC<0>{}, IC<2>{}, IC<R1>{}, IC<R2>{}, a0, b0, a1, b1, sc, 1, 0, t - 1 + NS, prv);
      if constexpr (VM >= 0) {
        if constexpr (!LW) WAIT_V(VM);
        WAIT_L0(); BAR();
        phase(IC<0>{}, IC<2>{}, IC<0>{}, IC<F1>{}, a1, b1, a0, b0, sn, 0, 0, t + NS, cur);
      } else {
        phase(IC<0>{}, IC<0>{}, IC<0>{}, IC<0>{}, a1, b1, a0, b0, sn, 0, 0, 0, 0);
      }
    } else {
      phase(IC<0>{}, IC<1>{}, IC<R1>{}, IC<R2>{}, a0, b0, a1, b1, sc, 0, 1, t - 1 + NS, prv);
      phase(IC<1>{}, IC<2>{}, IC<R2>{}, IC<R3>{}, a0, b1, a1, b0, sc, 1, 0, t - 1 + NS, prv);
      phase(IC<0>{}, IC<1>{}, IC<R3>{}, IC<R4>{}, a1, b0, a1, b1, sc, 1, 1, t - 1 + NS, prv);
      if constexpr (VM >= 0) {
        WAIT_V(VM); WAIT_L0(); BAR();
        phase(IC<1>{}, IC<2>{}, IC<0>{}, IC<F1>{}, a1, b1, a0, b0, sn, 0, 0, t + NS, cur);
      } else {
        phase(IC<1>{}, IC<0>{}, IC<0>{}, IC<0>{}, a1, b1, a0, b0, sn, 0, 0, 0, 0);
      }
    }
    prv = cur; cur = nxt;
  };

  if constexpr (!LW) {
    sfor<0, NS>([&](auto sc_) { sfor<0, G>([&](auto gc) { issue1(gc, decltype(sc_)::value, decltype(sc_)::value * ST_BYTES); }); });
    WAIT_V((NS - 1) * G);
  }
  BAR();
  sfor<0, 2 * NH>([&](auto rc) { rd_b(rc, b0, smem, 0, 0); });
  sfor<0, MI>([&](auto rc) { rd_a(rc, a0, smem, 0); });
  using TT = IC<1>; using FF = IC<0>;
  ktile(IC<(NS - 2) * G>{}, FF{}, TT{}, 0);
  for (int t = 1; t < NT - NS; ++t) ktile(IC<(NS - 2) * G>{}, TT{}, TT{}, t);
  ktile(IC<(NS - 2) * G>{}, TT{}, FF{}, NT - NS);
  sfor<0, NS - 2>([&](auto rc) { ktile(IC<(NS - 3 - decltype(rc)::value) * G>{}, FF{}, FF{}, 0); });
  ktile(IC<-1>{}, FF{}, FF{}, 0);

  BAR();
  if constexpr (MODE == 0) {
    const float qs = 0.17677669529663687f * 1.4426950408889634f;
    char* st = smem + wid * (64 * 208);
#pragma unroll
    for (int ni = 0; ni < NI; ++ni) {
      const int n = tn * BN + wn * TN + ni * 16;
      const float sc = n < 1024 ? qs : 1.0f;
#pragma unroll
      for (int mi = 0; mi < MI; ++mi) {
        u32x2 w;
        w[0] = pack2(acc[mi][ni][0] * sc, acc[mi][ni][1] * sc);
        w[1] = pack2(acc[mi][ni][2] * sc, acc[mi][ni][3] * sc);
        *(LAS u32x2*)(st + (mi * 16 + fr) * 208 + ni * 32 + fq * 8) = w;
      }
    }
#pragma unroll
    for (int j = 0; j < 3; ++j) {
      const int hh = (tn * BN + wn * TN + j * 32) >> 5;
      u16* ob = p.out + ((size_t)hh * S_ + m0 + wm * TM) * 32 + lane * 8;
#pragma unroll
      for (int i = 0; i < 4; ++i) {
        const u32x4 v = *(const LAS u32x4*)(st + (i * 16 + (lane >> 2)) * 208 + j * 64 + (lane & 3) * 16);
        ST16(ob + i * 512, v);
      }
    }
  } else if constexpr (MODE == 1 || MODE == 3) {
#pragma unroll
    for (int ni = 0; ni < NI; ++ni)
#pragma unroll
      for (int mi = 0; mi < MI; ++mi) {
        u32x2 w;
        w[0] = pack2(gv[ni][0] * rbf(acc[mi][ni][0]), gv[ni][1] * rbf(acc[mi][ni][1]));
        w[1] = pack2(gv[ni][2] * rbf(acc[mi][ni][2]), gv[ni][3] * rbf(acc[mi][ni][3]));
        *(LAS u32x2*)(smem + (wm * TM + mi * 16 + fr) * 144 + wn * 64 + ni * 32 + fq * 8) = w;
      }
    WAIT_L0();
    BAR();
#pragma unroll
    for (int i = 0; i < 4; ++i) {
      const u32x4 v = *(const LAS u32x4*)(smem + (i * 32 + (tid >> 3)) * 144 + (tid & 7) * 16);
      u32x4 w;
#pragma unroll
      for (int j = 0; j < 4; ++j) w[j] = pack2(lo_f(rv[i][j]) + lo_f(v[j]), hi_f(rv[i][j]) + hi_f(v[j]));
      ST16(p.out + (size_t)(m0 + i * 32 + (tid >> 3)) * D_ + tn * BN + (tid & 7) * 8, w);
    }
  } else {
    char* st = smem + wid * (64 * 144);
#pragma unroll
    for (int ni = 0; ni < 4; ++ni)
#pragma unroll
      for (int mi = 0; mi < MI; ++mi) {
        float o[4];
#pragma unroll
        for (int r = 0; r < 4; ++r) o[r] = rbf(acc[mi][ni][r]) * rbf(acc[mi][ni + 4][r]);
        u32x2 w;
        w[0] = pack2(o[0], o[1]);
        w[1] = pack2(o[2], o[3]);
        *(LAS u32x2*)(st + (mi * 16 + fr) * 144 + ni * 32 + fq * 8) = w;
      }
    u16* ob = p.out + (size_t)(m0 + wm * TM + (lane >> 3)) * F_ + tn * 128 + wn * 64 + (lane & 7) * 8;
#pragma unroll
    for (int i = 0; i < 8; ++i) {
      const u32x4 v = *(const LAS u32x4*)(st + (i * 8 + (lane >> 3)) * 144 + (lane & 7) * 16);
      ST16(ob + (size_t)i * 8 * F_, v);
    }
  }
}

#define MX3(a, b, c) __builtin_fmaxf(__builtin_fmaxf((a), (b)), (c))
__device__ __forceinline__ float tile_rowmax(const f32x16& s0, const f32x16& s1) {
  float a = MX3(s0[0], s0[1], s1[0]), b = MX3(s0[2], s0[3], s1[1]);
  a = MX3(a, s1[2], s1[3]);
#pragma unroll
  for (int r = 4; r < 16; r += 4) {
    a = MX3(a, s0[r], s0[r + 1]); b = MX3(b, s0[r + 2], s0[r + 3]);
    a = MX3(a, s1[r], s1[r + 1]); b = MX3(b, s1[r + 2], s1[r + 3]);
  }
  float mx = __builtin_fmaxf(a, b);
  auto rr = __builtin_amdgcn_permlane32_swap(__float_as_uint(mx), __float_as_uint(mx), false, false);
  return __builtin_fmaxf(__uint_as_float(rr[0]), __uint_as_float(rr[1]));
}

constexpr int A2_KT = 256, A2_NS = 3, A2_ST = 32768, A2_MOD = A2_NS * A2_ST + 1024, A2_RED = A2_MOD + 32768,
              A2_FLG = A2_RED + 4096, A2_LDS = A2_FLG + 64;
template <bool SAFE>
__device__ __forceinline__ void attn_pass(char* smem, const u16* Kp, const u16* Vp, bf16x8 qf0, bf16x8 qf1, int wid, int lane,
                                          f32x16& oacc, float& m, float& l,
                                          const u16* pw0, const u16* pw1, const u16* pw2, const u16* pw3, const u16* wm, unsigned cc) {
  const int wk = wid >> 3, r32 = lane & 31, hi = lane >> 5;
  const unsigned k_voff = (unsigned)(((lane >> 2) * 32 + 8 * ((lane & 3) ^ ((lane >> 4) & 3))) * 2);
  const unsigned v_voff = (unsigned)(lane * 16);
  const unsigned lds0 = (unsigned)(size_t)(LAS char*)smem;
  auto issue = [&](int t, int bufoff) {
    const char* gk = (const char*)(Kp + ((size_t)t * A2_KT + wid * 16) * 32);
    const char* gv = (const char*)(Vp + ((size_t)t * A2_KT + wid * 16) * 32);
    glds16(gk, k_voff, lds0 + bufoff + wid * 1024);
    glds16(gv, v_voff, lds0 + bufoff + 16384 + wid * 1024);
  };
  issue(0, 0);
  issue(1, A2_ST);
  const int k_rd0 = wk * 8192 + r32 * 64 + ((hi ^ ((r32 >> 2) & 3)) << 4);
  const int k_rd1 = k_rd0 ^ 32;
  const int v_rd = 16384 + wk * 8192 + (4 * hi + ((lane & 15) >> 2)) * 64 + ((lane >> 4) & 1) * 32 + (lane & 3) * 8;
  f32x16 negm = {};
  f32x2 lsa = {0.f, 0.f}, lsb = {0.f, 0.f};
  oacc = (f32x16){};
  m = 0.f; l = 0.f;
  constexpr int NT = S_ / A2_KT;
  int cur = 0;
  for (int t = 0; t < NT; ++t) {
    if (t == 0) WAIT_V(2); else if (!SAFE && t == 4) WAIT_V(5); else if (t + 1 < NT) WAIT_V(3); else WAIT_V(0);
    BAR();
    if (!SAFE && t == 6) {
      const int rg = wid * 8 + (lane >> 3), cl = lane & 7;
      const float c0 = lo_f(cc), c1 = hi_f(cc);
      const float s0 = rbf(c0 / (1.0f + __expf(-c0))), s1 = rbf(c1 / (1.0f + __expf(-c1)));
      const u32x4 w0 = *(const LAS u32x4*)(smem + A2_MOD + (2 * rg) * 128 + cl * 16);
      const u32x4 w1 = *(const LAS u32x4*)(smem + A2_MOD + (2 * rg + 1) * 128 + cl * 16);
      float a[8];
#pragma unroll
      for (int j = 0; j < 4; ++j) {
        a[2 * j] = s0 * lo_f(w0[j]) + s1 * lo_f(w1[j]);
        a[2 * j + 1] = s0 * hi_f(w0[j]) + s1 * hi_f(w1[j]);
      }
#pragma unroll
      for (int j = 0; j < 8; ++j) {
        a[j] += __shfl_xor(a[j], 8); a[j] += __shfl_xor(a[j], 16); a[j] += __shfl_xor(a[j], 32);
      }
      if (lane < 8) {
        float* mred = (float*)(smem + A2_RED);
#pragma unroll
        for (int j = 0; j < 8; ++j) mred[wid * 64 + lane * 8 + j] = a[j];
      }
    }
    if (!SAFE && t == 3) {
      const unsigned m_voff = (unsigned)(((lane >> 3) * 6144 + (lane & 7) * 8) * 2);
      glds16(wm + (size_t)(wid * 16) * 6144, m_voff, lds0 + A2_MOD + wid * 2048);
      glds16(wm + (size_t)(wid * 16 + 8) * 6144, m_voff, lds0 + A2_MOD + wid * 2048 + 1024);
    }
    if (t + 2 < NT) {
      int ck = (blockIdx.x * 6 + t) * 16 + wid;
      if (ck >= 2048 + 3 * 7168) ck -= 16384;
      const u16* pb = pw0;
      if (ck >= 2048) { pb = pw1; ck -= 2048; if (ck >= 7168) { pb = pw2; ck -= 7168; if (ck >= 7168) { pb = pw3; ck -= 7168; } } }
      glds16((const char*)pb + (size_t)ck * 1024, v_voff, lds0 + A2_NS * A2_ST);
      int nb = cur + 2 * A2_ST; if (nb >= A2_NS * A2_ST) nb -= A2_NS * A2_ST;
      issue(t + 2, nb);
    }
#pragma unroll
    for (int hb = 0; hb < 2; ++hb) {
      const char* kb = smem + cur + hb * 4096;
      const bf16x8 k00 = lds_read16(kb + k_rd0), k01 = lds_read16(kb + k_rd1);
      const bf16x8 k10 = lds_read16(kb + 2048 + k_rd0), k11 = lds_read16(kb + 2048 + k_rd1);
      f32x16 s0, s1;
      if (SAFE || (t == 0 && hb == 0)) {
        f32x16 z_ = {};
        s0 = MFMA32(k00, qf0, z_); s1 = MFMA32(k10, qf0, z_);
        s0 = MFMA32(k01, qf1, s0); s1 = MFMA32(k11, qf1, s1);
        const float tm = tile_rowmax(s0, s1);
        const bool first = (t == 0 && hb == 0);
        const float mn = first ? tm : __builtin_fmaxf(m, tm);
        if (SAFE) {
          const float f = first ? 0.f : __builtin_amdgcn_exp2f(m - mn);
          lsa *= f; lsb *= f;
          oacc *= f;
        }
        m = mn;
#pragma unroll
        for (int r = 0; r < 16; ++r) { negm[r] = -mn; s0[r] -= mn; s1[r] -= mn; }
      } else {
        s0 = MFMA32(k00, qf0, negm); s1 = MFMA32(k10, qf0, negm);
        s0 = MFMA32(k01, qf1, s0); s1 = MFMA32(k11, qf1, s1);
      }
#pragma unroll
      for (int r = 0; r < 16; r += 2) {
        s0[r] = __builtin_amdgcn_exp2f(s0[r]); s0[r + 1] = __builtin_amdgcn_exp2f(s0[r + 1]);
        s1[r] = __builtin_amdgcn_exp2f(s1[r]); s1[r + 1] = __builtin_amdgcn_exp2f(s1[r + 1]);
        lsa += (f32x2){s0[r], s0[r + 1]};
        lsb += (f32x2){s1[r], s1[r + 1]};
      }
      u32x4 p0, p1, p2, p3;
#pragma unroll
      for (int j = 0; j < 4; ++j) {
        p0[j] = pack2(s0[2 * j], s0[2 * j + 1]);
        p1[j] = pack2(s0[8 + 2 * j], s0[8 + 2 * j + 1]);
        p2[j] = pack2(s1[2 * j], s1[2 * j + 1]);
        p3[j] = pack2(s1[8 + 2 * j], s1[8 + 2 * j + 1]);
      }
      const char* vb = smem + cur + hb * 4096 + v_rd;
#define VFRAG(s) __builtin_shufflevector(tr_read(vb + (s) * 1024), tr_read(vb + (s) * 1024 + 512), 0, 1, 2, 3, 4, 5, 6, 7)
      oacc = MFMA32(VFRAG(0), __builtin_bit_cast(bf16x8, p0), oacc);
      oacc = MFMA32(VFRAG(1), __builtin_bit_cast(bf16x8, p1), oacc);
      oacc = MFMA32(VFRAG(2), __builtin_bit_cast(bf16x8, p2), oacc);
      oacc = MFMA32(VFRAG(3), __builtin_bit_cast(bf16x8, p3), oacc);
#undef VFRAG
    }
    WAIT_L0();
    cur += A2_ST; if (cur >= A2_NS * A2_ST) cur = 0;
  }
  lsa += lsb;
  l = lsa[0] + lsa[1];
  {
    auto rr = __builtin_amdgcn_permlane32_swap(__float_as_uint(l), __float_as_uint(l), false, false);
    l = __uint_as_float(rr[0]) + __uint_as_float(rr[1]);
  }
  WAIT_V(0);
}

__global__ __launch_bounds__(1024, 4) void attn_kernel(const u16* __restrict__ qkv, u16* __restrict__ o,
                                                       const u16* pw0, const u16* pw1, const u16* pw2, const u16* pw3,
                                                       const u16* __restrict__ cvec, const u16* wmod, float* __restrict__ modp) {
  extern __shared__ __attribute__((aligned(16))) char smem[];
  const int tid = threadIdx.x, lane = tid & 63;
  const int wid = __builtin_amdgcn_readfirstlane(tid >> 6);
  const int wq = wid & 7, wk = wid >> 3;
  const int x = blockIdx.x & 7, idx = blockIdx.x >> 3;
  const int hd = x * 4 + (idx >> 3), qb = idx & 7;
  const int r32 = lane & 31, hi = lane >> 5;
  const u16* Q = qkv + ((size_t)(0 * 32 + hd) * S_) * 32;
  const u16* Kp = qkv + ((size_t)(1 * 32 + hd) * S_) * 32;
  const u16* Vp = qkv + ((size_t)(2 * 32 + hd) * S_) * 32;
  const int q0 = qb * 256 + wq * 32;
  const bf16x8 qf0 = *(const bf16x8*)(Q + (size_t)(q0 + r32) * 32 + hi * 8);
  const bf16x8 qf1 = *(const bf16x8*)(Q + (size_t)(q0 + r32) * 32 + 16 + hi * 8);

  f32x16 oacc;
  float m, l;
  const int mcg = 32 + (blockIdx.x >> 2), mks = blockIdx.x & 3;
  const u16* wm = wmod + (size_t)(mks * 256) * 6144 + mcg * 64;
  unsigned cc = *(const unsigned*)(cvec + mks * 256 + 2 * (tid >> 3));
  asm volatile("" : "+v"(cc));
  attn_pass<false>(smem, Kp, Vp, qf0, qf1, wid, lane, oacc, m, l, pw0, pw1, pw2, pw3, wm, cc);
  float* xb = (float*)smem + wq * (20 * 64);
  const float* mred = (const float*)(smem + A2_RED);
  volatile LAS int* badf = (volatile LAS int*)(smem + A2_FLG);
  {
    float chk = l;
#pragma unroll
    for (int r = 0; r < 16; ++r) chk += __builtin_fabsf(oacc[r]);
    const int bad = __any(!(chk < 1.0e37f));
    if (lane == 0) badf[wid] = bad;
    __syncthreads();
    const int anybad = __any(badf[lane & 15] != 0);
    if (anybad) {
      __syncthreads();
      attn_pass<true>(smem, Kp, Vp, qf0, qf1, wid, lane, oacc, m, l, pw0, pw1, pw2, pw3, wm, cc);
      __syncthreads();
    }
  }
  xb[wk * 64 + lane] = m; xb[(2 + wk) * 64 + lane] = l;
#pragma unroll
  for (int r = 0; r < 8; ++r) xb[(4 + 8 * wk + r) * 64 + lane] = wk ? oacc[r] : oacc[8 + r];
  __syncthreads();
  {
    const float m2 = xb[(wk ^ 1) * 64 + lane], l2 = xb[(2 + (wk ^ 1)) * 64 + lane];
    const float mn = __builtin_fmaxf(m, m2);
    const float f1 = __builtin_amdgcn_exp2f((m - mn) * 0.5f), f2 = __builtin_amdgcn_exp2f((m2 - mn) * 0.5f);
    const float inv = 1.0f / ((l * f1) * f1 + (l2 * f2) * f2);
    u16* op = o + ((size_t)hd * S_ + q0 + r32) * 32 + 4 * hi + 16 * wk;
#pragma unroll
    for (int g = 0; g < 2; ++g) {
      float v[4];
#pragma unroll
      for (int e = 0; e < 4; ++e) {
        const float own = wk ? oacc[8 + 4 * g + e] : oacc[4 * g + e];
        v[e] = ((own * f1) * f1 + (xb[(4 + 8 * (wk ^ 1) + 4 * g + e) * 64 + lane] * f2) * f2) * inv;
      }
      u32x2 w;
      w[0] = pack2(v[0], v[1]);
      w[1] = pack2(v[2], v[3]);
      *(u32x2*)(op + 8 * g) = w;
    }
  }
  if (wid == 15) {
    float sacc = 0.f;
#pragma unroll
    for (int w = 0; w < 16; ++w) sacc += mred[w * 64 + lane];
    modp[mks * 6144 + mcg * 64 + lane] = sacc;
  }
}

template <int MODE> static void launch_gemm(const GemmP& p, hipStream_t stream) {
  using C = GCfg<MODE>;
  constexpr int lds = C::NS * (C::BM + C::BN) * 128;
  (void)hipFuncSetAttribute((const void*)gemm_kernel<MODE>, hipFuncAttributeMaxDynamicSharedMemorySize, lds);
  gemm_kernel<MODE><<<C::NTM * C::NTN, C::WM * C::WN * 64 * (C::LW ? 2 : 1), lds, stream>>>(p);
}

extern "C" void kernel_launch(void* const* d_in, const int* in_sizes, int n_in, void* d_out, int out_size,
                              void* d_ws, size_t ws_size, hipStream_t stream) {
  const u16* x = (const u16*)d_in[0];
  const u16* wqkv = (const u16*)d_in[1];
  const u16* wo = (const u16*)d_in[2];
  const u16* wg = (const u16*)d_in[4];
  const u16* wu = (const u16*)d_in[5];
  const u16* wd = (const u16*)d_in[6];
  const u16* c = (const u16*)d_in[7];
  const u16* wmod = (const u16*)d_in[8];
  u16* out = (u16*)d_out;

  char* ws = (char*)d_ws;
  float* mod = (float*)ws;
  u16* h = (u16*)(ws + (1 << 20));
  u16* qkv = (u16*)(ws + (8 << 20));
  u16* ao = (u16*)(ws + (24 << 20));
  u16* x1 = (u16*)(ws + (32 << 20));
  u16* hh = (u16*)(ws + (40 << 20));

  mod_kernel<<<256, 256, 0, stream>>>(c, wmod, mod, x);
  ln_kernel<<<512 + 192, 256, 0, stream>>>(x, mod, h, wqkv);
  { GemmP p{h, wqkv, nullptr, nullptr, nullptr, qkv}; launch_gemm<0>(p, stream); }
  (void)hipFuncSetAttribute((const void*)attn_kernel, hipFuncAttributeMaxDynamicSharedMemorySize, A2_LDS);
  attn_kernel<<<256, 1024, A2_LDS, stream>>>(qkv, ao, wo, wg, wu, wd, c, wmod, mod);
  { GemmP p{ao, wo, nullptr, x, mod + 2 * 1024, x1}; launch_gemm<1>(p, stream); }
  ln_kernel<<<512, 256, 0, stream>>>(x1, mod + 3 * 1024, h, wqkv);
  { GemmP p{h, wg, wu, nullptr, nullptr, hh}; launch_gemm<2>(p, stream); }
  { GemmP p{hh, wd, nullptr, x1, mod + 5 * 1024, out}; launch_gemm<3>(p, stream); }
}
```
